```python
import torch

def _softmax(x, axis=-1):
    return torch.softmax(x, dim=axis)
def _gelu(x):
    return torch.nn.functional.gelu(x, approximate="tanh")
def _silu(x):
    return torch.nn.functional.silu(x)
def _sigmoid(x):
    return torch.sigmoid(x)
def _layernorm(x, axis=-1, eps=1e-5):
    m = x.mean(dim=axis, keepdim=True)
    v = x.var(dim=axis, keepdim=True, unbiased=False)
    return (x - m) / torch.sqrt(v + eps)
def _scatter_add(tgt, idx, src, axis=0):
    return tgt.clone().index_add_(axis, idx, src)
def _segment_sum(src, seg, n):
    out = torch.zeros((n,) + src.shape[1:], dtype=src.dtype)
    return out.index_add_(0, seg, src)
def _pad(x, pw):
    flat = tuple(v for pair in reversed(pw) for v in pair)
    return torch.nn.functional.pad(x, flat)

kernel_name = 'standalone__mha_softmax__near_ridge'

def setup_inputs():

    _w = (lambda a: (lambda t: t.to(torch.bfloat16) if t.is_floating_point() else t)(torch.as_tensor(a)))
    B, H, Sq, Sk, D = 16, 16, 736, 512, 256
    return {
        "q": _w((lambda *s: torch.randn(*s, dtype=torch.bfloat16) * 0.8)(B, H, Sq, D)),
        "k": _w((lambda *s: torch.randn(*s, dtype=torch.bfloat16) * 0.8)(B, H, Sk, D)),
        "v": _w((lambda *s: torch.randn(*s, dtype=torch.bfloat16) * 0.05)(B, H, Sk, D)),
    }

def reference(q, k, v):
    scale = q.shape[-1] ** -0.5
    scores = torch.einsum("bhqd,bhkd->bhqk", q, k) * scale
    p = _softmax(scores, axis=-1)
    return torch.einsum("bhqk,bhkd->bhqd", p, v)
```

```cpp
#include <hip/hip_runtime.h>
#include <hip/hip_bf16.h>

typedef short    bf16x8 __attribute__((ext_vector_type(8)));
typedef short    s16x4  __attribute__((ext_vector_type(4)));
typedef float    f32x16 __attribute__((ext_vector_type(16)));
typedef float    f32x2  __attribute__((ext_vector_type(2)));
typedef __bf16   bf16x2 __attribute__((ext_vector_type(2)));
typedef unsigned u32x4  __attribute__((ext_vector_type(4)));
typedef unsigned u32x2  __attribute__((ext_vector_type(2)));
#define DI __device__ __forceinline__

constexpr int NBH = 256, SQ = 736, SK = 512, DH = 256;
constexpr int KSTR = 528;
constexpr int VSTR = 528;

DI unsigned cvtpk(float lo, float hi) {
  f32x2 v = {lo, hi}; bf16x2 b = __builtin_convertvector(v, bf16x2); return __builtin_bit_cast(unsigned, b);
}

DI void simple_block(const unsigned short* __restrict__ Qh, const unsigned short* __restrict__ Kh,
                     const unsigned short* __restrict__ Vh, unsigned short* __restrict__ Oh, int row0, char* lds) {
  const int tid = threadIdx.x, wid = tid >> 6, lane = tid & 63, r = lane & 31, h = lane >> 5;
  char* Kl = lds; char* Vl = lds + 32 * KSTR;
  int qrow = row0 + 32 * wid + r; const bool valid = qrow < SQ; if (!valid) qrow = SQ - 1;
  bf16x8 qf[16];
#pragma unroll
  for (int s = 0; s < 16; ++s) qf[s] = *(const bf16x8*)(Qh + (size_t)qrow * DH + 16 * s + 8 * h);
  f32x16 o[8];
#pragma unroll
  for (int i = 0; i < 8; ++i) o[i] = f32x16{};
  float m = -INFINITY, l = 0.f;
  const float C = 0.0625f * 1.4426950408889634f;
  for (int t = 0; t < SK / 32; ++t) {
    __syncthreads();
#pragma unroll
    for (int i = 0; i < 4; ++i) {
      const int c = tid + 256 * i, row = c >> 5, ch = c & 31;
      *(u32x4*)(Kl + row * KSTR + ch * 16) = *(const u32x4*)(Kh + (size_t)(32 * t + row) * DH + ch * 8);
      *(u32x4*)(Vl + row * VSTR + ch * 16) = *(const u32x4*)(Vh + (size_t)(32 * t + row) * DH + ch * 8);
    }
    __syncthreads();
    f32x16 s = f32x16{};
#pragma unroll
    for (int ks = 0; ks < 16; ++ks) {
      bf16x8 kf = *(const bf16x8*)(Kl + r * KSTR + ks * 32 + h * 16);
      s = __builtin_amdgcn_mfma_f32_32x32x16_bf16(kf, qf[ks], s, 0, 0, 0);
    }
    float mx = s[0];
#pragma unroll
    for (int i = 1; i < 16; ++i) mx = fmaxf(mx, s[i]);
    mx = fmaxf(mx, __shfl_xor(mx, 32));
    const float mn = fmaxf(m, mx);
    const float alpha = __builtin_amdgcn_exp2f((m - mn) * C);
    const float mc = -mn * C;
    float ps = 0.f; unsigned pk[8];
#pragma unroll
    for (int i = 0; i < 8; ++i) {
      const float p0 = __builtin_amdgcn_exp2f(fmaf(s[2 * i], C, mc)), p1 = __builtin_amdgcn_exp2f(fmaf(s[2 * i + 1], C, mc));
      ps += p0 + p1; pk[i] = cvtpk(p0, p1);
    }
    l = l * alpha + ps; m = mn;
#pragma unroll
    for (int db = 0; db < 8; ++db) {
#pragma unroll
      for (int i = 0; i < 16; ++i) o[db][i] *= alpha;
#pragma unroll
      for (int ks = 0; ks < 2; ++ks) {
        const int q4 = (lane & 15) >> 2, p4 = lane & 3, blk = (lane >> 4) & 1;
        const char* a0 = Vl + (16 * ks + 4 * h + q4) * VSTR + (32 * db + 16 * blk + 4 * p4) * 2;
        s16x4 lo = __builtin_amdgcn_ds_read_tr16_b64_v4i16((__attribute__((address_space(3))) s16x4*)a0);
        s16x4 hi = __builtin_amdgcn_ds_read_tr16_b64_v4i16((__attribute__((address_space(3))) s16x4*)(a0 + 8 * VSTR));
        bf16x8 vf = __builtin_shufflevector(lo, hi, 0, 1, 2, 3, 4, 5, 6, 7);
        u32x4 pw = {pk[4 * ks], pk[4 * ks + 1], pk[4 * ks + 2], pk[4 * ks + 3]};
        o[db] = __builtin_amdgcn_mfma_f32_32x32x16_bf16(vf, __builtin_bit_cast(bf16x8, pw), o[db], 0, 0, 0);
      }
    }
  }
  l += __shfl_xor(l, 32);
  const float il = 1.f / l;
  if (valid) {
#pragma unroll
    for (int db = 0; db < 8; ++db)
#pragma unroll
      for (int g = 0; g < 4; ++g) {
        u32x2 w = {cvtpk(o[db][4 * g] * il, o[db][4 * g + 1] * il), cvtpk(o[db][4 * g + 2] * il, o[db][4 * g + 3] * il)};
        *(u32x2*)(Oh + (size_t)qrow * DH + 32 * db + 8 * g + 4 * h) = w;
      }
  }
}

__global__ __launch_bounds__(256, 1) void attn_simple(const unsigned short* __restrict__ Q, const unsigned short* __restrict__ K,
                                                     const unsigned short* __restrict__ V, unsigned short* __restrict__ O) {
  __shared__ __attribute__((aligned(16))) char lds[32 * KSTR + 32 * VSTR];
  const int bid = blockIdx.x, xcd = bid & 7, idx = bid >> 3;
  const int head = xcd * 32 + idx / 6, part = idx % 6;
  simple_block(Q + (size_t)head * SQ * DH, K + (size_t)head * SK * DH, V + (size_t)head * SK * DH, O + (size_t)head * SQ * DH, part * 128, lds);
}

extern "C" void kernel_launch(void* const* d_in, const int* in_sizes, int n_in, void* d_out, int out_size, void* d_ws, size_t ws_size,
                              hipStream_t stream) {
  attn_simple<<<NBH * 6, 256, 0, stream>>>((const unsigned short*)d_in[0], (const unsigned short*)d_in[1], (const unsigned short*)d_in[2],
                                           (unsigned short*)d_out);
}
```

```cpp
#include <hip/hip_runtime.h>
#include <hip/hip_bf16.h>

typedef short    bf16x8 __attribute__((ext_vector_type(8)));
typedef short    s16x4  __attribute__((ext_vector_type(4)));
typedef float    f32x16 __attribute__((ext_vector_type(16)));
typedef float    f32x2  __attribute__((ext_vector_type(2)));
typedef __bf16   bf16x2 __attribute__((ext_vector_type(2)));
typedef unsigned u32x4  __attribute__((ext_vector_type(4)));
typedef unsigned u32x2  __attribute__((ext_vector_type(2)));
#define DI __device__ __forceinline__

constexpr int SQ = 736, SK = 512, DH = 256;
constexpr int LDS_BYTES = 157184;
constexpr int FSTR = 528;

DI unsigned cvtpk(float lo, float hi) {
  f32x2 v = {lo, hi}; bf16x2 b = __builtin_convertvector(v, bf16x2); return __builtin_bit_cast(unsigned, b);
}

DI void exact_rows(const unsigned short* __restrict__ Qh, const unsigned short* __restrict__ Kh, const unsigned short* __restrict__ Vh,
                   unsigned short* __restrict__ Oh, int row0, char* lds, int lane) {
  const int r = lane & 31, h = lane >> 5;
  char* Kl = lds; char* Vl = lds + 32 * FSTR;
  const int qrow = row0 + r;
  bf16x8 qf[16];
#pragma unroll
  for (int s = 0; s < 16; ++s) qf[s] = *(const bf16x8*)(Qh + (size_t)qrow * DH + 16 * s + 8 * h);
  f32x16 o[8];
#pragma unroll
  for (int i = 0; i < 8; ++i) o[i] = f32x16{};
  float m = -INFINITY, l = 0.f;
  const float C = 0.0625f * 1.4426950408889634f;
  for (int t = 0; t < SK / 32; ++t) {
    asm volatile("s_waitcnt lgkmcnt(0)" ::: "memory");
#pragma unroll 4
    for (int i = 0; i < 16; ++i) {
      const int c = lane + 64 * i, row = c >> 5, ch = c & 31;
      *(u32x4*)(Kl + row * FSTR + ch * 16) = *(const u32x4*)(Kh + (size_t)(32 * t + row) * DH + ch * 8);
      *(u32x4*)(Vl + row * FSTR + ch * 16) = *(const u32x4*)(Vh + (size_t)(32 * t + row) * DH + ch * 8);
    }
    asm volatile("s_waitcnt lgkmcnt(0)" ::: "memory");
    f32x16 s = f32x16{};
#pragma unroll
    for (int ks = 0; ks < 16; ++ks) {
      bf16x8 kf = *(const bf16x8*)(Kl + r * FSTR + ks * 32 + h * 16);
      s = __builtin_amdgcn_mfma_f32_32x32x16_bf16(kf, qf[ks], s, 0, 0, 0);
    }
    float mx = s[0];
#pragma unroll
    for (int i = 1; i < 16; ++i) mx = fmaxf(mx, s[i]);
    { auto pr = __builtin_amdgcn_permlane32_swap(__float_as_uint(mx), __float_as_uint(mx), false, false);
      mx = fmaxf(__uint_as_float(pr[0]), __uint_as_float(pr[1])); }
    const float mn = fmaxf(m, mx);
    const float alpha = __builtin_amdgcn_exp2f((m - mn) * C);
    float ps = 0.f; unsigned pk[8];
#pragma unroll
    for (int i = 0; i < 8; ++i) {
      const float p0 = __builtin_amdgcn_exp2f((s[2 * i] - mn) * C), p1 = __builtin_amdgcn_exp2f((s[2 * i + 1] - mn) * C);
      ps += p0 + p1; pk[i] = cvtpk(p0, p1);
    }
    l = l * alpha + ps; m = mn;
#pragma unroll
    for (int db = 0; db < 8; ++db) {
#pragma unroll
      for (int i = 0; i < 16; ++i) o[db][i] *= alpha;
#pragma unroll
      for (int ks = 0; ks < 2; ++ks) {
        const int q4 = (lane & 15) >> 2, p4 = lane & 3, blk = (lane >> 4) & 1;
        const char* a0 = Vl + (16 * ks + 4 * h + q4) * FSTR + (32 * db + 16 * blk + 4 * p4) * 2;
        s16x4 lo = __builtin_amdgcn_ds_read_tr16_b64_v4i16((__attribute__((address_space(3))) s16x4*)a0);
        s16x4 hi = __builtin_amdgcn_ds_read_tr16_b64_v4i16((__attribute__((address_space(3))) s16x4*)(a0 + 8 * FSTR));
        bf16x8 vf = __builtin_shufflevector(lo, hi, 0, 1, 2, 3, 4, 5, 6, 7);
        u32x4 pw = {pk[4 * ks], pk[4 * ks + 1], pk[4 * ks + 2], pk[4 * ks + 3]};
        o[db] = __builtin_amdgcn_mfma_f32_32x32x16_bf16(vf, __builtin_bit_cast(bf16x8, pw), o[db], 0, 0, 0);
      }
    }
  }
  { auto pr = __builtin_amdgcn_permlane32_swap(__float_as_uint(l), __float_as_uint(l), false, false);
    l = __uint_as_float(pr[0]) + __uint_as_float(pr[1]); }
  const float il = 1.f / l;
#pragma unroll
  for (int db = 0; db < 8; ++db)
#pragma unroll
    for (int g = 0; g < 4; ++g) {
      u32x2 w = {cvtpk(o[db][4 * g] * il, o[db][4 * g + 1] * il), cvtpk(o[db][4 * g + 2] * il, o[db][4 * g + 3] * il)};
      *(u32x2*)(Oh + (size_t)qrow * DH + 32 * db + 8 * g + 4 * h) = w;
    }
}

__global__ __launch_bounds__(256, 1) void attn_fwd(const unsigned short* __restrict__ Q, const unsigned short* __restrict__ K,
                                                  const unsigned short* __restrict__ V, unsigned short* __restrict__ O) {
  __shared__ __attribute__((aligned(64))) char lds[LDS_BYTES];
  const int wid = __builtin_amdgcn_readfirstlane(threadIdx.x >> 6);
  const int bid = blockIdx.x;
  const unsigned j = bid >> 3, hb = (bid & 7) * 32, j3 = j / 3, jr = j - 3 * j3;
  const unsigned rep = 1u | 1u << 10 | 1u << 20;
  const unsigned parts = jr == 0 ? (0u | 1u << 10 | 2u << 20) : jr == 1 ? (1u | 2u << 10 | 0u << 20) : (2u | 0u << 10 | 1u << 20);
  const unsigned tab = hb * rep + (j < 30 ? j3 * rep + (10u << 10 | 20u << 20) + (parts << 8)
                                          : (30u | 31u << 10 | j << 20) + (((j - 30) * (1u | 1u << 10) | 2u << 20) << 8));
  unsigned bad = 1u;
  if ((unsigned)(size_t)lds == 0u)
  asm volatile(
    "s_load_dwordx2 s[44:45], %1, 0x0\n"
    "s_load_dwordx2 s[36:37], %1, 0x8\n"
    "s_load_dwordx2 s[40:41], %1, 0x10\n"
    "s_load_dwordx2 s[48:49], %1, 0x18\n"
    "v_mbcnt_lo_u32_b32 v0, -1, 0\n"
    "v_mbcnt_hi_u32_b32 v0, -1, v0\n"
    "s_lshr_b32 s77, %3, 3\n"
    "s_getpc_b64 s[20:21]\n"
    "L_pf%=:\n"
    "s_lshl_b32 s22, s77, 2\n"
    "s_add_u32 s22, s22, %2\n"
    "s_lshl_b32 s22, s22, 9\n"
    "v_lshrrev_b32 v4, 4, v0\n"
    "v_lshl_add_u32 v4, v4, 7, s22\n"
    "v_min_u32 v4, L_end%=-L_pf%=-4, v4\n"
    "global_load_dword v248, v4, s[20:21]\n"
    "v_lshlrev_b32 v230, 4, v0\n"
    "s_mov_b32 s23, %4\n"
    "s_and_b32 s78, %4, 255\n"
    "s_bfe_u32 s93, %4, 0x20008\n"
    "s_lshl_b32 s79, %2, 15\n"
    "s_mul_i32 s80, s78, 376832\n"
    "s_lshl_b32 s81, s93, 17\n"
    "s_add_u32 s46, s80, 376832\n"
    "s_add_u32 s82, s80, s81\n"
    "s_add_u32 s82, s82, s79\n"
    "s_mov_b32 s47, 0x20000\n"
    "s_mul_i32 s59, %2, 17664\n"
    "s_add_u32 s59, s59, 86528\n"
    "s_waitcnt lgkmcnt(0)\n"
    "s_add_u32 s58, s82, 16384\n"
    "s_add_u32 m0, s59, 8448\n"
    "s_mov_b32 s77, 0\n"
    "buffer_load_dwordx4 v230, s[44:47], s58 offen nt lds\n"
    "s_add_u32 s58, s58, 1024\n"
    "s_add_u32 m0, s59, 9504\n"
    "s_mov_b32 s38, 0x4000000\n"
    "buffer_load_dwordx4 v230, s[44:47], s58 offen nt lds\n"
    "s_add_u32 s58, s58, 1024\n"
    "s_add_u32 m0, s59, 10560\n"
    "s_mov_b32 s39, 0x20000\n"
    "buffer_load_dwordx4 v230, s[44:47], s58 offen nt lds\n"
    "s_add_u32 s58, s58, 1024\n"
    "s_add_u32 m0, s59, 11616\n"
    "s_mov_b32 s42, 0x4000000\n"
    "buffer_load_dwordx4 v230, s[44:47], s58 offen nt lds\n"
    "s_add_u32 s58, s58, 1024\n"
    "s_add_u32 m0, s59, 12672\n"
    "s_mov_b32 s43, 0x20000\n"
    "buffer_load_dwordx4 v230, s[44:47], s58 offen nt lds\n"
    "s_add_u32 s58, s58, 1024\n"
    "s_add_u32 m0, s59, 13728\n"
    "s_mov_b32 s50, 0x0\n"
    "buffer_load_dwordx4 v230, s[44:47], s58 offen nt lds\n"
    "s_add_u32 s58, s58, 1024\n"
    "s_add_u32 m0, s59, 14784\n"
    "s_mov_b32 s51, 0x20000\n"
    "buffer_load_dwordx4 v230, s[44:47], s58 offen nt lds\n"
    "s_add_u32 s58, s58, 1024\n"
    "s_add_u32 m0, s59, 15840\n"
    "s_mul_i32 s62, %2, 1056\n"
    "buffer_load_dwordx4 v230, s[44:47], s58 offen nt lds\n"
    "s_add_u32 s58, s58, 1024\n"
    "s_lshl_b32 s63, %2, 10\n"
    "s_and_b32 s80, s23, 255\n"
    "s_bfe_u32 s81, s23, 0x20008\n"
    "s_lshr_b32 s23, s23, 10\n"
    "s_lshl_b32 s74, s80, 18\n"
    "s_mul_i32 s80, s80, 376832\n"
    "s_lshl_b32 s34, s81, 7\n"
    "s_and_b32 s34, s34, 384\n"
    "s_lshl_b32 s81, s81, 17\n"
    "s_add_u32 s81, s81, s79\n"
    "s_add_u32 s72, s80, s81\n"
    "s_add_u32 s73, s80, 376832\n"
    "s_cmp_gt_u32 s81, 344064\n"
    "s_cselect_b32 s75, 0xff800000, 0\n"
    "s_cmp_lt_u32 s77, 3\n"
    "s_cselect_b32 s73, s73, 0\n"
    "s_cselect_b32 s76, 0x4000000, 0\n"
    "s_add_u32 s77, s77, 1\n"
    "s_mov_b32 s54, 0\n"
    "s_mov_b32 s66, 0\n"
    "s_mov_b32 s67, 0\n"
    "s_mov_b32 s61, -1\n"
    "s_mov_b32 s60, s63\n"
    "s_add_u32 s62, s62, 2048\n"
    "s_add_u32 s63, s63, 2048\n"
    "s_add_u32 s52, s74, s60\n"
    "s_getreg_b32 s80, hwreg(HW_REG_XCC_ID, 0, 4)\n"
    "s_and_b32 s80, s80, 1\n"
    "s_cmp_lg_u32 s80, 0\n"
    "s_cbranch_scc1 L_xd%=\n"
    "s_sleep 60\n"
    "L_xd%=:\n"
    "s_add_u32 s96, s62, 0\n"
    "s_add_u32 m0, s96, 0\n"
    "s_nop 0\n"
    "buffer_load_dwordx4 v230, s[36:39], s52 offen lds\n"
    "s_add_u32 s52, s52, 4096\n"
    "s_add_u32 m0, s96, 4224\n"
    "s_nop 0\n"
    "buffer_load_dwordx4 v230, s[36:39], s52 offen lds\n"
    "s_add_u32 s52, s52, 4096\n"
    "s_add_u32 m0, s96, 8448\n"
    "s_nop 0\n"
    "buffer_load_dwordx4 v230, s[36:39], s52 offen lds\n"
    "s_add_u32 s52, s52, 4096\n"
    "s_add_u32 m0, s96, 12672\n"
    "s_nop 0\n"
    "buffer_load_dwordx4 v230, s[36:39], s52 offen lds\n"
    "s_add_u32 s52, s52, 4096\n"
    "v_and_b32 v1, 15, v0\n"
    "v_lshrrev_b32 v2, 4, v0\n"
    "v_mul_u32_u24 v228, 1056, v1\n"
    "v_lshl_add_u32 v228, v2, 4, v228\n"
    "v_add_u32 v228, 2048, v228\n"
    "v_lshrrev_b32 v5, 1, v1\n"
    "v_mul_u32_u24 v5, 1056, v5\n"
    "v_and_b32 v6, 1, v1\n"
    "v_lshl_add_u32 v5, v6, 9, v5\n"
    "v_lshl_add_u32 v5, v2, 4, v5\n"
    "v_add_u32 v236, s59, v5\n"
    "s_add_u32 s80, s59, 8448\n"
    "v_lshrrev_b32 v5, 5, v0\n"
    "v_mul_u32_u24 v5, 288, v5\n"
    "v_and_b32 v6, 31, v0\n"
    "v_lshl_add_u32 v5, v6, 3, v5\n"
    "v_add_u32 v237, s80, v5\n"
    "v_lshrrev_b32 v5, 3, v0\n"
    "v_and_b32 v6, 7, v0\n"
    "v_mul_u32_u24 v7, 288, v6\n"
    "v_lshl_add_u32 v7, v5, 3, v7\n"
    "v_add_u32 v238, s80, v7\n"
    "v_lshlrev_b32 v7, 9, v5\n"
    "v_lshl_add_u32 v232, v6, 4, v7\n"
    "v_mov_b32 v233, v228\n"
    "s_lshl_b32 s80, %2, 8\n"
    "v_lshlrev_b32 v5, 2, v0\n"
    "v_add_u32 v228, s80, v5\n"
    "v_mov_b32 v5, 2\n"
    "ds_write_b32 v228, v5\n"
    "v_mov_b32 v6, 0\n"
    "ds_write_b32 v228, v6 offset:1024\n"
    "v_bfe_u32 v5, v0, 2, 3\n"
    "v_lshrrev_b32 v6, 2, v5\n"
    "v_lshl_add_u32 v7, v5, 1, v6\n"
    "v_lshlrev_b32 v7, 5, v7\n"
    "v_lshrrev_b32 v8, 5, v0\n"
    "v_lshl_add_u32 v7, v8, 10, v7\n"
    "v_and_b32 v9, 3, v0\n"
    "v_lshl_add_u32 v7, v9, 3, v7\n"
    "v_add_u32 v234, 2048, v7\n"
    "v_bfe_u32 v9, v0, 1, 1\n"
    "v_xor_b32 v9, v9, v6\n"
    "v_lshlrev_b32 v7, 10, v5\n"
    "v_lshl_add_u32 v7, v8, 6, v7\n"
    "v_lshl_add_u32 v7, v9, 5, v7\n"
    "v_and_b32 v9, 1, v0\n"
    "v_lshl_add_u32 v231, v9, 4, v7\n"
    "v_and_b32 v9, 3, v0\n"
    "v_cmp_eq_u32 vcc, 0, v9\n"
    "v_mov_b32 v10, 0x3f803f80\n"
    "s_nop 1\n"
    "v_cndmask_b32 v9, 0, v10, vcc\n"
    "v_accvgpr_write_b32 a144, v9\n"
    "v_accvgpr_write_b32 a145, v9\n"
    "v_accvgpr_write_b32 a146, v9\n"
    "v_accvgpr_write_b32 a147, v9\n"
    "s_and_b32 s80, %2, 1\n"
    "s_lshl_b32 s80, s80, 13\n"
    "s_lshr_b32 s81, %2, 1\n"
    "s_lshl_b32 s81, s81, 9\n"
    "s_add_u32 s64, s80, s81\n"
    "s_mov_b32 s56, 0x3db8aa3b\n"
    "s_mov_b32 s83, 0xbdb8aa3b\n"
    "s_mov_b32 s57, 0x4f800000\n"
    "s_mov_b64 s[84:85], 0\n"
    "s_mov_b32 s65, 3\n"
    "s_mov_b32 s24, 0\n"
    "s_mov_b32 s25, 0\n"
    "s_mov_b32 s26, 0\n"
    "s_mov_b32 s27, 0\n"
    "s_mov_b32 s28, 0\n"
    "s_mov_b32 s29, 0\n"
    "s_mov_b32 s30, 0\n"
    "s_mov_b32 s31, 0\n"
    "s_mov_b32 s35, 0\n"
    "s_mov_b32 s68, s72\n"
    "s_mov_b32 s69, s73\n"
    "s_mov_b32 s70, s74\n"
    "s_mov_b32 s71, s75\n"
    "s_mov_b32 s33, s34\n"
    "s_and_b32 s80, s23, 255\n"
    "s_bfe_u32 s81, s23, 0x20008\n"
    "s_lshr_b32 s23, s23, 10\n"
    "s_lshl_b32 s74, s80, 18\n"
    "s_mul_i32 s80, s80, 376832\n"
    "s_lshl_b32 s34, s81, 7\n"
    "s_and_b32 s34, s34, 384\n"
    "s_lshl_b32 s81, s81, 17\n"
    "s_add_u32 s81, s81, s79\n"
    "s_add_u32 s72, s80, s81\n"
    "s_add_u32 s73, s80, 376832\n"
    "s_cmp_gt_u32 s81, 344064\n"
    "s_cselect_b32 s75, 0xff800000, 0\n"
    "s_cmp_lt_u32 s77, 3\n"
    "s_cselect_b32 s73, s73, 0\n"
    "s_cselect_b32 s76, 0x4000000, 0\n"
    "s_add_u32 s77, s77, 1\n"
    "v_mov_b32 v196, 0\n"
    "v_mov_b32 v197, 0\n"
    "v_mov_b32 v198, 0\n"
    "v_mov_b32 v199, 0\n"
    "v_mov_b32 v244, 0\n"
    "v_mov_b32 v200, 0\n"
    "v_mov_b32 v201, 0\n"
    "v_mov_b32 v202, 0\n"
    "v_mov_b32 v203, 0\n"
    "v_mov_b32 v245, 0\n"
    "v_mov_b32 v204, 0\n"
    "v_mov_b32 v205, 0\n"
    "v_mov_b32 v206, 0\n"
    "v_mov_b32 v207, 0\n"
    "v_mov_b32 v246, 0\n"
    "v_mov_b32 v208, 0\n"
    "v_mov_b32 v209, 0\n"
    "v_mov_b32 v210, 0\n"
    "v_mov_b32 v211, 0\n"
    "v_mov_b32 v247, 0\n"
    "v_mov_b32 v128, v233\n"
    "s_add_u32 s55, s62, 16896\n"
    "s_add_u32 m0, s55, 0\n"
    "s_nop 0\n"
    "buffer_load_dwordx4 v230, s[36:39], s52 offen lds\n"
    "s_add_u32 s52, s52, 4096\n"
    "s_add_u32 m0, s55, 4224\n"
    "s_nop 0\n"
    "buffer_load_dwordx4 v230, s[36:39], s52 offen lds\n"
    "s_add_u32 s52, s52, 4096\n"
    "s_add_u32 m0, s55, 8448\n"
    "s_nop 0\n"
    "buffer_load_dwordx4 v230, s[36:39], s52 offen lds\n"
    "s_add_u32 s52, s52, 4096\n"
    "s_add_u32 m0, s55, 12672\n"
    "s_nop 0\n"
    "buffer_load_dwordx4 v230, s[36:39], s52 offen lds\n"
    "s_add_u32 s52, s52, 4096\n"
    "s_add_u32 s96, s62, 33792\n"
    "s_add_u32 m0, s96, 0\n"
    "s_nop 0\n"
    "buffer_load_dwordx4 v230, s[36:39], s52 offen lds\n"
    "s_add_u32 s52, s52, 4096\n"
    "s_add_u32 m0, s96, 4224\n"
    "s_nop 0\n"
    "buffer_load_dwordx4 v230, s[36:39], s52 offen lds\n"
    "s_add_u32 s52, s52, 4096\n"
    "s_add_u32 m0, s96, 8448\n"
    "s_nop 0\n"
    "buffer_load_dwordx4 v230, s[36:39], s52 offen lds\n"
    "s_add_u32 s52, s52, 4096\n"
    "s_add_u32 m0, s96, 12672\n"
    "s_nop 0\n"
    "buffer_load_dwordx4 v230, s[36:39], s52 offen lds\n"
    "s_add_u32 s52, s52, 4096\n"
    "s_add_u32 s58, s82, 24576\n"
    "s_add_u32 m0, s59, 0\n"
    "s_nop 0\n"
    "buffer_load_dwordx4 v230, s[44:47], s58 offen nt lds\n"
    "s_add_u32 s58, s58, 1024\n"
    "s_add_u32 m0, s59, 1056\n"
    "s_nop 0\n"
    "buffer_load_dwordx4 v230, s[44:47], s58 offen nt lds\n"
    "s_add_u32 s58, s58, 1024\n"
    "s_add_u32 m0, s59, 2112\n"
    "s_nop 0\n"
    "buffer_load_dwordx4 v230, s[44:47], s58 offen nt lds\n"
    "s_add_u32 s58, s58, 1024\n"
    "s_add_u32 m0, s59, 3168\n"
    "s_nop 0\n"
    "buffer_load_dwordx4 v230, s[44:47], s58 offen nt lds\n"
    "s_add_u32 s58, s58, 1024\n"
    "s_add_u32 m0, s59, 4224\n"
    "s_nop 0\n"
    "buffer_load_dwordx4 v230, s[44:47], s58 offen nt lds\n"
    "s_add_u32 s58, s58, 1024\n"
    "s_add_u32 m0, s59, 5280\n"
    "s_nop 0\n"
    "buffer_load_dwordx4 v230, s[44:47], s58 offen nt lds\n"
    "s_add_u32 s58, s58, 1024\n"
    "s_add_u32 m0, s59, 6336\n"
    "s_nop 0\n"
    "buffer_load_dwordx4 v230, s[44:47], s58 offen nt lds\n"
    "s_add_u32 s58, s58, 1024\n"
    "s_add_u32 m0, s59, 7392\n"
    "s_nop 0\n"
    "buffer_load_dwordx4 v230, s[44:47], s58 offen nt lds\n"
    "s_add_u32 s58, s58, 1024\n"
    "s_add_u32 s96, s62, 67584\n"
    "s_waitcnt vmcnt(20)\n"
    "ds_read_b128 a[64:67], v236 offset:8448\n"
    "ds_read_b128 a[68:71], v236 offset:8512\n"
    "ds_read_b128 a[72:75], v236 offset:8576\n"
    "ds_read_b128 a[76:79], v236 offset:8640\n"
    "ds_read_b128 a[80:83], v236 offset:8704\n"
    "ds_read_b128 a[84:87], v236 offset:8768\n"
    "ds_read_b128 a[88:91], v236 offset:8832\n"
    "ds_read_b128 a[92:95], v236 offset:8896\n"
    "s_waitcnt vmcnt(8)\n"
    "s_barrier\n"
    "ds_read_b128 v[148:151], v128 offset:0\n"
    "ds_read_b128 v[152:155], v128 offset:64\n"
    "ds_read_b128 v[156:159], v128 offset:128\n"
    "ds_read_b128 v[160:163], v128 offset:192\n"
    "ds_read_b128 v[164:167], v128 offset:256\n"
    "ds_read_b128 v[168:171], v128 offset:320\n"
    "ds_read_b128 v[172:175], v128 offset:384\n"
    "ds_read_b128 v[176:179], v128 offset:448\n"
    "s_waitcnt lgkmcnt(0)\n"
    "v_mfma_f32_16x16x32_bf16 v[80:83], v[148:151], a[64:67], 0\n"
    "s_add_u32 s58, s82, 8192\n"
    "ds_read_b128 v[180:183], v128 offset:512\n"
    "v_mfma_f32_16x16x32_bf16 v[80:83], v[152:155], a[68:71], v[80:83]\n"
    "s_add_u32 m0, s59, 8448\n"
    "ds_read_b128 v[184:187], v128 offset:576\n"
    "v_mfma_f32_16x16x32_bf16 v[80:83], v[156:159], a[72:75], v[80:83]\n"
    "ds_read_b128 v[188:191], v128 offset:640\n"
    "v_mfma_f32_16x16x32_bf16 v[80:83], v[160:163], a[76:79], v[80:83]\n"
    "ds_read_b128 v[192:195], v128 offset:704\n"
    "v_mfma_f32_16x16x32_bf16 v[80:83], v[164:167], a[80:83], v[80:83]\n"
    "ds_read_b128 v[196:199], v128 offset:768\n"
    "buffer_load_dwordx4 v230, s[44:47], s58 offen nt lds\n"
    "s_add_u32 s58, s58, 1024\n"
    "v_mfma_f32_16x16x32_bf16 v[80:83], v[168:171], a[84:87], v[80:83]\n"
    "ds_read_b128 v[200:203], v128 offset:832\n"
    "v_mfma_f32_16x16x32_bf16 v[80:83], v[172:175], a[88:91], v[80:83]\n"
    "ds_read_b128 v[204:207], v128 offset:896\n"
    "v_mfma_f32_16x16x32_bf16 v[80:83], v[176:179], a[92:95], v[80:83]\n"
    "s_waitcnt lgkmcnt(3)\n"
    "ds_read_b128 v[208:211], v128 offset:960\n"
    "s_add_u32 m0, s59, 9504\n"
    "v_mfma_f32_16x16x32_bf16 v[84:87], v[180:183], a[64:67], 0\n"
    "ds_read_b128 v[148:151], v128 offset:16896\n"
    "v_mfma_f32_16x16x32_bf16 v[84:87], v[184:187], a[68:71], v[84:87]\n"
    "ds_read_b128 v[152:155], v128 offset:16960\n"
    "v_mfma_f32_16x16x32_bf16 v[84:87], v[188:191], a[72:75], v[84:87]\n"
    "ds_read_b128 v[156:159], v128 offset:17024\n"
    "buffer_load_dwordx4 v230, s[44:47], s58 offen nt lds\n"
    "s_add_u32 s58, s58, 1024\n"
    "v_mfma_f32_16x16x32_bf16 v[84:87], v[192:195], a[76:79], v[84:87]\n"
    "s_waitcnt lgkmcnt(3)\n"
    "ds_read_b128 v[160:163], v128 offset:17088\n"
    "v_max3_f32 v96, v80, v81, v82\n"
    "v_max_f32 v96, v96, v83\n"
    "v_mfma_f32_16x16x32_bf16 v[84:87], v[196:199], a[80:83], v[84:87]\n"
    "ds_read_b128 v[164:167], v128 offset:17152\n"
    "v_mov_b32 v97, v96\n"
    "v_mfma_f32_16x16x32_bf16 v[84:87], v[200:203], a[84:87], v[84:87]\n"
    "s_add_u32 m0, s59, 10560\n"
    "ds_read_b128 v[168:171], v128 offset:17216\n"
    "v_permlane16_swap_b32 v96, v97\n"
    "v_max_f32 v96, v96, v97\n"
    "v_mfma_f32_16x16x32_bf16 v[84:87], v[204:207], a[88:91], v[84:87]\n"
    "ds_read_b128 v[172:175], v128 offset:17280\n"
    "v_mov_b32 v97, v96\n"
    "v_mfma_f32_16x16x32_bf16 v[84:87], v[208:211], a[92:95], v[84:87]\n"
    "s_waitcnt lgkmcnt(3)\n"
    "ds_read_b128 v[176:179], v128 offset:17344\n"
    "v_permlane32_swap_b32 v96, v97\n"
    "v_max_f32 v96, v96, v97\n"
    "v_mfma_f32_16x16x32_bf16 v[88:91], v[148:151], a[64:67], 0\n"
    "ds_read_b128 v[180:183], v128 offset:17408\n"
    "buffer_load_dwordx4 v230, s[44:47], s58 offen nt lds\n"
    "s_add_u32 s58, s58, 1024\n"
    "v_mfma_f32_16x16x32_bf16 v[88:91], v[152:155], a[68:71], v[88:91]\n"
    "ds_read_b128 v[184:187], v128 offset:17472\n"
    "v_mul_f32 v242, s83, v96\n"
    "v_add_f32 v242, s71, v242\n"
    "v_mfma_f32_16x16x32_bf16 v[88:91], v[156:159], a[72:75], v[88:91]\n"
    "ds_read_b128 v[188:191], v128 offset:17536\n"
    "v_fma_f32 v80, v80, s56, v242\n"
    "v_fma_f32 v81, v81, s56, v242\n"
    "v_mfma_f32_16x16x32_bf16 v[88:91], v[160:163], a[76:79], v[88:91]\n"
    "s_waitcnt lgkmcnt(3)\n"
    "ds_read_b128 v[192:195], v128 offset:17600\n"
    "v_fma_f32 v82, v82, s56, v242\n"
    "v_fma_f32 v83, v83, s56, v242\n"
    "v_mfma_f32_16x16x32_bf16 v[88:91], v[164:167], a[80:83], v[88:91]\n"
    "s_add_u32 m0, s59, 11616\n"
    "ds_read_b128 v[196:199], v128 offset:17664\n"
    "v_exp_f32 v80, v80\n"
    "v_mfma_f32_16x16x32_bf16 v[88:91], v[168:171], a[84:87], v[88:91]\n"
    "ds_read_b128 v[200:203], v128 offset:17728\n"
    "v_exp_f32 v81, v81\n"
    "v_mfma_f32_16x16x32_bf16 v[88:91], v[172:175], a[88:91], v[88:91]\n"
    "ds_read_b128 v[204:207], v128 offset:17792\n"
    "buffer_load_dwordx4 v230, s[44:47], s58 offen nt lds\n"
    "s_add_u32 s58, s58, 1024\n"
    "v_mfma_f32_16x16x32_bf16 v[88:91], v[176:179], a[92:95], v[88:91]\n"
    "s_waitcnt lgkmcnt(3)\n"
    "ds_read_b128 v[208:211], v128 offset:17856\n"
    "v_exp_f32 v82, v82\n"
    "v_mfma_f32_16x16x32_bf16 v[92:95], v[180:183], a[64:67], 0\n"
    "ds_read_b128 v[148:151], v128 offset:33792\n"
    "v_exp_f32 v83, v83\n"
    "v_mfma_f32_16x16x32_bf16 v[92:95], v[184:187], a[68:71], v[92:95]\n"
    "s_add_u32 m0, s59, 12672\n"
    "ds_read_b128 v[152:155], v128 offset:33856\n"
    "v_cvt_pk_bf16_f32 v8, v80, v81\n"
    "v_cvt_pk_bf16_f32 v9, v82, v83\n"
    "v_mfma_f32_16x16x32_bf16 v[92:95], v[188:191], a[72:75], v[92:95]\n"
    "ds_read_b128 v[156:159], v128 offset:33920\n"
    "v_fma_f32 v84, v84, s56, v242\n"
    "v_fma_f32 v85, v85, s56, v242\n"
    "v_mfma_f32_16x16x32_bf16 v[92:95], v[192:195], a[76:79], v[92:95]\n"
    "s_waitcnt lgkmcnt(3)\n"
    "ds_read_b128 v[160:163], v128 offset:33984\n"
    "v_fma_f32 v86, v86, s56, v242\n"
    "v_fma_f32 v87, v87, s56, v242\n"
    "v_mfma_f32_16x16x32_bf16 v[92:95], v[196:199], a[80:83], v[92:95]\n"
    "ds_read_b128 v[164:167], v128 offset:34048\n"
    "buffer_load_dwordx4 v230, s[44:47], s58 offen nt lds\n"
    "s_add_u32 s58, s58, 1024\n"
    "v_mfma_f32_16x16x32_bf16 v[92:95], v[200:203], a[84:87], v[92:95]\n"
    "ds_read_b128 v[168:171], v128 offset:34112\n"
    "v_exp_f32 v84, v84\n"
    "v_mfma_f32_16x16x32_bf16 v[92:95], v[204:207], a[88:91], v[92:95]\n"
    "ds_read_b128 v[172:175], v128 offset:34176\n"
    "v_exp_f32 v85, v85\n"
    "v_mfma_f32_16x16x32_bf16 v[92:95], v[208:211], a[92:95], v[92:95]\n"
    "s_waitcnt lgkmcnt(3)\n"
    "ds_read_b128 v[176:179], v128 offset:34240\n"
    "v_exp_f32 v86, v86\n"
    "v_mfma_f32_16x16x32_bf16 v[80:83], v[148:151], a[64:67], 0\n"
    "s_add_u32 m0, s59, 13728\n"
    "ds_read_b128 v[180:183], v128 offset:34304\n"
    "v_exp_f32 v87, v87\n"
    "v_mfma_f32_16x16x32_bf16 v[80:83], v[152:155], a[68:71], v[80:83]\n"
    "ds_read_b128 v[184:187], v128 offset:34368\n"
    "v_cvt_pk_bf16_f32 v10, v84, v85\n"
    "v_cvt_pk_bf16_f32 v11, v86, v87\n"
    "v_mfma_f32_16x16x32_bf16 v[80:83], v[156:159], a[72:75], v[80:83]\n"
    "ds_read_b128 v[188:191], v128 offset:34432\n"
    "buffer_load_dwordx4 v230, s[44:47], s58 offen nt lds\n"
    "s_add_u32 s58, s58, 1024\n"
    "v_mfma_f32_16x16x32_bf16 v[80:83], v[160:163], a[76:79], v[80:83]\n"
    "s_waitcnt lgkmcnt(3)\n"
    "ds_read_b128 v[192:195], v128 offset:34496\n"
    "v_fma_f32 v88, v88, s56, v242\n"
    "v_fma_f32 v89, v89, s56, v242\n"
    "v_mfma_f32_16x16x32_bf16 v[80:83], v[164:167], a[80:83], v[80:83]\n"
    "ds_read_b128 v[196:199], v128 offset:34560\n"
    "v_fma_f32 v90, v90, s56, v242\n"
    "v_fma_f32 v91, v91, s56, v242\n"
    "v_mfma_f32_16x16x32_bf16 v[80:83], v[168:171], a[84:87], v[80:83]\n"
    "s_add_u32 m0, s59, 14784\n"
    "ds_read_b128 v[200:203], v128 offset:34624\n"
    "v_fma_f32 v92, v92, s56, v242\n"
    "v_fma_f32 v93, v93, s56, v242\n"
    "v_mfma_f32_16x16x32_bf16 v[80:83], v[172:175], a[88:91], v[80:83]\n"
    "ds_read_b128 v[204:207], v128 offset:34688\n"
    "v_fma_f32 v94, v94, s56, v242\n"
    "v_fma_f32 v95, v95, s56, v242\n"
    "v_mfma_f32_16x16x32_bf16 v[80:83], v[176:179], a[92:95], v[80:83]\n"
    "s_waitcnt lgkmcnt(3)\n"
    "ds_read_b128 v[208:211], v128 offset:34752\n"
    "v_exp_f32 v88, v88\n"
    "v_mfma_f32_16x16x32_bf16 v[84:87], v[180:183], a[64:67], 0\n"
    "ds_read_b128 v[148:151], v128 offset:0\n"
    "buffer_load_dwordx4 v230, s[44:47], s58 offen nt lds\n"
    "s_add_u32 s58, s58, 1024\n"
    "v_mfma_f32_16x16x32_bf16 v[84:87], v[184:187], a[68:71], v[84:87]\n"
    "ds_read_b128 v[152:155], v128 offset:64\n"
    "v_exp_f32 v89, v89\n"
    "v_mfma_f32_16x16x32_bf16 v[84:87], v[188:191], a[72:75], v[84:87]\n"
    "ds_read_b128 v[156:159], v128 offset:128\n"
    "v_exp_f32 v90, v90\n"
    "v_mfma_f32_16x16x32_bf16 v[84:87], v[192:195], a[76:79], v[84:87]\n"
    "s_waitcnt lgkmcnt(3)\n"
    "ds_read_b128 v[160:163], v128 offset:192\n"
    "v_fma_f32 v80, v80, s56, v242\n"
    "v_fma_f32 v81, v81, s56, v242\n"
    "v_mfma_f32_16x16x32_bf16 v[84:87], v[196:199], a[80:83], v[84:87]\n"
    "s_add_u32 m0, s59, 15840\n"
    "ds_read_b128 v[164:167], v128 offset:256\n"
    "v_fma_f32 v82, v82, s56, v242\n"
    "v_fma_f32 v83, v83, s56, v242\n"
    "v_mfma_f32_16x16x32_bf16 v[84:87], v[200:203], a[84:87], v[84:87]\n"
    "ds_read_b128 v[168:171], v128 offset:320\n"
    "v_exp_f32 v91, v91\n"
    "v_mfma_f32_16x16x32_bf16 v[84:87], v[204:207], a[88:91], v[84:87]\n"
    "ds_read_b128 v[172:175], v128 offset:384\n"
    "buffer_load_dwordx4 v230, s[44:47], s58 offen nt lds\n"
    "s_add_u32 s58, s58, 1024\n"
    "v_mfma_f32_16x16x32_bf16 v[84:87], v[208:211], a[92:95], v[84:87]\n"
    "ds_read_b128 v[176:179], v128 offset:448\n"
    "v_exp_f32 v92, v92\n"
    "v_exp_f32 v93, v93\n"
    "v_exp_f32 v94, v94\n"
    "v_exp_f32 v95, v95\n"
    "v_exp_f32 v80, v80\n"
    "v_exp_f32 v81, v81\n"
    "v_exp_f32 v82, v82\n"
    "v_fma_f32 v84, v84, s56, v242\n"
    "v_fma_f32 v85, v85, s56, v242\n"
    "v_fma_f32 v86, v86, s56, v242\n"
    "v_fma_f32 v87, v87, s56, v242\n"
    "v_exp_f32 v83, v83\n"
    "v_exp_f32 v84, v84\n"
    "v_exp_f32 v85, v85\n"
    "v_exp_f32 v86, v86\n"
    "v_exp_f32 v87, v87\n"
    "v_cvt_pk_bf16_f32 v24, v88, v89\n"
    "v_cvt_pk_bf16_f32 v25, v90, v91\n"
    "v_cvt_pk_bf16_f32 v26, v92, v93\n"
    "v_cvt_pk_bf16_f32 v27, v94, v95\n"
    "v_cvt_pk_bf16_f32 v36, v80, v81\n"
    "v_cvt_pk_bf16_f32 v37, v82, v83\n"
    "v_cvt_pk_bf16_f32 v38, v84, v85\n"
    "v_cvt_pk_bf16_f32 v39, v86, v87\n"
    "s_waitcnt lgkmcnt(4)\n"
    "s_waitcnt vmcnt(8)\n"
    "ds_read_b128 a[96:99], v236 offset:0\n"
    "ds_read_b128 a[100:103], v236 offset:64\n"
    "ds_read_b128 a[104:107], v236 offset:128\n"
    "ds_read_b128 a[108:111], v236 offset:192\n"
    "ds_read_b128 a[112:115], v236 offset:256\n"
    "ds_read_b128 a[116:119], v236 offset:320\n"
    "ds_read_b128 a[120:123], v236 offset:384\n"
    "ds_read_b128 a[124:127], v236 offset:448\n"
    "s_waitcnt lgkmcnt(0)\n"
    "v_mfma_f32_16x16x32_bf16 v[88:91], v[148:151], a[96:99], 0\n"
    "s_mov_b32 s58, s82\n"
    "ds_read_b128 v[180:183], v128 offset:512\n"
    "v_mfma_f32_16x16x32_bf16 v[88:91], v[152:155], a[100:103], v[88:91]\n"
    "s_add_u32 m0, s59, 0\n"
    "ds_read_b128 v[184:187], v128 offset:576\n"
    "v_mfma_f32_16x16x32_bf16 v[88:91], v[156:159], a[104:107], v[88:91]\n"
    "ds_read_b128 v[188:191], v128 offset:640\n"
    "v_mfma_f32_16x16x32_bf16 v[88:91], v[160:163], a[108:111], v[88:91]\n"
    "s_waitcnt lgkmcnt(11)\n"
    "ds_read_b128 v[192:195], v128 offset:704\n"
    "v_mfma_f32_16x16x32_bf16 v[88:91], v[164:167], a[112:115], v[88:91]\n"
    "ds_read_b128 v[196:199], v128 offset:768\n"
    "buffer_load_dwordx4 v230, s[44:47], s58 offen nt lds\n"
    "s_add_u32 s58, s58, 1024\n"
    "v_mfma_f32_16x16x32_bf16 v[88:91], v[168:171], a[116:119], v[88:91]\n"
    "ds_read_b128 v[200:203], v128 offset:832\n"
    "v_mfma_f32_16x16x32_bf16 v[88:91], v[172:175], a[120:123], v[88:91]\n"
    "ds_read_b128 v[204:207], v128 offset:896\n"
    "v_mfma_f32_16x16x32_bf16 v[88:91], v[176:179], a[124:127], v[88:91]\n"
    "s_waitcnt lgkmcnt(3)\n"
    "ds_read_b128 v[208:211], v128 offset:960\n"
    "s_add_u32 m0, s59, 1056\n"
    "v_mfma_f32_16x16x32_bf16 v[92:95], v[180:183], a[96:99], 0\n"
    "ds_read_b128 v[148:151], v128 offset:16896\n"
    "v_mfma_f32_16x16x32_bf16 v[92:95], v[184:187], a[100:103], v[92:95]\n"
    "ds_read_b128 v[152:155], v128 offset:16960\n"
    "v_mfma_f32_16x16x32_bf16 v[92:95], v[188:191], a[104:107], v[92:95]\n"
    "ds_read_b128 v[156:159], v128 offset:17024\n"
    "buffer_load_dwordx4 v230, s[44:47], s58 offen nt lds\n"
    "s_add_u32 s58, s58, 1024\n"
    "v_mfma_f32_16x16x32_bf16 v[92:95], v[192:195], a[108:111], v[92:95]\n"
    "s_waitcnt lgkmcnt(3)\n"
    "ds_read_b128 v[160:163], v128 offset:17088\n"
    "v_max3_f32 v98, v88, v89, v90\n"
    "v_max_f32 v98, v98, v91\n"
    "v_mfma_f32_16x16x32_bf16 v[92:95], v[196:199], a[112:115], v[92:95]\n"
    "ds_read_b128 v[164:167], v128 offset:17152\n"
    "v_mov_b32 v99, v98\n"
    "v_mfma_f32_16x16x32_bf16 v[92:95], v[200:203], a[116:119], v[92:95]\n"
    "s_add_u32 m0, s59, 2112\n"
    "ds_read_b128 v[168:171], v128 offset:17216\n"
    "v_permlane16_swap_b32 v98, v99\n"
    "v_max_f32 v98, v98, v99\n"
    "v_mfma_f32_16x16x32_bf16 v[92:95], v[204:207], a[120:123], v[92:95]\n"
    "ds_read_b128 v[172:175], v128 offset:17280\n"
    "v_mov_b32 v99, v98\n"
    "v_mfma_f32_16x16x32_bf16 v[92:95], v[208:211], a[124:127], v[92:95]\n"
    "s_waitcnt lgkmcnt(3)\n"
    "ds_read_b128 v[176:179], v128 offset:17344\n"
    "v_permlane32_swap_b32 v98, v99\n"
    "v_max_f32 v98, v98, v99\n"
    "v_mfma_f32_16x16x32_bf16 v[80:83], v[148:151], a[96:99], 0\n"
    "ds_read_b128 v[180:183], v128 offset:17408\n"
    "buffer_load_dwordx4 v230, s[44:47], s58 offen nt lds\n"
    "s_add_u32 s58, s58, 1024\n"
    "v_mfma_f32_16x16x32_bf16 v[80:83], v[152:155], a[100:103], v[80:83]\n"
    "ds_read_b128 v[184:187], v128 offset:17472\n"
    "v_mul_f32 v243, s83, v98\n"
    "v_add_f32 v243, s71, v243\n"
    "v_mfma_f32_16x16x32_bf16 v[80:83], v[156:159], a[104:107], v[80:83]\n"
    "ds_read_b128 v[188:191], v128 offset:17536\n"
    "v_fma_f32 v88, v88, s56, v243\n"
    "v_fma_f32 v89, v89, s56, v243\n"
    "v_mfma_f32_16x16x32_bf16 v[80:83], v[160:163], a[108:111], v[80:83]\n"
    "s_waitcnt lgkmcnt(3)\n"
    "ds_read_b128 v[192:195], v128 offset:17600\n"
    "v_fma_f32 v90, v90, s56, v243\n"
    "v_fma_f32 v91, v91, s56, v243\n"
    "v_mfma_f32_16x16x32_bf16 v[80:83], v[164:167], a[112:115], v[80:83]\n"
    "s_add_u32 m0, s59, 3168\n"
    "ds_read_b128 v[196:199], v128 offset:17664\n"
    "v_exp_f32 v88, v88\n"
    "v_mfma_f32_16x16x32_bf16 v[80:83], v[168:171], a[116:119], v[80:83]\n"
    "ds_read_b128 v[200:203], v128 offset:17728\n"
    "v_exp_f32 v89, v89\n"
    "v_mfma_f32_16x16x32_bf16 v[80:83], v[172:175], a[120:123], v[80:83]\n"
    "ds_read_b128 v[204:207], v128 offset:17792\n"
    "buffer_load_dwordx4 v230, s[44:47], s58 offen nt lds\n"
    "s_add_u32 s58, s58, 1024\n"
    "v_mfma_f32_16x16x32_bf16 v[80:83], v[176:179], a[124:127], v[80:83]\n"
    "ds_read_b128 v[208:211], v128 offset:17856\n"
    "v_exp_f32 v90, v90\n"
    "v_mfma_f32_16x16x32_bf16 a[136:139], a[144:147], v[8:11], 0\n"
    "s_waitcnt lgkmcnt(4)\n"
    "v_exp_f32 v91, v91\n"
    "v_cvt_pk_bf16_f32 v12, v88, v89\n"
    "v_mfma_f32_16x16x32_bf16 v[84:87], v[180:183], a[96:99], 0\n"
    "s_add_u32 m0, s59, 4224\n"
    "ds_read_b128 v[148:151], v128 offset:33792\n"
    "v_cvt_pk_bf16_f32 v13, v90, v91\n"
    "v_fma_f32 v92, v92, s56, v243\n"
    "v_mfma_f32_16x16x32_bf16 v[84:87], v[184:187], a[100:103], v[84:87]\n"
    "ds_read_b128 v[152:155], v128 offset:33856\n"
    "v_fma_f32 v93, v93, s56, v243\n"
    "v_fma_f32 v94, v94, s56, v243\n"
    "v_mfma_f32_16x16x32_bf16 v[84:87], v[188:191], a[104:107], v[84:87]\n"
    "ds_read_b128 v[156:159], v128 offset:33920\n"
    "v_fma_f32 v95, v95, s56, v243\n"
    "v_fma_f32 v80, v80, s56, v243\n"
    "v_mfma_f32_16x16x32_bf16 v[84:87], v[192:195], a[108:111], v[84:87]\n"
    "s_waitcnt lgkmcnt(3)\n"
    "ds_read_b128 v[160:163], v128 offset:33984\n"
    "buffer_load_dwordx4 v230, s[44:47], s58 offen nt lds\n"
    "v_mfma_f32_16x16x32_bf16 v[84:87], v[196:199], a[112:115], v[84:87]\n"
    "s_add_u32 s58, s58, 1024\n"
    "ds_read_b128 v[164:167], v128 offset:34048\n"
    "v_exp_f32 v92, v92\n"
    "v_mfma_f32_16x16x32_bf16 v[84:87], v[200:203], a[116:119], v[84:87]\n"
    "ds_read_b128 v[168:171], v128 offset:34112\n"
    "v_exp_f32 v93, v93\n"
    "v_mfma_f32_16x16x32_bf16 v[84:87], v[204:207], a[120:123], v[84:87]\n"
    "s_add_u32 m0, s59, 5280\n"
    "ds_read_b128 v[172:175], v128 offset:34176\n"
    "v_exp_f32 v94, v94\n"
    "v_mfma_f32_16x16x32_bf16 v[84:87], v[208:211], a[124:127], v[84:87]\n"
    "s_waitcnt lgkmcnt(3)\n"
    "ds_read_b128 v[176:179], v128 offset:34240\n"
    "v_exp_f32 v95, v95\n"
    "v_mfma_f32_16x16x32_bf16 v[88:91], v[148:151], a[96:99], 0\n"
    "ds_read_b128 v[180:183], v128 offset:34304\n"
    "v_cvt_pk_bf16_f32 v14, v92, v93\n"
    "v_cvt_pk_bf16_f32 v15, v94, v95\n"
    "v_mfma_f32_16x16x32_bf16 v[88:91], v[152:155], a[100:103], v[88:91]\n"
    "ds_read_b128 v[184:187], v128 offset:34368\n"
    "buffer_load_dwordx4 v230, s[44:47], s58 offen nt lds\n"
    "s_add_u32 s58, s58, 1024\n"
    "v_mfma_f32_16x16x32_bf16 v[88:91], v[156:159], a[104:107], v[88:91]\n"
    "ds_read_b128 v[188:191], v128 offset:34432\n"
    "v_fma_f32 v81, v81, s56, v243\n"
    "v_fma_f32 v82, v82, s56, v243\n"
    "v_mfma_f32_16x16x32_bf16 v[88:91], v[160:163], a[108:111], v[88:91]\n"
    "s_waitcnt lgkmcnt(3)\n"
    "ds_read_b128 v[192:195], v128 offset:34496\n"
    "v_fma_f32 v83, v83, s56, v243\n"
    "v_fma_f32 v84, v84, s56, v243\n"
    "v_mfma_f32_16x16x32_bf16 v[88:91], v[164:167], a[112:115], v[88:91]\n"
    "s_add_u32 m0, s59, 6336\n"
    "ds_read_b128 v[196:199], v128 offset:34560\n"
    "v_fma_f32 v85, v85, s56, v243\n"
    "v_fma_f32 v86, v86, s56, v243\n"
    "v_mfma_f32_16x16x32_bf16 v[88:91], v[168:171], a[116:119], v[88:91]\n"
    "ds_read_b128 v[200:203], v128 offset:34624\n"
    "v_fma_f32 v87, v87, s56, v243\n"
    "v_mfma_f32_16x16x32_bf16 v[88:91], v[172:175], a[120:123], v[88:91]\n"
    "ds_read_b128 v[204:207], v128 offset:34688\n"
    "v_exp_f32 v80, v80\n"
    "v_mfma_f32_16x16x32_bf16 v[88:91], v[176:179], a[124:127], v[88:91]\n"
    "ds_read_b128 v[208:211], v128 offset:34752\n"
    "buffer_load_dwordx4 v230, s[44:47], s58 offen nt lds\n"
    "s_add_u32 s58, s58, 1024\n"
    "v_mfma_f32_16x16x32_bf16 a[136:139], a[144:147], v[24:27], a[136:139]\n"
    "s_waitcnt lgkmcnt(4)\n"
    "v_exp_f32 v81, v81\n"
    "v_mfma_f32_16x16x32_bf16 v[216:219], v[180:183], a[96:99], 0\n"
    "ds_read_b128 v[148:151], v128 offset:0\n"
    "v_exp_f32 v82, v82\n"
    "v_mfma_f32_16x16x32_bf16 v[216:219], v[184:187], a[100:103], v[216:219]\n"
    "s_add_u32 m0, s59, 7392\n"
    "ds_read_b128 v[152:155], v128 offset:64\n"
    "v_exp_f32 v83, v83\n"
    "v_mfma_f32_16x16x32_bf16 v[216:219], v[188:191], a[104:107], v[216:219]\n"
    "ds_read_b128 v[156:159], v128 offset:128\n"
    "v_fma_f32 v88, v88, s56, v243\n"
    "v_fma_f32 v89, v89, s56, v243\n"
    "v_mfma_f32_16x16x32_bf16 v[216:219], v[192:195], a[108:111], v[216:219]\n"
    "s_waitcnt lgkmcnt(3)\n"
    "ds_read_b128 v[160:163], v128 offset:192\n"
    "v_fma_f32 v90, v90, s56, v243\n"
    "v_fma_f32 v91, v91, s56, v243\n"
    "v_mfma_f32_16x16x32_bf16 v[216:219], v[196:199], a[112:115], v[216:219]\n"
    "ds_read_b128 v[164:167], v128 offset:256\n"
    "buffer_load_dwordx4 v230, s[44:47], s58 offen nt lds\n"
    "s_add_u32 s58, s58, 1024\n"
    "v_mfma_f32_16x16x32_bf16 v[216:219], v[200:203], a[116:119], v[216:219]\n"
    "ds_read_b128 v[168:171], v128 offset:320\n"
    "v_exp_f32 v84, v84\n"
    "v_mfma_f32_16x16x32_bf16 v[216:219], v[204:207], a[120:123], v[216:219]\n"
    "ds_read_b128 v[172:175], v128 offset:384\n"
    "v_exp_f32 v85, v85\n"
    "v_mfma_f32_16x16x32_bf16 v[216:219], v[208:211], a[124:127], v[216:219]\n"
    "ds_read_b128 v[176:179], v128 offset:448\n"
    "v_exp_f32 v86, v86\n"
    "v_exp_f32 v87, v87\n"
    "v_exp_f32 v88, v88\n"
    "v_exp_f32 v89, v89\n"
    "v_exp_f32 v90, v90\n"
    "v_exp_f32 v91, v91\n"
    "v_cvt_pk_bf16_f32 v28, v80, v81\n"
    "v_cvt_pk_bf16_f32 v29, v82, v83\n"
    "v_cvt_pk_bf16_f32 v30, v84, v85\n"
    "v_cvt_pk_bf16_f32 v31, v86, v87\n"
    "v_cvt_pk_bf16_f32 v100, v88, v89\n"
    "v_accvgpr_write_b32 a152, v100\n"
    "v_cvt_pk_bf16_f32 v101, v90, v91\n"
    "v_accvgpr_write_b32 a153, v101\n"
    "v_fma_f32 v216, v216, s56, v243\n"
    "s_waitcnt lgkmcnt(4)\n"
    "s_waitcnt vmcnt(8)\n"
    "ds_read_b128 a[32:35], v236 offset:8448\n"
    "ds_read_b128 a[36:39], v236 offset:8512\n"
    "ds_read_b128 a[40:43], v236 offset:8576\n"
    "ds_read_b128 a[44:47], v236 offset:8640\n"
    "ds_read_b128 a[48:51], v236 offset:8704\n"
    "ds_read_b128 a[52:55], v236 offset:8768\n"
    "ds_read_b128 a[56:59], v236 offset:8832\n"
    "ds_read_b128 a[60:63], v236 offset:8896\n"
    "s_waitcnt lgkmcnt(0)\n"
    "v_mfma_f32_16x16x32_bf16 v[80:83], v[148:151], a[32:35], 0\n"
    "s_add_u32 s55, s62, 50688\n"
    "ds_read_b128 v[180:183], v128 offset:512\n"
    "v_mfma_f32_16x16x32_bf16 v[80:83], v[152:155], a[36:39], v[80:83]\n"
    "s_add_u32 m0, s55, 0\n"
    "ds_read_b128 v[184:187], v128 offset:576\n"
    "v_mfma_f32_16x16x32_bf16 v[80:83], v[156:159], a[40:43], v[80:83]\n"
    "ds_read_b128 v[188:191], v128 offset:640\n"
    "v_mfma_f32_16x16x32_bf16 v[80:83], v[160:163], a[44:47], v[80:83]\n"
    "s_waitcnt lgkmcnt(11)\n"
    "ds_read_b128 v[192:195], v128 offset:704\n"
    "v_mfma_f32_16x16x32_bf16 v[80:83], v[164:167], a[48:51], v[80:83]\n"
    "ds_read_b128 v[196:199], v128 offset:768\n"
    "buffer_load_dwordx4 v230, s[36:39], s52 offen lds\n"
    "s_add_u32 s52, s52, 4096\n"
    "v_mfma_f32_16x16x32_bf16 v[80:83], v[168:171], a[52:55], v[80:83]\n"
    "ds_read_b128 v[200:203], v128 offset:832\n"
    "v_mfma_f32_16x16x32_bf16 v[80:83], v[172:175], a[56:59], v[80:83]\n"
    "s_add_u32 m0, s55, 4224\n"
    "ds_read_b128 v[204:207], v128 offset:896\n"
    "v_mfma_f32_16x16x32_bf16 v[80:83], v[176:179], a[60:63], v[80:83]\n"
    "s_waitcnt lgkmcnt(3)\n"
    "ds_read_b128 v[208:211], v128 offset:960\n"
    "v_mfma_f32_16x16x32_bf16 v[84:87], v[180:183], a[32:35], 0\n"
    "ds_read_b128 v[148:151], v128 offset:16896\n"
    "v_mfma_f32_16x16x32_bf16 v[84:87], v[184:187], a[36:39], v[84:87]\n"
    "ds_read_b128 v[152:155], v128 offset:16960\n"
    "buffer_load_dwordx4 v230, s[36:39], s52 offen lds\n"
    "s_add_u32 s52, s52, 4096\n"
    "v_mfma_f32_16x16x32_bf16 v[84:87], v[188:191], a[40:43], v[84:87]\n"
    "ds_read_b128 v[156:159], v128 offset:17024\n"
    "v_lshrrev_b32 v129, 8, v230\n"
    "v_lshl_add_u32 v129, v129, 7, s22\n"
    "v_mfma_f32_16x16x32_bf16 v[84:87], v[192:195], a[44:47], v[84:87]\n"
    "s_waitcnt lgkmcnt(3)\n"
    "ds_read_b128 v[160:163], v128 offset:17088\n"
    "v_min_u32 v129, L_end%=-L_pf%=-4, v129\n"
    "v_max3_f32 v102, v80, v81, v82\n"
    "v_mfma_f32_16x16x32_bf16 v[84:87], v[196:199], a[48:51], v[84:87]\n"
    "s_add_u32 m0, s55, 8448\n"
    "ds_read_b128 v[164:167], v128 offset:17152\n"
    "v_max_f32 v102, v102, v83\n"
    "v_mov_b32 v103, v102\n"
    "v_mfma_f32_16x16x32_bf16 v[84:87], v[200:203], a[52:55], v[84:87]\n"
    "ds_read_b128 v[168:171], v128 offset:17216\n"
    "v_permlane16_swap_b32 v102, v103\n"
    "v_max_f32 v102, v102, v103\n"
    "v_mfma_f32_16x16x32_bf16 v[84:87], v[204:207], a[56:59], v[84:87]\n"
    "ds_read_b128 v[172:175], v128 offset:17280\n"
    "buffer_load_dwordx4 v230, s[36:39], s52 offen lds\n"
    "s_add_u32 s52, s52, 4096\n"
    "v_mfma_f32_16x16x32_bf16 v[84:87], v[208:211], a[60:63], v[84:87]\n"
    "s_waitcnt lgkmcnt(3)\n"
    "ds_read_b128 v[176:179], v128 offset:17344\n"
    "v_mov_b32 v103, v102\n"
    "v_mfma_f32_16x16x32_bf16 v[88:91], v[148:151], a[32:35], 0\n"
    "s_add_u32 m0, s55, 12672\n"
    "ds_read_b128 v[180:183], v128 offset:17408\n"
    "v_permlane32_swap_b32 v102, v103\n"
    "v_max_f32 v102, v102, v103\n"
    "v_mfma_f32_16x16x32_bf16 v[88:91], v[152:155], a[36:39], v[88:91]\n"
    "ds_read_b128 v[184:187], v128 offset:17472\n"
    "v_mul_f32 v241, s83, v102\n"
    "v_fma_f32 v80, v80, s56, v241\n"
    "v_mfma_f32_16x16x32_bf16 v[88:91], v[156:159], a[40:43], v[88:91]\n"
    "ds_read_b128 v[188:191], v128 offset:17536\n"
    "v_fma_f32 v81, v81, s56, v241\n"
    "v_fma_f32 v82, v82, s56, v241\n"
    "v_mfma_f32_16x16x32_bf16 v[88:91], v[160:163], a[44:47], v[88:91]\n"
    "s_waitcnt lgkmcnt(3)\n"
    "ds_read_b128 v[192:195], v128 offset:17600\n"
    "buffer_load_dwordx4 v230, s[36:39], s52 offen lds\n"
    "v_mfma_f32_16x16x32_bf16 v[88:91], v[164:167], a[48:51], v[88:91]\n"
    "s_add_u32 s52, s52, 4096\n"
    "ds_read_b128 v[196:199], v128 offset:17664\n"
    "v_fma_f32 v83, v83, s56, v241\n"
    "v_fma_f32 v84, v84, s56, v241\n"
    "v_mfma_f32_16x16x32_bf16 v[88:91], v[168:171], a[52:55], v[88:91]\n"
    "ds_read_b128 v[200:203], v128 offset:17728\n"
    "v_exp_f32 v80, v80\n"
    "v_mfma_f32_16x16x32_bf16 v[88:91], v[172:175], a[56:59], v[88:91]\n"
    "ds_read_b128 v[204:207], v128 offset:17792\n"
    "global_load_dword v130, v129, s[20:21]\n"
    "v_mfma_f32_16x16x32_bf16 v[88:91], v[176:179], a[60:63], v[88:91]\n"
    "ds_read_b128 v[208:211], v128 offset:17856\n"
    "v_exp_f32 v81, v81\n"
    "v_mfma_f32_16x16x32_bf16 a[140:143], a[144:147], v[12:15], 0\n"
    "s_waitcnt lgkmcnt(4)\n"
    "v_exp_f32 v82, v82\n"
    "v_cvt_pk_bf16_f32 v4, v80, v81\n"
    "v_mfma_f32_16x16x32_bf16 v[92:95], v[180:183], a[32:35], 0\n"
    "s_add_u32 s61, s61, 1\n"
    "ds_read_b128 v[148:151], v128 offset:33792\n"
    "v_exp_f32 v83, v83\n"
    "v_mfma_f32_16x16x32_bf16 v[92:95], v[184:187], a[36:39], v[92:95]\n"
    "s_add_u32 s55, s54, s62\n"
    "ds_read_b128 v[152:155], v128 offset:33856\n"
    "v_cvt_pk_bf16_f32 v5, v82, v83\n"
    "v_fma_f32 v85, v85, s56, v241\n"
    "v_mfma_f32_16x16x32_bf16 v[92:95], v[188:191], a[40:43], v[92:95]\n"
    "s_add_u32 s54, s54, 16896\n"
    "ds_read_b128 v[156:159], v128 offset:33920\n"
    "v_fma_f32 v86, v86, s56, v241\n"
    "v_fma_f32 v87, v87, s56, v241\n"
    "v_mfma_f32_16x16x32_bf16 v[92:95], v[192:195], a[44:47], v[92:95]\n"
    "s_waitcnt lgkmcnt(3)\n"
    "ds_read_b128 v[160:163], v128 offset:33984\n"
    "v_exp_f32 v84, v84\n"
    "v_mfma_f32_16x16x32_bf16 v[92:95], v[196:199], a[48:51], v[92:95]\n"
    "s_sub_u32 s97, s54, 84480\n"
    "ds_read_b128 v[164:167], v128 offset:34048\n"
    "v_exp_f32 v85, v85\n"
    "v_mfma_f32_16x16x32_bf16 v[92:95], v[200:203], a[52:55], v[92:95]\n"
    "s_min_u32 s54, s54, s97\n"
    "ds_read_b128 v[168:171], v128 offset:34112\n"
    "v_exp_f32 v86, v86\n"
    "v_mfma_f32_16x16x32_bf16 v[92:95], v[204:207], a[56:59], v[92:95]\n"
    "s_sub_u32 s97, s54, s66\n"
    "ds_read_b128 v[172:175], v128 offset:34176\n"
    "v_exp_f32 v87, v87\n"
    "v_mfma_f32_16x16x32_bf16 v[92:95], v[208:211], a[60:63], v[92:95]\n"
    "s_waitcnt lgkmcnt(3)\n"
    "ds_read_b128 v[176:179], v128 offset:34240\n"
    "v_cvt_pk_bf16_f32 v6, v84, v85\n"
    "v_cvt_pk_bf16_f32 v7, v86, v87\n"
    "v_mfma_f32_16x16x32_bf16 v[80:83], v[148:151], a[32:35], 0\n"
    "s_mov_b32 s66, s54\n"
    "ds_read_b128 v[180:183], v128 offset:34304\n"
    "v_fma_f32 v88, v88, s56, v241\n"
    "v_fma_f32 v89, v89, s56, v241\n"
    "v_mfma_f32_16x16x32_bf16 v[80:83], v[152:155], a[36:39], v[80:83]\n"
    "s_add_u32 m0, s96, 0\n"
    "ds_read_b128 v[184:187], v128 offset:34368\n"
    "v_fma_f32 v90, v90, s56, v241\n"
    "v_fma_f32 v91, v91, s56, v241\n"
    "v_mfma_f32_16x16x32_bf16 v[80:83], v[156:159], a[40:43], v[80:83]\n"
    "ds_read_b128 v[188:191], v128 offset:34432\n"
    "v_exp_f32 v88, v88\n"
    "v_mfma_f32_16x16x32_bf16 v[80:83], v[160:163], a[44:47], v[80:83]\n"
    "s_waitcnt lgkmcnt(3)\n"
    "ds_read_b128 v[192:195], v128 offset:34496\n"
    "v_fma_f32 v92, v92, s56, v241\n"
    "v_fma_f32 v93, v93, s56, v241\n"
    "v_mfma_f32_16x16x32_bf16 v[80:83], v[164:167], a[48:51], v[80:83]\n"
    "ds_read_b128 v[196:199], v128 offset:34560\n"
    "v_fma_f32 v94, v94, s56, v241\n"
    "v_fma_f32 v95, v95, s56, v241\n"
    "v_mfma_f32_16x16x32_bf16 v[80:83], v[168:171], a[52:55], v[80:83]\n"
    "ds_read_b128 v[200:203], v128 offset:34624\n"
    "v_exp_f32 v89, v89\n"
    "v_mfma_f32_16x16x32_bf16 v[80:83], v[172:175], a[56:59], v[80:83]\n"
    "ds_read_b128 v[204:207], v128 offset:34688\n"
    "v_exp_f32 v90, v90\n"
    "v_mfma_f32_16x16x32_bf16 v[80:83], v[176:179], a[60:63], v[80:83]\n"
    "ds_read_b128 v[208:211], v128 offset:34752\n"
    "v_exp_f32 v91, v91\n"
    "v_mfma_f32_16x16x32_bf16 a[140:143], a[144:147], v[28:31], a[140:143]\n"
    "s_waitcnt lgkmcnt(4)\n"
    "v_exp_f32 v92, v92\n"
    "v_cvt_pk_bf16_f32 v20, v88, v89\n"
    "v_mfma_f32_16x16x32_bf16 v[220:223], v[180:183], a[32:35], 0\n"
    "ds_read_b128 v[148:151], v128 offset:0\n"
    "v_exp_f32 v93, v93\n"
    "v_mfma_f32_16x16x32_bf16 v[220:223], v[184:187], a[36:39], v[220:223]\n"
    "ds_read_b128 v[152:155], v128 offset:64\n"
    "v_exp_f32 v94, v94\n"
    "v_mfma_f32_16x16x32_bf16 v[220:223], v[188:191], a[40:43], v[220:223]\n"
    "ds_read_b128 v[156:159], v128 offset:128\n"
    "v_fma_f32 v80, v80, s56, v241\n"
    "v_fma_f32 v81, v81, s56, v241\n"
    "v_mfma_f32_16x16x32_bf16 v[220:223], v[192:195], a[44:47], v[220:223]\n"
    "s_waitcnt lgkmcnt(3)\n"
    "ds_read_b128 v[160:163], v128 offset:192\n"
    "v_fma_f32 v82, v82, s56, v241\n"
    "v_fma_f32 v83, v83, s56, v241\n"
    "v_mfma_f32_16x16x32_bf16 v[220:223], v[196:199], a[48:51], v[220:223]\n"
    "ds_read_b128 v[164:167], v128 offset:256\n"
    "v_exp_f32 v95, v95\n"
    "v_mfma_f32_16x16x32_bf16 v[220:223], v[200:203], a[52:55], v[220:223]\n"
    "ds_read_b128 v[168:171], v128 offset:320\n"
    "v_exp_f32 v80, v80\n"
    "v_mfma_f32_16x16x32_bf16 v[220:223], v[204:207], a[56:59], v[220:223]\n"
    "ds_read_b128 v[172:175], v128 offset:384\n"
    "v_exp_f32 v81, v81\n"
    "v_mfma_f32_16x16x32_bf16 v[220:223], v[208:211], a[60:63], v[220:223]\n"
    "ds_read_b128 v[176:179], v128 offset:448\n"
    "v_exp_f32 v82, v82\n"
    "v_exp_f32 v83, v83\n"
    "v_cvt_pk_bf16_f32 v21, v90, v91\n"
    "v_cvt_pk_bf16_f32 v22, v92, v93\n"
    "v_cvt_pk_bf16_f32 v23, v94, v95\n"
    "v_cvt_pk_bf16_f32 v32, v80, v81\n"
    "v_cvt_pk_bf16_f32 v33, v82, v83\n"
    "buffer_load_dwordx4 v230, s[36:39], s52 offen lds\n"
    "s_add_u32 s52, s52, 4096\n"
    "s_add_u32 m0, s96, 4224\n"
    "s_add_u32 s58, s72, 16384\n"
    "buffer_load_dwordx4 v230, s[36:39], s52 offen lds\n"
    "v_add_u32 v233, s97, v233\n"
    "s_add_u32 s52, s52, 4096\n"
    "s_mov_b32 s46, s73\n"
    "s_add_u32 s61, s61, 1\n"
    "s_add_u32 m0, s96, 8448\n"
    "s_nop 0\n"
    "buffer_load_dwordx4 v230, s[36:39], s52 offen lds\n"
    "s_add_u32 s52, s52, 4096\n"
    "s_add_u32 s53, s70, s64\n"
    "s_add_u32 m0, s96, 12672\n"
    "s_add_u32 s53, s53, s33\n"
    "buffer_load_dwordx4 v230, s[36:39], s52 offen lds\n"
    "s_add_u32 s52, s52, 4096\n"
    "s_add_u32 m0, s55, 0\n"
    "s_waitcnt lgkmcnt(4)\n"
    "s_waitcnt vmcnt(9)\n"
    "ds_read_b128 a[0:3], v236 offset:0\n"
    "ds_read_b128 a[4:7], v236 offset:64\n"
    "ds_read_b128 a[8:11], v236 offset:128\n"
    "ds_read_b128 a[12:15], v236 offset:192\n"
    "ds_read_b128 a[16:19], v236 offset:256\n"
    "ds_read_b128 a[20:23], v236 offset:320\n"
    "ds_read_b128 a[24:27], v236 offset:384\n"
    "ds_read_b128 a[28:31], v236 offset:448\n"
    "s_waitcnt lgkmcnt(0)\n"
    "v_mfma_f32_16x16x32_bf16 v[88:91], v[148:151], a[0:3], 0\n"
    "ds_read_b128 v[180:183], v128 offset:512\n"
    "v_mfma_f32_16x16x32_bf16 v[88:91], v[152:155], a[4:7], v[88:91]\n"
    "ds_read_b128 v[184:187], v128 offset:576\n"
    "v_mfma_f32_16x16x32_bf16 v[88:91], v[156:159], a[8:11], v[88:91]\n"
    "ds_read_b128 v[188:191], v128 offset:640\n"
    "v_mfma_f32_16x16x32_bf16 v[88:91], v[160:163], a[12:15], v[88:91]\n"
    "s_waitcnt lgkmcnt(11)\n"
    "ds_read_b128 v[192:195], v128 offset:704\n"
    "v_mfma_f32_16x16x32_bf16 v[88:91], v[164:167], a[16:19], v[88:91]\n"
    "ds_read_b128 v[196:199], v128 offset:768\n"
    "v_mfma_f32_16x16x32_bf16 v[88:91], v[168:171], a[20:23], v[88:91]\n"
    "ds_read_b128 v[200:203], v128 offset:832\n"
    "v_mfma_f32_16x16x32_bf16 v[88:91], v[172:175], a[24:27], v[88:91]\n"
    "ds_read_b128 v[204:207], v128 offset:896\n"
    "v_mfma_f32_16x16x32_bf16 v[88:91], v[176:179], a[28:31], v[88:91]\n"
    "s_waitcnt lgkmcnt(3)\n"
    "ds_read_b128 v[208:211], v128 offset:960\n"
    "v_mfma_f32_16x16x32_bf16 v[92:95], v[180:183], a[0:3], 0\n"
    "ds_read_b128 v[148:151], v128 offset:16896\n"
    "v_mfma_f32_16x16x32_bf16 v[92:95], v[184:187], a[4:7], v[92:95]\n"
    "ds_read_b128 v[152:155], v128 offset:16960\n"
    "v_mfma_f32_16x16x32_bf16 v[92:95], v[188:191], a[8:11], v[92:95]\n"
    "ds_read_b128 v[156:159], v128 offset:17024\n"
    "v_mfma_f32_16x16x32_bf16 v[92:95], v[192:195], a[12:15], v[92:95]\n"
    "s_waitcnt lgkmcnt(3)\n"
    "ds_read_b128 v[160:163], v128 offset:17088\n"
    "v_max3_f32 v104, v88, v89, v90\n"
    "v_max_f32 v104, v104, v91\n"
    "v_mfma_f32_16x16x32_bf16 v[92:95], v[196:199], a[16:19], v[92:95]\n"
    "ds_read_b128 v[164:167], v128 offset:17152\n"
    "v_mov_b32 v105, v104\n"
    "v_mfma_f32_16x16x32_bf16 v[92:95], v[200:203], a[20:23], v[92:95]\n"
    "ds_read_b128 v[168:171], v128 offset:17216\n"
    "v_permlane16_swap_b32 v104, v105\n"
    "v_max_f32 v104, v104, v105\n"
    "v_mfma_f32_16x16x32_bf16 v[92:95], v[204:207], a[24:27], v[92:95]\n"
    "ds_read_b128 v[172:175], v128 offset:17280\n"
    "v_mov_b32 v105, v104\n"
    "v_mfma_f32_16x16x32_bf16 v[92:95], v[208:211], a[28:31], v[92:95]\n"
    "ds_read_b128 v[176:179], v128 offset:17344\n"
    "v_permlane32_swap_b32 v104, v105\n"
    "v_max_f32 v104, v104, v105\n"
    "v_mfma_f32_16x16x32_bf16 a[132:135], a[144:147], v[4:7], 0\n"
    "s_waitcnt lgkmcnt(4)\n"
    "v_mul_f32 v240, s83, v104\n"
    "v_fma_f32 v88, v88, s56, v240\n"
    "v_fma_f32 v89, v89, s56, v240\n"
    "v_mfma_f32_16x16x32_bf16 v[80:83], v[148:151], a[0:3], 0\n"
    "ds_read_b128 v[180:183], v128 offset:17408\n"
    "v_fma_f32 v90, v90, s56, v240\n"
    "v_fma_f32 v91, v91, s56, v240\n"
    "v_mfma_f32_16x16x32_bf16 v[80:83], v[152:155], a[4:7], v[80:83]\n"
    "ds_read_b128 v[184:187], v128 offset:17472\n"
    "v_exp_f32 v88, v88\n"
    "v_mfma_f32_16x16x32_bf16 v[80:83], v[156:159], a[8:11], v[80:83]\n"
    "ds_read_b128 v[188:191], v128 offset:17536\n"
    "v_exp_f32 v89, v89\n"
    "v_mfma_f32_16x16x32_bf16 v[80:83], v[160:163], a[12:15], v[80:83]\n"
    "s_waitcnt lgkmcnt(3)\n"
    "ds_read_b128 v[192:195], v128 offset:17600\n"
    "v_exp_f32 v90, v90\n"
    "v_mfma_f32_16x16x32_bf16 v[80:83], v[164:167], a[16:19], v[80:83]\n"
    "ds_read_b128 v[196:199], v128 offset:17664\n"
    "v_exp_f32 v91, v91\n"
    "v_mfma_f32_16x16x32_bf16 v[80:83], v[168:171], a[20:23], v[80:83]\n"
    "ds_read_b128 v[200:203], v128 offset:17728\n"
    "v_cvt_pk_bf16_f32 v0, v88, v89\n"
    "v_cvt_pk_bf16_f32 v1, v90, v91\n"
    "v_mfma_f32_16x16x32_bf16 v[80:83], v[172:175], a[24:27], v[80:83]\n"
    "ds_read_b128 v[204:207], v128 offset:17792\n"
    "v_fma_f32 v92, v92, s56, v240\n"
    "v_fma_f32 v93, v93, s56, v240\n"
    "v_mfma_f32_16x16x32_bf16 v[80:83], v[176:179], a[28:31], v[80:83]\n"
    "s_waitcnt lgkmcnt(3)\n"
    "ds_read_b128 v[208:211], v128 offset:17856\n"
    "v_fma_f32 v94, v94, s56, v240\n"
    "v_fma_f32 v95, v95, s56, v240\n"
    "v_mfma_f32_16x16x32_bf16 v[84:87], v[180:183], a[0:3], 0\n"
    "ds_read_b128 v[148:151], v128 offset:33792\n"
    "v_exp_f32 v92, v92\n"
    "v_mfma_f32_16x16x32_bf16 v[84:87], v[184:187], a[4:7], v[84:87]\n"
    "ds_read_b128 v[152:155], v128 offset:33856\n"
    "v_exp_f32 v93, v93\n"
    "v_mfma_f32_16x16x32_bf16 v[84:87], v[188:191], a[8:11], v[84:87]\n"
    "ds_read_b128 v[156:159], v128 offset:33920\n"
    "v_exp_f32 v94, v94\n"
    "v_mfma_f32_16x16x32_bf16 v[84:87], v[192:195], a[12:15], v[84:87]\n"
    "s_waitcnt lgkmcnt(3)\n"
    "ds_read_b128 v[160:163], v128 offset:33984\n"
    "v_exp_f32 v95, v95\n"
    "v_mfma_f32_16x16x32_bf16 v[84:87], v[196:199], a[16:19], v[84:87]\n"
    "s_waitcnt vmcnt(5)\n"
    "ds_read_b128 v[164:167], v128 offset:34048\n"
    "v_cvt_pk_bf16_f32 v2, v92, v93\n"
    "v_cvt_pk_bf16_f32 v3, v94, v95\n"
    "v_mfma_f32_16x16x32_bf16 v[84:87], v[200:203], a[20:23], v[84:87]\n"
    "s_barrier\n"
    "ds_read_b128 v[168:171], v128 offset:34112\n"
    "v_fma_f32 v80, v80, s56, v240\n"
    "v_fma_f32 v81, v81, s56, v240\n"
    "v_mfma_f32_16x16x32_bf16 v[84:87], v[204:207], a[24:27], v[84:87]\n"
    "ds_read_b128 v[172:175], v128 offset:34176\n"
    "buffer_load_dwordx4 v230, s[36:39], s52 offen lds\n"
    "s_add_u32 s52, s52, 4096\n"
    "v_mfma_f32_16x16x32_bf16 v[84:87], v[208:211], a[28:31], v[84:87]\n"
    "s_waitcnt lgkmcnt(3)\n"
    "ds_read_b128 v[176:179], v128 offset:34240\n"
    "v_fma_f32 v82, v82, s56, v240\n"
    "v_fma_f32 v83, v83, s56, v240\n"
    "v_mfma_f32_16x16x32_bf16 v[88:91], v[148:151], a[0:3], 0\n"
    "s_add_u32 m0, s55, 4224\n"
    "ds_read_b128 v[180:183], v128 offset:34304\n"
    "v_exp_f32 v80, v80\n"
    "v_mfma_f32_16x16x32_bf16 v[88:91], v[152:155], a[4:7], v[88:91]\n"
    "ds_read_b128 v[184:187], v128 offset:34368\n"
    "buffer_load_dwordx4 v230, s[36:39], s52 offen lds\n"
    "s_add_u32 s52, s52, 4096\n"
    "v_mfma_f32_16x16x32_bf16 v[88:91], v[156:159], a[8:11], v[88:91]\n"
    "s_add_u32 m0, s55, 8448\n"
    "ds_read_b128 v[188:191], v128 offset:34432\n"
    "v_exp_f32 v81, v81\n"
    "v_mfma_f32_16x16x32_bf16 v[88:91], v[160:163], a[12:15], v[88:91]\n"
    "s_waitcnt lgkmcnt(3)\n"
    "ds_read_b128 v[192:195], v128 offset:34496\n"
    "buffer_load_dwordx4 v230, s[36:39], s52 offen lds\n"
    "v_mfma_f32_16x16x32_bf16 v[88:91], v[164:167], a[16:19], v[88:91]\n"
    "s_add_u32 s52, s52, 4096\n"
    "ds_read_b128 v[196:199], v128 offset:34560\n"
    "v_fma_f32 v84, v84, s56, v240\n"
    "v_fma_f32 v85, v85, s56, v240\n"
    "v_mfma_f32_16x16x32_bf16 v[88:91], v[168:171], a[20:23], v[88:91]\n"
    "s_add_u32 s96, s54, s62\n"
    "ds_read_b128 v[200:203], v128 offset:34624\n"
    "v_fma_f32 v86, v86, s56, v240\n"
    "v_fma_f32 v87, v87, s56, v240\n"
    "v_mfma_f32_16x16x32_bf16 v[88:91], v[172:175], a[24:27], v[88:91]\n"
    "s_add_u32 s54, s54, 16896\n"
    "ds_read_b128 v[204:207], v128 offset:34688\n"
    "v_exp_f32 v82, v82\n"
    "v_mfma_f32_16x16x32_bf16 v[88:91], v[176:179], a[28:31], v[88:91]\n"
    "s_waitcnt lgkmcnt(3)\n"
    "ds_read_b128 v[208:211], v128 offset:34752\n"
    "v_exp_f32 v83, v83\n"
    "v_mfma_f32_16x16x32_bf16 v[224:227], v[180:183], a[0:3], 0\n"
    "s_add_u32 m0, s55, 12672\n"
    "ds_read_b128 v[148:151], v128 offset:50688\n"
    "v_exp_f32 v84, v84\n"
    "v_mfma_f32_16x16x32_bf16 v[224:227], v[184:187], a[4:7], v[224:227]\n"
    "s_sub_u32 s97, s54, 84480\n"
    "ds_read_b128 v[152:155], v128 offset:50752\n"
    "v_exp_f32 v85, v85\n"
    "v_mfma_f32_16x16x32_bf16 v[224:227], v[188:191], a[8:11], v[224:227]\n"
    "ds_read_b128 v[156:159], v128 offset:50816\n"
    "buffer_load_dwordx4 v230, s[36:39], s52 offen lds\n"
    "s_add_u32 s52, s52, 4096\n"
    "v_mfma_f32_16x16x32_bf16 v[224:227], v[192:195], a[12:15], v[224:227]\n"
    "s_waitcnt lgkmcnt(3)\n"
    "ds_read_b128 v[160:163], v128 offset:50880\n"
    "v_fma_f32 v88, v88, s56, v240\n"
    "v_fma_f32 v89, v89, s56, v240\n"
    "v_mfma_f32_16x16x32_bf16 v[224:227], v[196:199], a[16:19], v[224:227]\n"
    "s_min_u32 s54, s54, s97\n"
    "ds_read_b128 v[164:167], v128 offset:50944\n"
    "v_fma_f32 v90, v90, s56, v240\n"
    "v_fma_f32 v91, v91, s56, v240\n"
    "v_mfma_f32_16x16x32_bf16 v[224:227], v[200:203], a[20:23], v[224:227]\n"
    "s_sub_u32 s97, s54, s66\n"
    "ds_read_b128 v[168:171], v128 offset:51008\n"
    "v_exp_f32 v86, v86\n"
    "v_mfma_f32_16x16x32_bf16 v[224:227], v[204:207], a[24:27], v[224:227]\n"
    "s_mov_b32 s66, s54\n"
    "ds_read_b128 v[172:175], v128 offset:51072\n"
    "v_add_u32 v233, s97, v233\n"
    "s_add_u32 s61, s61, 1\n"
    "s_add_u32 m0, s96, 0\n"
    "v_mfma_f32_16x16x32_bf16 v[224:227], v[208:211], a[28:31], v[224:227]\n"
    "ds_read_b128 v[176:179], v128 offset:51136\n"
    "buffer_load_dwordx4 v230, s[36:39], s52 offen lds\n"
    "s_add_u32 s52, s52, 4096\n"
    "s_add_u32 m0, s96, 4224\n"
    "v_mfma_f32_16x16x32_bf16 a[128:131], a[144:147], v[0:3], 0\n"
    "buffer_load_dwordx4 v230, s[36:39], s52 offen lds\n"
    "s_add_u32 s52, s52, 4096\n"
    "s_add_u32 m0, s96, 8448\n"
    "s_nop 0\n"
    "buffer_load_dwordx4 v230, s[36:39], s52 offen lds\n"
    "v_exp_f32 v87, v87\n"
    "v_exp_f32 v88, v88\n"
    "v_exp_f32 v89, v89\n"
    "v_exp_f32 v90, v90\n"
    "v_exp_f32 v91, v91\n"
    "s_add_u32 s52, s52, 4096\n"
    "s_add_u32 m0, s96, 12672\n"
    "s_nop 0\n"
    "buffer_load_dwordx4 v230, s[36:39], s52 offen lds\n"
    "v_cvt_pk_bf16_f32 v16, v80, v81\n"
    "v_cvt_pk_bf16_f32 v17, v82, v83\n"
    "v_cvt_pk_bf16_f32 v18, v84, v85\n"
    "v_cvt_pk_bf16_f32 v19, v86, v87\n"
    "v_cvt_pk_bf16_f32 v106, v88, v89\n"
    "v_accvgpr_write_b32 a148, v106\n"
    "v_cvt_pk_bf16_f32 v107, v90, v91\n"
    "v_accvgpr_write_b32 a149, v107\n"
    "s_add_u32 s52, s52, 4096\n"
    "s_add_u32 s55, s54, s62\n"
    "s_add_u32 s54, s54, 16896\n"
    "s_sub_u32 s97, s54, 84480\n"
    "s_min_u32 s54, s54, s97\n"
    "s_sub_u32 s97, s54, s66\n"
    "s_mov_b32 s66, s54\n"
    "v_add_u32 v233, s97, v233\n"
    "s_waitcnt vmcnt(8) lgkmcnt(0)\n"
    "ds_inc_u32 v228, v236\n"
    "s_branch L_ent%=\n"
    ".p2align 6\n"
    "L_blk%=:\n"
    "v_mfma_f32_16x16x32_bf16 v[212:215], v[148:151], a[64:67], 0\n"
    "s_add_u32 s61, s61, 1\n"
    "ds_read_b128 a[0:3], v236 offset:0\n"
    "v_mul_f32 v196, v196, v244\n"
    "v_mfma_f32_16x16x32_bf16 v[212:215], v[152:155], a[68:71], v[212:215]\n"
    "ds_read_b128 a[4:7], v236 offset:64\n"
    "v_mul_f32 v197, v197, v244\n"
    "v_mfma_f32_16x16x32_bf16 v[212:215], v[156:159], a[72:75], v[212:215]\n"
    "ds_read_b32 v229, v230\n"
    "v_mul_f32 v198, v198, v244\n"
    "v_mfma_f32_16x16x32_bf16 v[212:215], v[160:163], a[76:79], v[212:215]\n"
    "ds_read_b128 a[8:11], v236 offset:128\n"
    "v_mul_f32 v199, v199, v244\n"
    "v_mfma_f32_16x16x32_bf16 v[212:215], v[164:167], a[80:83], v[212:215]\n"
    "ds_read_b128 a[12:15], v236 offset:192\n"
    "v_cvt_pk_bf16_f32 v196, v196, v197\n"
    "v_mfma_f32_16x16x32_bf16 v[212:215], v[168:171], a[84:87], v[212:215]\n"
    "ds_read_b128 a[16:19], v236 offset:256\n"
    "v_cvt_pk_bf16_f32 v197, v198, v199\n"
    "v_mfma_f32_16x16x32_bf16 v[212:215], v[172:175], a[88:91], v[212:215]\n"
    "ds_write_b64 v237, v[196:197] offset:1728\n"
    "v_mfma_f32_16x16x32_bf16 v[212:215], v[176:179], a[92:95], v[212:215]\n"
    "ds_read_b128 a[20:23], v236 offset:320\n"
    "v_mul_f32 v200, v200, v245\n"
    "v_mfma_f32_16x16x32_bf16 v[216:219], v[148:151], a[96:99], 0\n"
    "s_waitcnt lgkmcnt(5)\n"
    "v_cmp_gt_u32 vcc, s61, v229\n"
    "v_mul_f32 v201, v201, v245\n"
    "v_mfma_f32_16x16x32_bf16 v[216:219], v[152:155], a[100:103], v[216:219]\n"
    "v_mul_f32 v202, v202, v245\n"
    "v_mul_f32 v203, v203, v245\n"
    "v_mfma_f32_16x16x32_bf16 v[216:219], v[156:159], a[104:107], v[216:219]\n"
    "v_cvt_pk_bf16_f32 v200, v200, v201\n"
    "v_cvt_pk_bf16_f32 v201, v202, v203\n"
    "v_mfma_f32_16x16x32_bf16 v[216:219], v[160:163], a[108:111], v[216:219]\n"
    "s_cbranch_vccnz L_sp31%=\n"
    "L_rt31%=:\n"
    "ds_read_b128 a[24:27], v236 offset:384\n"
    "v_mfma_f32_16x16x32_bf16 v[216:219], v[164:167], a[112:115], v[216:219]\n"
    "s_add_u32 s55, s54, s62\n"
    "ds_write_b64 v237, v[200:201] offset:4032\n"
    "v_mfma_f32_16x16x32_bf16 v[216:219], v[168:171], a[116:119], v[216:219]\n"
    "s_add_u32 s54, s54, 16896\n"
    "ds_read_b128 v[180:183], v233 offset:512\n"
    "v_mul_f32 v204, v204, v246\n"
    "v_mfma_f32_16x16x32_bf16 v[216:219], v[172:175], a[120:123], v[216:219]\n"
    "s_sub_u32 s97, s54, 84480\n"
    "v_mul_f32 v205, v205, v246\n"
    "v_mul_f32 v206, v206, v246\n"
    "v_mfma_f32_16x16x32_bf16 v[216:219], v[176:179], a[124:127], v[216:219]\n"
    "s_min_u32 s54, s54, s97\n"
    "v_mul_f32 v207, v207, v246\n"
    "v_cvt_pk_bf16_f32 v204, v204, v205\n"
    "v_mfma_f32_16x16x32_bf16 v[220:223], v[148:151], a[32:35], 0\n"
    "s_sub_u32 s97, s54, s66\n"
    "ds_read_b128 v[184:187], v233 offset:576\n"
    "v_cvt_pk_bf16_f32 v205, v206, v207\n"
    "v_mfma_f32_16x16x32_bf16 v[220:223], v[152:155], a[36:39], v[220:223]\n"
    "s_mov_b32 s66, s54\n"
    "ds_read_b128 a[28:31], v236 offset:448\n"
    "v_mul_f32 v208, v208, v247\n"
    "v_mfma_f32_16x16x32_bf16 v[220:223], v[156:159], a[40:43], v[220:223]\n"
    "ds_write_b64 v237, v[204:205] offset:6336\n"
    "ds_read_b128 v[188:191], v233 offset:640\n"
    "v_mfma_f32_16x16x32_bf16 v[220:223], v[160:163], a[44:47], v[220:223]\n"
    "v_mul_f32 v209, v209, v247\n"
    "v_mul_f32 v210, v210, v247\n"
    "v_mul_f32 v211, v211, v247\n"
    "v_cvt_pk_bf16_f32 v208, v208, v209\n"
    "v_cvt_pk_bf16_f32 v209, v210, v211\n"
    "v_mfma_f32_16x16x32_bf16 v[220:223], v[164:167], a[48:51], v[220:223]\n"
    "ds_write_b64 v237, v[208:209] offset:8640\n"
    "ds_read_b128 v[192:195], v233 offset:704\n"
    "v_mfma_f32_16x16x32_bf16 v[220:223], v[168:171], a[52:55], v[220:223]\n"
    "ds_read_b128 v[196:199], v233 offset:768\n"
    "v_max3_f32 v248, v212, v213, v214\n"
    "v_mfma_f32_16x16x32_bf16 v[220:223], v[172:175], a[56:59], v[220:223]\n"
    "ds_read_b128 v[200:203], v233 offset:832\n"
    "v_max_f32 v248, v248, v215\n"
    "v_mfma_f32_16x16x32_bf16 v[220:223], v[176:179], a[60:63], v[220:223]\n"
    "s_add_u32 m0, s96, 0\n"
    "ds_read_b128 v[204:207], v233 offset:896\n"
    "v_mov_b32 v249, v248\n"
    "v_mfma_f32_16x16x32_bf16 v[224:227], v[148:151], a[0:3], 0\n"
    "s_waitcnt lgkmcnt(7)\n"
    "buffer_load_dwordx4 v230, s[36:39], s52 offen lds\n"
    "v_mfma_f32_16x16x32_bf16 v[224:227], v[152:155], a[4:7], v[224:227]\n"
    "s_add_u32 s52, s52, 4096\n"
    "ds_read_b128 v[208:211], v233 offset:960\n"
    "v_permlane16_swap_b32 v248, v249\n"
    "v_mfma_f32_16x16x32_bf16 v[224:227], v[156:159], a[8:11], v[224:227]\n"
    "v_max_f32 v248, v248, v249\n"
    "v_mov_b32 v249, v248\n"
    "v_mfma_f32_16x16x32_bf16 v[224:227], v[160:163], a[12:15], v[224:227]\n"
    "s_nop 0\n"
    "v_permlane32_swap_b32 v248, v249\n"
    "v_max_f32 v248, v248, v249\n"
    "v_mfma_f32_16x16x32_bf16 v[224:227], v[164:167], a[16:19], v[224:227]\n"
    "v_mul_f32 v242, s83, v248\n"
    "v_add_f32 v242, s71, v242\n"
    "v_fma_f32 v212, v212, s56, v242\n"
    "v_fma_f32 v213, v213, s56, v242\n"
    "v_fma_f32 v214, v214, s56, v242\n"
    "v_fma_f32 v215, v215, s56, v242\n"
    "v_mfma_f32_16x16x32_bf16 v[224:227], v[168:171], a[20:23], v[224:227]\n"
    "s_add_u32 m0, s96, 4224\n"
    "v_exp_f32 v212, v212\n"
    "v_exp_f32 v213, v213\n"
    "v_exp_f32 v214, v214\n"
    "v_exp_f32 v215, v215\n"
    "v_mfma_f32_16x16x32_bf16 v[224:227], v[172:175], a[24:27], v[224:227]\n"
    "s_add_u32 s58, s72, 16384\n"
    "buffer_load_dwordx4 v230, s[36:39], s52 offen lds\n"
    "v_mfma_f32_16x16x32_bf16 v[224:227], v[176:179], a[28:31], v[224:227]\n"
    "s_waitcnt vmcnt(8) lgkmcnt(0)\n"
    "v_cvt_pk_bf16_f32 v8, v212, v213\n"
    "v_cvt_pk_bf16_f32 v9, v214, v215\n"
    "v_add_u32 v233, s97, v233\n"
    "v_mfma_f32_16x16x32_bf16 v[212:215], v[180:183], a[64:67], 0\n"
    "s_add_u32 s52, s52, 4096\n"
    "ds_inc_u32 v228, v236\n"
    "v_mfma_f32_16x16x32_bf16 v[212:215], v[184:187], a[68:71], v[212:215]\n"
    "s_mov_b32 s46, s73\n"
    "v_max3_f32 v250, v216, v217, v218\n"
    "v_max_f32 v250, v250, v219\n"
    "v_mfma_f32_16x16x32_bf16 v[212:215], v[188:191], a[72:75], v[212:215]\n"
    "s_add_u32 s61, s61, 1\n"
    "ds_read_b128 v[148:151], v233 offset:0\n"
    "v_mov_b32 v251, v250\n"
    "v_mfma_f32_16x16x32_bf16 v[212:215], v[192:195], a[76:79], v[212:215]\n"
    "s_add_u32 m0, s96, 8448\n"
    "v_permlane16_swap_b32 v250, v251\n"
    "v_max_f32 v250, v250, v251\n"
    "v_mov_b32 v251, v250\n"
    "v_mfma_f32_16x16x32_bf16 v[212:215], v[196:199], a[80:83], v[212:215]\n"
    "buffer_load_dwordx4 v230, s[36:39], s52 offen lds\n"
    "v_mfma_f32_16x16x32_bf16 v[212:215], v[200:203], a[84:87], v[212:215]\n"
    "v_permlane32_swap_b32 v250, v251\n"
    "v_max_f32 v250, v250, v251\n"
    "v_mul_f32 v243, s83, v250\n"
    "v_add_f32 v243, s71, v243\n"
    "v_fma_f32 v216, v216, s56, v243\n"
    "v_fma_f32 v217, v217, s56, v243\n"
    "v_fma_f32 v218, v218, s56, v243\n"
    "v_fma_f32 v219, v219, s56, v243\n"
    "v_mfma_f32_16x16x32_bf16 v[212:215], v[204:207], a[88:91], v[212:215]\n"
    "ds_read_b128 v[152:155], v233 offset:64\n"
    "v_exp_f32 v216, v216\n"
    "v_exp_f32 v217, v217\n"
    "v_exp_f32 v218, v218\n"
    "v_exp_f32 v219, v219\n"
    "v_mfma_f32_16x16x32_bf16 v[212:215], v[208:211], a[92:95], v[212:215]\n"
    "s_add_u32 s52, s52, 4096\n"
    "v_cvt_pk_bf16_f32 v12, v216, v217\n"
    "v_cvt_pk_bf16_f32 v13, v218, v219\n"
    "v_mfma_f32_16x16x32_bf16 v[216:219], v[180:183], a[96:99], 0\n"
    "s_add_u32 s53, s70, s64\n"
    "ds_read_b128 v[156:159], v233 offset:128\n"
    "v_max3_f32 v248, v220, v221, v222\n"
    "v_mfma_f32_16x16x32_bf16 v[216:219], v[184:187], a[100:103], v[216:219]\n"
    "s_add_u32 m0, s96, 12672\n"
    "ds_read_b32 v229, v230\n"
    "v_max_f32 v248, v248, v223\n"
    "v_mfma_f32_16x16x32_bf16 v[216:219], v[188:191], a[104:107], v[216:219]\n"
    "s_add_u32 s53, s53, s33\n"
    "buffer_load_dwordx4 v230, s[36:39], s52 offen lds\n"
    "v_mfma_f32_16x16x32_bf16 v[216:219], v[192:195], a[108:111], v[216:219]\n"
    "s_add_u32 s52, s52, 4096\n"
    "ds_read_b128 v[160:163], v233 offset:192\n"
    "v_mov_b32 v249, v248\n"
    "v_mfma_f32_16x16x32_bf16 v[216:219], v[196:199], a[112:115], v[216:219]\n"
    "s_nop 0\n"
    "v_permlane16_swap_b32 v248, v249\n"
    "v_max_f32 v248, v248, v249\n"
    "v_mov_b32 v249, v248\n"
    "v_mfma_f32_16x16x32_bf16 v[216:219], v[200:203], a[116:119], v[216:219]\n"
    "s_waitcnt lgkmcnt(1)\n"
    "v_permlane32_swap_b32 v248, v249\n"
    "v_max_f32 v248, v248, v249\n"
    "v_mul_f32 v241, s83, v248\n"
    "v_fma_f32 v220, v220, s56, v241\n"
    "v_fma_f32 v221, v221, s56, v241\n"
    "v_fma_f32 v222, v222, s56, v241\n"
    "v_fma_f32 v223, v223, s56, v241\n"
    "v_cmp_gt_u32 vcc, s61, v229\n"
    "v_mfma_f32_16x16x32_bf16 v[216:219], v[204:207], a[120:123], v[216:219]\n"
    "v_exp_f32 v220, v220\n"
    "v_exp_f32 v221, v221\n"
    "v_exp_f32 v222, v222\n"
    "v_exp_f32 v223, v223\n"
    "v_mfma_f32_16x16x32_bf16 v[216:219], v[208:211], a[124:127], v[216:219]\n"
    "ds_read_b128 v[164:167], v233 offset:256\n"
    "v_cvt_pk_bf16_f32 v4, v220, v221\n"
    "v_cvt_pk_bf16_f32 v5, v222, v223\n"
    "v_mfma_f32_16x16x32_bf16 v[220:223], v[180:183], a[32:35], 0\n"
    "s_cbranch_vccnz L_sp0%=\n"
    "L_rt0%=:\n"
    "ds_read_b128 v[168:171], v233 offset:320\n"
    "v_mfma_f32_16x16x32_bf16 v[220:223], v[184:187], a[36:39], v[220:223]\n"
    "s_add_u32 m0, s55, 0\n"
    "v_max3_f32 v250, v224, v225, v226\n"
    "v_max_f32 v250, v250, v227\n"
    "v_mfma_f32_16x16x32_bf16 v[220:223], v[188:191], a[40:43], v[220:223]\n"
    "buffer_load_dwordx4 v230, s[36:39], s52 offen lds\n"
    "v_mfma_f32_16x16x32_bf16 v[220:223], v[192:195], a[44:47], v[220:223]\n"
    "s_add_u32 s52, s52, 4096\n"
    "ds_read_b128 v[172:175], v233 offset:384\n"
    "v_mov_b32 v251, v250\n"
    "v_mfma_f32_16x16x32_bf16 v[220:223], v[196:199], a[48:51], v[220:223]\n"
    "s_nop 0\n"
    "v_permlane16_swap_b32 v250, v251\n"
    "v_max_f32 v250, v250, v251\n"
    "v_mov_b32 v251, v250\n"
    "v_mfma_f32_16x16x32_bf16 v[220:223], v[200:203], a[52:55], v[220:223]\n"
    "s_nop 0\n"
    "v_permlane32_swap_b32 v250, v251\n"
    "v_max_f32 v250, v250, v251\n"
    "v_mul_f32 v240, s83, v250\n"
    "v_fma_f32 v224, v224, s56, v240\n"
    "v_fma_f32 v225, v225, s56, v240\n"
    "v_fma_f32 v226, v226, s56, v240\n"
    "v_fma_f32 v227, v227, s56, v240\n"
    "v_mfma_f32_16x16x32_bf16 v[220:223], v[204:207], a[56:59], v[220:223]\n"
    "v_exp_f32 v224, v224\n"
    "v_exp_f32 v225, v225\n"
    "v_exp_f32 v226, v226\n"
    "v_exp_f32 v227, v227\n"
    "v_mfma_f32_16x16x32_bf16 v[220:223], v[208:211], a[60:63], v[220:223]\n"
    "v_cvt_pk_bf16_f32 v0, v224, v225\n"
    "v_cvt_pk_bf16_f32 v1, v226, v227\n"
    "v_mfma_f32_16x16x32_bf16 v[224:227], v[180:183], a[0:3], 0\n"
    "ds_read_b128 v[176:179], v233 offset:448\n"
    "v_fma_f32 v212, v212, s56, v242\n"
    "v_mfma_f32_16x16x32_bf16 v[224:227], v[184:187], a[4:7], v[224:227]\n"
    "s_add_u32 m0, s55, 4224\n"
    "v_fma_f32 v213, v213, s56, v242\n"
    "v_fma_f32 v214, v214, s56, v242\n"
    "v_mfma_f32_16x16x32_bf16 v[224:227], v[188:191], a[8:11], v[224:227]\n"
    "buffer_load_dwordx4 v230, s[36:39], s52 offen lds\n"
    "v_mfma_f32_16x16x32_bf16 v[224:227], v[192:195], a[12:15], v[224:227]\n"
    "s_add_u32 s52, s52, 4096\n"
    "ds_read_b64 v[252:253], v238 offset:0\n"
    "v_fma_f32 v215, v215, s56, v242\n"
    "v_mfma_f32_16x16x32_bf16 v[224:227], v[196:199], a[16:19], v[224:227]\n"
    "v_exp_f32 v212, v212\n"
    "v_mfma_f32_16x16x32_bf16 v[224:227], v[200:203], a[20:23], v[224:227]\n"
    "v_exp_f32 v213, v213\n"
    "v_mfma_f32_16x16x32_bf16 v[224:227], v[204:207], a[24:27], v[224:227]\n"
    "v_exp_f32 v214, v214\n"
    "v_exp_f32 v215, v215\n"
    "v_mfma_f32_16x16x32_bf16 v[224:227], v[208:211], a[28:31], v[224:227]\n"
    "s_waitcnt lgkmcnt(1)\n"
    "v_cvt_pk_bf16_f32 v10, v212, v213\n"
    "v_cvt_pk_bf16_f32 v11, v214, v215\n"
    "v_mfma_f32_16x16x32_bf16 v[212:215], v[148:151], a[64:67], 0\n"
    "ds_read_b128 v[180:183], v233 offset:512\n"
    "v_fma_f32 v216, v216, s56, v243\n"
    "v_mfma_f32_16x16x32_bf16 v[212:215], v[152:155], a[68:71], v[212:215]\n"
    "s_add_u32 m0, s55, 8448\n"
    "ds_read_b64 v[254:255], v238 offset:128\n"
    "v_fma_f32 v217, v217, s56, v243\n"
    "v_mfma_f32_16x16x32_bf16 v[212:215], v[156:159], a[72:75], v[212:215]\n"
    "buffer_load_dwordx4 v230, s[36:39], s52 offen lds\n"
    "v_mfma_f32_16x16x32_bf16 v[212:215], v[160:163], a[76:79], v[212:215]\n"
    "s_add_u32 s52, s52, 4096\n"
    "v_fma_f32 v218, v218, s56, v243\n"
    "v_fma_f32 v219, v219, s56, v243\n"
    "v_mfma_f32_16x16x32_bf16 v[212:215], v[164:167], a[80:83], v[212:215]\n"
    "v_exp_f32 v216, v216\n"
    "v_mfma_f32_16x16x32_bf16 v[212:215], v[168:171], a[84:87], v[212:215]\n"
    "v_exp_f32 v217, v217\n"
    "v_mfma_f32_16x16x32_bf16 v[212:215], v[172:175], a[88:91], v[212:215]\n"
    "ds_read_b128 v[184:187], v233 offset:576\n"
    "v_exp_f32 v218, v218\n"
    "v_exp_f32 v219, v219\n"
    "v_mfma_f32_16x16x32_bf16 v[212:215], v[176:179], a[92:95], v[212:215]\n"
    "v_cvt_pk_bf16_f32 v14, v216, v217\n"
    "v_cvt_pk_bf16_f32 v15, v218, v219\n"
    "v_mfma_f32_16x16x32_bf16 v[216:219], v[148:151], a[96:99], 0\n"
    "ds_read_b128 v[188:191], v233 offset:640\n"
    "v_fma_f32 v220, v220, s56, v241\n"
    "v_mfma_f32_16x16x32_bf16 v[216:219], v[152:155], a[100:103], v[216:219]\n"
    "s_waitcnt lgkmcnt(2)\n"
    "ds_read_b128 v[192:195], v233 offset:704\n"
    "v_fma_f32 v221, v221, s56, v241\n"
    "v_mfma_f32_16x16x32_bf16 v[216:219], v[156:159], a[104:107], v[216:219]\n"
    "buffer_store_dwordx4 v[252:255], v232, s[48:51], s24 offen offset:0 sc1\n"
    "v_mfma_f32_16x16x32_bf16 v[216:219], v[160:163], a[108:111], v[216:219]\n"
    "v_fma_f32 v222, v222, s56, v241\n"
    "v_fma_f32 v223, v223, s56, v241\n"
    "v_mfma_f32_16x16x32_bf16 v[216:219], v[164:167], a[112:115], v[216:219]\n"
    "s_add_u32 s96, s54, s62\n"
    "v_exp_f32 v220, v220\n"
    "v_mfma_f32_16x16x32_bf16 v[216:219], v[168:171], a[116:119], v[216:219]\n"
    "s_add_u32 s54, s54, 16896\n"
    "v_exp_f32 v221, v221\n"
    "v_mfma_f32_16x16x32_bf16 v[216:219], v[172:175], a[120:123], v[216:219]\n"
    "v_exp_f32 v222, v222\n"
    "v_exp_f32 v223, v223\n"
    "v_mfma_f32_16x16x32_bf16 v[216:219], v[176:179], a[124:127], v[216:219]\n"
    "s_add_u32 m0, s55, 12672\n"
    "ds_read_b128 v[196:199], v233 offset:768\n"
    "v_cvt_pk_bf16_f32 v6, v220, v221\n"
    "v_cvt_pk_bf16_f32 v7, v222, v223\n"
    "v_mfma_f32_16x16x32_bf16 v[220:223], v[148:151], a[32:35], 0\n"
    "s_sub_u32 s97, s54, 84480\n"
    "buffer_load_dwordx4 v230, s[36:39], s52 offen lds\n"
    "v_mfma_f32_16x16x32_bf16 v[220:223], v[152:155], a[36:39], v[220:223]\n"
    "s_add_u32 s52, s52, 4096\n"
    "ds_read_b128 v[200:203], v233 offset:832\n"
    "v_fma_f32 v224, v224, s56, v240\n"
    "v_mfma_f32_16x16x32_bf16 v[220:223], v[156:159], a[40:43], v[220:223]\n"
    "s_min_u32 s54, s54, s97\n"
    "v_fma_f32 v225, v225, s56, v240\n"
    "v_fma_f32 v226, v226, s56, v240\n"
    "v_mfma_f32_16x16x32_bf16 v[220:223], v[160:163], a[44:47], v[220:223]\n"
    "s_sub_u32 s97, s54, s66\n"
    "ds_read_b128 v[204:207], v233 offset:896\n"
    "v_fma_f32 v227, v227, s56, v240\n"
    "v_mfma_f32_16x16x32_bf16 v[220:223], v[164:167], a[48:51], v[220:223]\n"
    "s_mov_b32 s66, s54\n"
    "v_exp_f32 v224, v224\n"
    "v_mfma_f32_16x16x32_bf16 v[220:223], v[168:171], a[52:55], v[220:223]\n"
    "v_exp_f32 v225, v225\n"
    "v_mfma_f32_16x16x32_bf16 v[220:223], v[172:175], a[56:59], v[220:223]\n"
    "v_exp_f32 v226, v226\n"
    "v_exp_f32 v227, v227\n"
    "v_mfma_f32_16x16x32_bf16 v[220:223], v[176:179], a[60:63], v[220:223]\n"
    "v_cvt_pk_bf16_f32 v2, v224, v225\n"
    "v_cvt_pk_bf16_f32 v3, v226, v227\n"
    "v_mfma_f32_16x16x32_bf16 v[224:227], v[148:151], a[0:3], 0\n"
    "ds_read_b128 v[208:211], v233 offset:960\n"
    "v_fma_f32 v212, v212, s56, v242\n"
    "v_mfma_f32_16x16x32_bf16 v[224:227], v[152:155], a[4:7], v[224:227]\n"
    "v_fma_f32 v213, v213, s56, v242\n"
    "v_fma_f32 v214, v214, s56, v242\n"
    "v_mfma_f32_16x16x32_bf16 v[224:227], v[156:159], a[8:11], v[224:227]\n"
    "v_fma_f32 v215, v215, s56, v242\n"
    "v_add_u32 v233, s97, v233\n"
    "v_mfma_f32_16x16x32_bf16 v[224:227], v[160:163], a[12:15], v[224:227]\n"
    "v_exp_f32 v212, v212\n"
    "v_mfma_f32_16x16x32_bf16 v[224:227], v[164:167], a[16:19], v[224:227]\n"
    "v_exp_f32 v213, v213\n"
    "v_mfma_f32_16x16x32_bf16 v[224:227], v[168:171], a[20:23], v[224:227]\n"
    "v_exp_f32 v214, v214\n"
    "v_mfma_f32_16x16x32_bf16 v[224:227], v[172:175], a[24:27], v[224:227]\n"
    "v_exp_f32 v215, v215\n"
    "v_mfma_f32_16x16x32_bf16 v[224:227], v[176:179], a[28:31], v[224:227]\n"
    "s_waitcnt vmcnt(9) lgkmcnt(0)\n"
    "v_cvt_pk_bf16_f32 v24, v212, v213\n"
    "v_cvt_pk_bf16_f32 v25, v214, v215\n"
    "v_mfma_f32_16x16x32_bf16 v[212:215], v[180:183], a[64:67], 0\n"
    "ds_inc_u32 v228, v236\n"
    "v_mfma_f32_16x16x32_bf16 v[212:215], v[184:187], a[68:71], v[212:215]\n"
    "ds_read_b64 v[252:253], v238 offset:64\n"
    "v_fma_f32 v216, v216, s56, v243\n"
    "v_mfma_f32_16x16x32_bf16 v[212:215], v[188:191], a[72:75], v[212:215]\n"
    "s_add_u32 s61, s61, 1\n"
    "ds_read_b128 v[148:151], v233 offset:0\n"
    "v_fma_f32 v217, v217, s56, v243\n"
    "v_mfma_f32_16x16x32_bf16 v[212:215], v[192:195], a[76:79], v[212:215]\n"
    "ds_read_b64 v[254:255], v238 offset:192\n"
    "v_fma_f32 v218, v218, s56, v243\n"
    "v_mfma_f32_16x16x32_bf16 v[212:215], v[196:199], a[80:83], v[212:215]\n"
    "ds_read_b128 v[152:155], v233 offset:64\n"
    "v_fma_f32 v219, v219, s56, v243\n"
    "v_mfma_f32_16x16x32_bf16 v[212:215], v[200:203], a[84:87], v[212:215]\n"
    "v_exp_f32 v216, v216\n"
    "v_mfma_f32_16x16x32_bf16 v[212:215], v[204:207], a[88:91], v[212:215]\n"
    "v_exp_f32 v217, v217\n"
    "v_exp_f32 v218, v218\n"
    "v_exp_f32 v219, v219\n"
    "v_mfma_f32_16x16x32_bf16 v[212:215], v[208:211], a[92:95], v[212:215]\n"
    "v_cvt_pk_bf16_f32 v28, v216, v217\n"
    "v_cvt_pk_bf16_f32 v29, v218, v219\n"
    "v_mfma_f32_16x16x32_bf16 v[216:219], v[180:183], a[96:99], 0\n"
    "ds_read_b128 v[156:159], v233 offset:128\n"
    "v_fma_f32 v220, v220, s56, v241\n"
    "v_mfma_f32_16x16x32_bf16 v[216:219], v[184:187], a[100:103], v[216:219]\n"
    "s_waitcnt lgkmcnt(2)\n"
    "ds_read_b32 v229, v230\n"
    "v_fma_f32 v221, v221, s56, v241\n"
    "v_mfma_f32_16x16x32_bf16 v[216:219], v[188:191], a[104:107], v[216:219]\n"
    "buffer_store_dwordx4 v[252:255], v232, s[48:51], s25 offen offset:0 sc1\n"
    "v_mfma_f32_16x16x32_bf16 v[216:219], v[192:195], a[108:111], v[216:219]\n"
    "ds_read_b128 v[160:163], v233 offset:192\n"
    "v_fma_f32 v222, v222, s56, v241\n"
    "v_mfma_f32_16x16x32_bf16 v[216:219], v[196:199], a[112:115], v[216:219]\n"
    "v_fma_f32 v223, v223, s56, v241\n"
    "v_fma_f32 v224, v224, s56, v240\n"
    "v_mfma_f32_16x16x32_bf16 v[216:219], v[200:203], a[116:119], v[216:219]\n"
    "s_waitcnt lgkmcnt(1)\n"
    "ds_read_b128 v[164:167], v233 offset:256\n"
    "v_cmp_gt_u32 vcc, s61, v229\n"
    "v_mfma_f32_16x16x32_bf16 v[216:219], v[204:207], a[120:123], v[216:219]\n"
    "v_exp_f32 v220, v220\n"
    "v_exp_f32 v221, v221\n"
    "v_exp_f32 v222, v222\n"
    "v_exp_f32 v223, v223\n"
    "v_mfma_f32_16x16x32_bf16 v[216:219], v[208:211], a[124:127], v[216:219]\n"
    "v_cvt_pk_bf16_f32 v20, v220, v221\n"
    "v_cvt_pk_bf16_f32 v21, v222, v223\n"
    "v_mfma_f32_16x16x32_bf16 v[220:223], v[180:183], a[32:35], 0\n"
    "s_cbranch_vccnz L_sp1%=\n"
    "L_rt1%=:\n"
    "ds_read_b128 v[168:171], v233 offset:320\n"
    "v_mfma_f32_16x16x32_bf16 v[220:223], v[184:187], a[36:39], v[220:223]\n"
    "s_add_u32 m0, s96, 0\n"
    "v_fma_f32 v225, v225, s56, v240\n"
    "v_fma_f32 v226, v226, s56, v240\n"
    "v_mfma_f32_16x16x32_bf16 v[220:223], v[188:191], a[40:43], v[220:223]\n"
    "buffer_load_dwordx4 v230, s[36:39], s52 offen lds\n"
    "v_mfma_f32_16x16x32_bf16 v[220:223], v[192:195], a[44:47], v[220:223]\n"
    "s_add_u32 s52, s52, 4096\n"
    "ds_read_b128 v[172:175], v233 offset:384\n"
    "v_fma_f32 v227, v227, s56, v240\n"
    "v_mfma_f32_16x16x32_bf16 v[220:223], v[196:199], a[48:51], v[220:223]\n"
    "v_exp_f32 v224, v224\n"
    "v_mfma_f32_16x16x32_bf16 v[220:223], v[200:203], a[52:55], v[220:223]\n"
    "v_exp_f32 v225, v225\n"
    "v_mfma_f32_16x16x32_bf16 v[220:223], v[204:207], a[56:59], v[220:223]\n"
    "v_exp_f32 v226, v226\n"
    "v_mfma_f32_16x16x32_bf16 v[220:223], v[208:211], a[60:63], v[220:223]\n"
    "v_exp_f32 v227, v227\n"
    "v_mfma_f32_16x16x32_bf16 a[136:139], a[144:147], v[8:11], 0\n"
    "v_cvt_pk_bf16_f32 v16, v224, v225\n"
    "v_cvt_pk_bf16_f32 v17, v226, v227\n"
    "v_mfma_f32_16x16x32_bf16 v[224:227], v[180:183], a[0:3], 0\n"
    "s_add_u32 m0, s96, 4224\n"
    "ds_read_b128 v[176:179], v233 offset:448\n"
    "v_fma_f32 v212, v212, s56, v242\n"
    "v_mfma_f32_16x16x32_bf16 v[224:227], v[184:187], a[4:7], v[224:227]\n"
    "buffer_load_dwordx4 v230, s[36:39], s52 offen lds\n"
    "v_mfma_f32_16x16x32_bf16 v[224:227], v[188:191], a[8:11], v[224:227]\n"
    "s_add_u32 s52, s52, 4096\n"
    "v_fma_f32 v213, v213, s56, v242\n"
    "v_fma_f32 v214, v214, s56, v242\n"
    "v_mfma_f32_16x16x32_bf16 v[224:227], v[192:195], a[12:15], v[224:227]\n"
    "v_fma_f32 v215, v215, s56, v242\n"
    "v_fma_f32 v216, v216, s56, v243\n"
    "v_mfma_f32_16x16x32_bf16 v[224:227], v[196:199], a[16:19], v[224:227]\n"
    "v_exp_f32 v212, v212\n"
    "v_mfma_f32_16x16x32_bf16 v[224:227], v[200:203], a[20:23], v[224:227]\n"
    "v_exp_f32 v213, v213\n"
    "v_mfma_f32_16x16x32_bf16 v[224:227], v[204:207], a[24:27], v[224:227]\n"
    "v_exp_f32 v214, v214\n"
    "v_mfma_f32_16x16x32_bf16 v[224:227], v[208:211], a[28:31], v[224:227]\n"
    "v_exp_f32 v215, v215\n"
    "v_mfma_f32_16x16x32_bf16 a[140:143], a[144:147], v[12:15], 0\n"
    "s_waitcnt lgkmcnt(0)\n"
    "v_cvt_pk_bf16_f32 v26, v212, v213\n"
    "v_cvt_pk_bf16_f32 v27, v214, v215\n"
    "v_mfma_f32_16x16x32_bf16 v[212:215], v[148:151], a[64:67], 0\n"
    "s_add_u32 m0, s96, 8448\n"
    "ds_read_b128 v[180:183], v233 offset:512\n"
    "v_fma_f32 v217, v217, s56, v243\n"
    "v_mfma_f32_16x16x32_bf16 v[212:215], v[152:155], a[68:71], v[212:215]\n"
    "buffer_load_dwordx4 v230, s[36:39], s52 offen lds\n"
    "v_mfma_f32_16x16x32_bf16 v[212:215], v[156:159], a[72:75], v[212:215]\n"
    "s_add_u32 s52, s52, 4096\n"
    "v_fma_f32 v218, v218, s56, v243\n"
    "v_fma_f32 v219, v219, s56, v243\n"
    "v_mfma_f32_16x16x32_bf16 v[212:215], v[160:163], a[76:79], v[212:215]\n"
    "ds_read_b64 v[252:253], v238 offset:2304\n"
    "v_fma_f32 v220, v220, s56, v241\n"
    "v_mfma_f32_16x16x32_bf16 v[212:215], v[164:167], a[80:83], v[212:215]\n"
    "v_exp_f32 v216, v216\n"
    "v_mfma_f32_16x16x32_bf16 v[212:215], v[168:171], a[84:87], v[212:215]\n"
    "v_exp_f32 v217, v217\n"
    "v_mfma_f32_16x16x32_bf16 v[212:215], v[172:175], a[88:91], v[212:215]\n"
    "ds_read_b128 v[184:187], v233 offset:576\n"
    "v_exp_f32 v218, v218\n"
    "v_exp_f32 v219, v219\n"
    "v_mfma_f32_16x16x32_bf16 v[212:215], v[176:179], a[92:95], v[212:215]\n"
    "s_add_u32 m0, s96, 12672\n"
    "ds_read_b64 v[254:255], v238 offset:2432\n"
    "v_cvt_pk_bf16_f32 v30, v216, v217\n"
    "v_cvt_pk_bf16_f32 v31, v218, v219\n"
    "v_mfma_f32_16x16x32_bf16 v[216:219], v[148:151], a[96:99], 0\n"
    "buffer_load_dwordx4 v230, s[36:39], s52 offen lds\n"
    "v_mfma_f32_16x16x32_bf16 v[216:219], v[152:155], a[100:103], v[216:219]\n"
    "s_add_u32 s52, s52, 4096\n"
    "ds_read_b128 v[188:191], v233 offset:640\n"
    "v_fma_f32 v221, v221, s56, v241\n"
    "v_mfma_f32_16x16x32_bf16 v[216:219], v[156:159], a[104:107], v[216:219]\n"
    "v_fma_f32 v222, v222, s56, v241\n"
    "v_fma_f32 v223, v223, s56, v241\n"
    "v_mfma_f32_16x16x32_bf16 v[216:219], v[160:163], a[108:111], v[216:219]\n"
    "ds_read_b128 v[192:195], v233 offset:704\n"
    "v_fma_f32 v224, v224, s56, v240\n"
    "v_mfma_f32_16x16x32_bf16 v[216:219], v[164:167], a[112:115], v[216:219]\n"
    "v_exp_f32 v220, v220\n"
    "v_mfma_f32_16x16x32_bf16 v[216:219], v[168:171], a[116:119], v[216:219]\n"
    "s_waitcnt lgkmcnt(2)\n"
    "v_exp_f32 v221, v221\n"
    "v_exp_f32 v222, v222\n"
    "v_exp_f32 v223, v223\n"
    "v_mfma_f32_16x16x32_bf16 v[216:219], v[172:175], a[120:123], v[216:219]\n"
    "s_add_u32 s55, s54, s62\n"
    "buffer_store_dwordx4 v[252:255], v232, s[48:51], s26 offen offset:0 sc1\n"
    "v_mfma_f32_16x16x32_bf16 v[216:219], v[176:179], a[124:127], v[216:219]\n"
    "s_add_u32 s54, s54, 16896\n"
    "v_cvt_pk_bf16_f32 v22, v220, v221\n"
    "v_cvt_pk_bf16_f32 v23, v222, v223\n"
    "v_mfma_f32_16x16x32_bf16 v[220:223], v[148:151], a[32:35], 0\n"
    "s_sub_u32 s97, s54, 84480\n"
    "ds_read_b128 v[196:199], v233 offset:768\n"
    "v_fma_f32 v225, v225, s56, v240\n"
    "v_mfma_f32_16x16x32_bf16 v[220:223], v[152:155], a[36:39], v[220:223]\n"
    "s_min_u32 s54, s54, s97\n"
    "ds_read_b128 v[200:203], v233 offset:832\n"
    "v_fma_f32 v226, v226, s56, v240\n"
    "v_mfma_f32_16x16x32_bf16 v[220:223], v[156:159], a[40:43], v[220:223]\n"
    "s_sub_u32 s97, s54, s66\n"
    "v_fma_f32 v227, v227, s56, v240\n"
    "v_fma_f32 v212, v212, s56, v242\n"
    "v_mfma_f32_16x16x32_bf16 v[220:223], v[160:163], a[44:47], v[220:223]\n"
    "s_mov_b32 s66, s54\n"
    "ds_read_b128 v[204:207], v233 offset:896\n"
    "v_fma_f32 v213, v213, s56, v242\n"
    "v_mfma_f32_16x16x32_bf16 v[220:223], v[164:167], a[48:51], v[220:223]\n"
    "v_exp_f32 v224, v224\n"
    "v_mfma_f32_16x16x32_bf16 v[220:223], v[168:171], a[52:55], v[220:223]\n"
    "v_exp_f32 v225, v225\n"
    "v_mfma_f32_16x16x32_bf16 v[220:223], v[172:175], a[56:59], v[220:223]\n"
    "v_exp_f32 v226, v226\n"
    "v_mfma_f32_16x16x32_bf16 v[220:223], v[176:179], a[60:63], v[220:223]\n"
    "v_exp_f32 v227, v227\n"
    "v_mfma_f32_16x16x32_bf16 a[132:135], a[144:147], v[4:7], 0\n"
    "v_cvt_pk_bf16_f32 v18, v224, v225\n"
    "v_cvt_pk_bf16_f32 v19, v226, v227\n"
    "v_mfma_f32_16x16x32_bf16 v[224:227], v[148:151], a[0:3], 0\n"
    "ds_read_b128 v[208:211], v233 offset:960\n"
    "v_fma_f32 v214, v214, s56, v242\n"
    "v_mfma_f32_16x16x32_bf16 v[224:227], v[152:155], a[4:7], v[224:227]\n"
    "v_fma_f32 v215, v215, s56, v242\n"
    "v_add_u32 v233, s97, v233\n"
    "v_mfma_f32_16x16x32_bf16 v[224:227], v[156:159], a[8:11], v[224:227]\n"
    "v_exp_f32 v212, v212\n"
    "v_mfma_f32_16x16x32_bf16 v[224:227], v[160:163], a[12:15], v[224:227]\n"
    "v_exp_f32 v213, v213\n"
    "v_mfma_f32_16x16x32_bf16 v[224:227], v[164:167], a[16:19], v[224:227]\n"
    "v_exp_f32 v214, v214\n"
    "v_mfma_f32_16x16x32_bf16 v[224:227], v[168:171], a[20:23], v[224:227]\n"
    "v_exp_f32 v215, v215\n"
    "v_mfma_f32_16x16x32_bf16 v[224:227], v[172:175], a[24:27], v[224:227]\n"
    "v_cvt_pk_bf16_f32 v36, v212, v213\n"
    "v_cvt_pk_bf16_f32 v37, v214, v215\n"
    "v_mfma_f32_16x16x32_bf16 v[224:227], v[176:179], a[28:31], v[224:227]\n"
    "ds_read_b64 v[252:253], v238 offset:2368\n"
    "v_fma_f32 v216, v216, s56, v243\n"
    "v_mfma_f32_16x16x32_bf16 a[128:131], a[144:147], v[0:3], 0\n"
    "s_waitcnt vmcnt(11) lgkmcnt(1)\n"
    "ds_inc_u32 v228, v236\n"
    "v_mfma_f32_16x16x32_bf16 v[212:215], v[180:183], a[64:67], 0\n"
    "ds_read_b128 v[148:151], v233 offset:0\n"
    "v_fma_f32 v217, v217, s56, v243\n"
    "v_mfma_f32_16x16x32_bf16 v[212:215], v[184:187], a[68:71], v[212:215]\n"
    "ds_read_b64 v[254:255], v238 offset:2496\n"
    "v_fma_f32 v218, v218, s56, v243\n"
    "v_mfma_f32_16x16x32_bf16 v[212:215], v[188:191], a[72:75], v[212:215]\n"
    "v_fma_f32 v219, v219, s56, v243\n"
    "v_fma_f32 v220, v220, s56, v241\n"
    "v_mfma_f32_16x16x32_bf16 v[212:215], v[192:195], a[76:79], v[212:215]\n"
    "v_exp_f32 v216, v216\n"
    "v_mfma_f32_16x16x32_bf16 v[212:215], v[196:199], a[80:83], v[212:215]\n"
    "v_exp_f32 v217, v217\n"
    "v_mfma_f32_16x16x32_bf16 v[212:215], v[200:203], a[84:87], v[212:215]\n"
    "v_exp_f32 v218, v218\n"
    "v_mfma_f32_16x16x32_bf16 v[212:215], v[204:207], a[88:91], v[212:215]\n"
    "ds_read_b128 v[152:155], v233 offset:64\n"
    "v_exp_f32 v219, v219\n"
    "v_mfma_f32_16x16x32_bf16 v[212:215], v[208:211], a[92:95], v[212:215]\n"
    "v_cvt_pk_bf16_f32 v248, v216, v217\n"
    "v_cvt_pk_bf16_f32 v249, v218, v219\n"
    "v_mfma_f32_16x16x32_bf16 v[216:219], v[180:183], a[96:99], 0\n"
    "ds_read_b128 v[156:159], v233 offset:128\n"
    "v_accvgpr_write_b32 a152, v248\n"
    "v_mfma_f32_16x16x32_bf16 v[216:219], v[184:187], a[100:103], v[216:219]\n"
    "s_waitcnt lgkmcnt(2)\n"
    "ds_read_b128 v[160:163], v233 offset:192\n"
    "v_accvgpr_write_b32 a153, v249\n"
    "v_mfma_f32_16x16x32_bf16 v[216:219], v[188:191], a[104:107], v[216:219]\n"
    "buffer_store_dwordx4 v[252:255], v232, s[48:51], s27 offen offset:0 sc1\n"
    "v_mfma_f32_16x16x32_bf16 v[216:219], v[192:195], a[108:111], v[216:219]\n"
    "v_fma_f32 v221, v221, s56, v241\n"
    "v_fma_f32 v222, v222, s56, v241\n"
    "v_mfma_f32_16x16x32_bf16 v[216:219], v[196:199], a[112:115], v[216:219]\n"
    "v_fma_f32 v223, v223, s56, v241\n"
    "v_fma_f32 v224, v224, s56, v240\n"
    "v_mfma_f32_16x16x32_bf16 v[216:219], v[200:203], a[116:119], v[216:219]\n"
    "v_exp_f32 v220, v220\n"
    "v_mfma_f32_16x16x32_bf16 v[216:219], v[204:207], a[120:123], v[216:219]\n"
    "v_exp_f32 v221, v221\n"
    "v_exp_f32 v222, v222\n"
    "v_exp_f32 v223, v223\n"
    "v_mfma_f32_16x16x32_bf16 v[216:219], v[208:211], a[124:127], v[216:219]\n"
    "v_cvt_pk_bf16_f32 v32, v220, v221\n"
    "v_cvt_pk_bf16_f32 v33, v222, v223\n"
    "v_mfma_f32_16x16x32_bf16 v[220:223], v[180:183], a[32:35], 0\n"
    "ds_read_b128 v[164:167], v233 offset:256\n"
    "v_fma_f32 v225, v225, s56, v240\n"
    "v_mfma_f32_16x16x32_bf16 v[220:223], v[184:187], a[36:39], v[220:223]\n"
    "ds_read_b128 v[168:171], v233 offset:320\n"
    "v_fma_f32 v226, v226, s56, v240\n"
    "v_mfma_f32_16x16x32_bf16 v[220:223], v[188:191], a[40:43], v[220:223]\n"
    "v_fma_f32 v227, v227, s56, v240\n"
    "v_fma_f32 v212, v212, s56, v242\n"
    "v_mfma_f32_16x16x32_bf16 v[220:223], v[192:195], a[44:47], v[220:223]\n"
    "ds_read_b128 v[172:175], v233 offset:384\n"
    "v_fma_f32 v213, v213, s56, v242\n"
    "v_mfma_f32_16x16x32_bf16 v[220:223], v[196:199], a[48:51], v[220:223]\n"
    "v_exp_f32 v224, v224\n"
    "v_mfma_f32_16x16x32_bf16 v[220:223], v[200:203], a[52:55], v[220:223]\n"
    "v_exp_f32 v225, v225\n"
    "v_mfma_f32_16x16x32_bf16 v[220:223], v[204:207], a[56:59], v[220:223]\n"
    "v_exp_f32 v226, v226\n"
    "v_mfma_f32_16x16x32_bf16 v[220:223], v[208:211], a[60:63], v[220:223]\n"
    "v_exp_f32 v227, v227\n"
    "v_mfma_f32_16x16x32_bf16 a[136:139], a[144:147], v[24:27], a[136:139]\n"
    "v_cvt_pk_bf16_f32 v250, v224, v225\n"
    "v_cvt_pk_bf16_f32 v251, v226, v227\n"
    "v_mfma_f32_16x16x32_bf16 v[224:227], v[180:183], a[0:3], 0\n"
    "ds_read_b128 v[176:179], v233 offset:448\n"
    "v_accvgpr_write_b32 a148, v250\n"
    "v_mfma_f32_16x16x32_bf16 v[224:227], v[184:187], a[4:7], v[224:227]\n"
    "v_accvgpr_write_b32 a149, v251\n"
    "v_fma_f32 v214, v214, s56, v242\n"
    "v_mfma_f32_16x16x32_bf16 v[224:227], v[188:191], a[8:11], v[224:227]\n"
    "ds_read_b64 v[252:253], v238 offset:4608\n"
    "v_fma_f32 v215, v215, s56, v242\n"
    "v_mfma_f32_16x16x32_bf16 v[224:227], v[192:195], a[12:15], v[224:227]\n"
    "v_exp_f32 v212, v212\n"
    "v_mfma_f32_16x16x32_bf16 v[224:227], v[196:199], a[16:19], v[224:227]\n"
    "v_exp_f32 v213, v213\n"
    "v_mfma_f32_16x16x32_bf16 v[224:227], v[200:203], a[20:23], v[224:227]\n"
    "v_exp_f32 v214, v214\n"
    "v_mfma_f32_16x16x32_bf16 v[224:227], v[204:207], a[24:27], v[224:227]\n"
    "v_exp_f32 v215, v215\n"
    "v_mfma_f32_16x16x32_bf16 v[224:227], v[208:211], a[28:31], v[224:227]\n"
    "v_cvt_pk_bf16_f32 v38, v212, v213\n"
    "v_cvt_pk_bf16_f32 v39, v214, v215\n"
    "v_mfma_f32_16x16x32_bf16 a[140:143], a[144:147], v[28:31], a[140:143]\n"
    "s_waitcnt lgkmcnt(1)\n"
    "ds_read_b64 v[254:255], v238 offset:4736\n"
    "v_fma_f32 v216, v216, s56, v243\n"
    "L_ent%=:\n"
    "v_mfma_f32_16x16x32_bf16 v[212:215], v[148:151], a[64:67], 0\n"
    "ds_read_b128 v[180:183], v233 offset:512\n"
    "v_fma_f32 v217, v217, s56, v243\n"
    "v_mfma_f32_16x16x32_bf16 v[212:215], v[152:155], a[68:71], v[212:215]\n"
    "s_add_u32 m0, s59, 0\n"
    "v_fma_f32 v218, v218, s56, v243\n"
    "v_fma_f32 v219, v219, s56, v243\n"
    "v_mfma_f32_16x16x32_bf16 v[212:215], v[156:159], a[72:75], v[212:215]\n"
    "s_add_u32 s61, s61, 1\n"
    "buffer_load_dwordx4 v230, s[44:47], s58 offen nt lds\n"
    "v_mfma_f32_16x16x32_bf16 v[212:215], v[160:163], a[76:79], v[212:215]\n"
    "s_add_u32 s58, s58, 1024\n"
    "v_exp_f32 v216, v216\n"
    "v_mfma_f32_16x16x32_bf16 v[212:215], v[164:167], a[80:83], v[212:215]\n"
    "v_exp_f32 v217, v217\n"
    "v_mfma_f32_16x16x32_bf16 v[212:215], v[168:171], a[84:87], v[212:215]\n"
    "v_exp_f32 v218, v218\n"
    "v_mfma_f32_16x16x32_bf16 v[212:215], v[172:175], a[88:91], v[212:215]\n"
    "ds_read_b128 v[184:187], v233 offset:576\n"
    "v_exp_f32 v219, v219\n"
    "v_mfma_f32_16x16x32_bf16 v[212:215], v[176:179], a[92:95], v[212:215]\n"
    "v_cvt_pk_bf16_f32 v248, v216, v217\n"
    "v_cvt_pk_bf16_f32 v249, v218, v219\n"
    "v_mfma_f32_16x16x32_bf16 v[216:219], v[148:151], a[96:99], 0\n"
    "ds_read_b32 v229, v230\n"
    "v_accvgpr_write_b32 a154, v248\n"
    "v_mfma_f32_16x16x32_bf16 v[216:219], v[152:155], a[100:103], v[216:219]\n"
    "s_waitcnt lgkmcnt(3)\n"
    "ds_read_b128 v[188:191], v233 offset:640\n"
    "v_accvgpr_write_b32 a155, v249\n"
    "v_mfma_f32_16x16x32_bf16 v[216:219], v[156:159], a[104:107], v[216:219]\n"
    "buffer_store_dwordx4 v[252:255], v232, s[48:51], s28 offen offset:0 sc1\n"
    "v_mfma_f32_16x16x32_bf16 v[216:219], v[160:163], a[108:111], v[216:219]\n"
    "ds_read_b128 v[192:195], v233 offset:704\n"
    "v_fma_f32 v220, v220, s56, v241\n"
    "v_mfma_f32_16x16x32_bf16 v[216:219], v[164:167], a[112:115], v[216:219]\n"
    "s_waitcnt lgkmcnt(2)\n"
    "v_cmp_gt_u32 vcc, s61, v229\n"
    "v_fma_f32 v221, v221, s56, v241\n"
    "v_mfma_f32_16x16x32_bf16 v[216:219], v[168:171], a[116:119], v[216:219]\n"
    "v_fma_f32 v222, v222, s56, v241\n"
    "v_fma_f32 v223, v223, s56, v241\n"
    "v_mfma_f32_16x16x32_bf16 v[216:219], v[172:175], a[120:123], v[216:219]\n"
    "v_exp_f32 v220, v220\n"
    "v_exp_f32 v221, v221\n"
    "v_exp_f32 v222, v222\n"
    "v_exp_f32 v223, v223\n"
    "v_mfma_f32_16x16x32_bf16 v[216:219], v[176:179], a[124:127], v[216:219]\n"
    "s_cbranch_vccnz L_sp2%=\n"
    "L_rt2%=:\n"
    "v_cvt_pk_bf16_f32 v34, v220, v221\n"
    "v_cvt_pk_bf16_f32 v35, v222, v223\n"
    "v_mfma_f32_16x16x32_bf16 v[220:223], v[148:151], a[32:35], 0\n"
    "s_add_u32 m0, s55, 0\n"
    "ds_read_b128 v[196:199], v233 offset:768\n"
    "v_fma_f32 v224, v224, s56, v240\n"
    "v_mfma_f32_16x16x32_bf16 v[220:223], v[152:155], a[36:39], v[220:223]\n"
    "s_add_u32 s96, s54, s62\n"
    "buffer_load_dwordx4 v230, s[36:39], s52 offen lds\n"
    "v_mfma_f32_16x16x32_bf16 v[220:223], v[156:159], a[40:43], v[220:223]\n"
    "s_add_u32 s52, s52, 4096\n"
    "ds_read_b128 v[200:203], v233 offset:832\n"
    "v_fma_f32 v225, v225, s56, v240\n"
    "v_mfma_f32_16x16x32_bf16 v[220:223], v[160:163], a[44:47], v[220:223]\n"
    "s_add_u32 s54, s54, 16896\n"
    "v_fma_f32 v226, v226, s56, v240\n"
    "v_fma_f32 v227, v227, s56, v240\n"
    "v_mfma_f32_16x16x32_bf16 v[220:223], v[164:167], a[48:51], v[220:223]\n"
    "s_sub_u32 s97, s54, 84480\n"
    "ds_read_b128 v[204:207], v233 offset:896\n"
    "v_fma_f32 v212, v212, s56, v242\n"
    "v_mfma_f32_16x16x32_bf16 v[220:223], v[168:171], a[52:55], v[220:223]\n"
    "s_min_u32 s54, s54, s97\n"
    "v_exp_f32 v224, v224\n"
    "v_mfma_f32_16x16x32_bf16 v[220:223], v[172:175], a[56:59], v[220:223]\n"
    "s_sub_u32 s97, s54, s66\n"
    "v_exp_f32 v225, v225\n"
    "v_mfma_f32_16x16x32_bf16 v[220:223], v[176:179], a[60:63], v[220:223]\n"
    "v_exp_f32 v226, v226\n"
    "v_exp_f32 v227, v227\n"
    "v_mfma_f32_16x16x32_bf16 a[132:135], a[144:147], v[20:23], a[132:135]\n"
    "s_add_u32 m0, s55, 4224\n"
    "v_cvt_pk_bf16_f32 v250, v224, v225\n"
    "v_cvt_pk_bf16_f32 v251, v226, v227\n"
    "v_mfma_f32_16x16x32_bf16 v[224:227], v[148:151], a[0:3], 0\n"
    "s_mov_b32 s66, s54\n"
    "buffer_load_dwordx4 v230, s[36:39], s52 offen lds\n"
    "v_mfma_f32_16x16x32_bf16 v[224:227], v[152:155], a[4:7], v[224:227]\n"
    "s_add_u32 s52, s52, 4096\n"
    "ds_read_b128 v[208:211], v233 offset:960\n"
    "v_accvgpr_write_b32 a150, v250\n"
    "v_mfma_f32_16x16x32_bf16 v[224:227], v[156:159], a[8:11], v[224:227]\n"
    "v_accvgpr_write_b32 a151, v251\n"
    "v_fma_f32 v213, v213, s56, v242\n"
    "v_mfma_f32_16x16x32_bf16 v[224:227], v[160:163], a[12:15], v[224:227]\n"
    "v_fma_f32 v214, v214, s56, v242\n"
    "v_fma_f32 v215, v215, s56, v242\n"
    "v_mfma_f32_16x16x32_bf16 v[224:227], v[164:167], a[16:19], v[224:227]\n"
    "v_exp_f32 v212, v212\n"
    "v_mfma_f32_16x16x32_bf16 v[224:227], v[168:171], a[20:23], v[224:227]\n"
    "v_exp_f32 v213, v213\n"
    "v_mfma_f32_16x16x32_bf16 v[224:227], v[172:175], a[24:27], v[224:227]\n"
    "v_exp_f32 v214, v214\n"
    "v_exp_f32 v215, v215\n"
    "v_mfma_f32_16x16x32_bf16 v[224:227], v[176:179], a[28:31], v[224:227]\n"
    "s_add_u32 m0, s55, 8448\n"
    "v_cvt_pk_bf16_f32 v44, v212, v213\n"
    "v_cvt_pk_bf16_f32 v45, v214, v215\n"
    "v_add_u32 v233, s97, v233\n"
    "v_mfma_f32_16x16x32_bf16 a[128:131], a[144:147], v[16:19], a[128:131]\n"
    "s_waitcnt vmcnt(8) lgkmcnt(0)\n"
    "buffer_load_dwordx4 v230, s[36:39], s52 offen lds\n"
    "v_mfma_f32_16x16x32_bf16 v[212:215], v[180:183], a[64:67], 0\n"
    "s_add_u32 s52, s52, 4096\n"
    "ds_inc_u32 v228, v236\n"
    "v_mfma_f32_16x16x32_bf16 v[212:215], v[184:187], a[68:71], v[212:215]\n"
    "ds_read_b64 v[252:253], v238 offset:4672\n"
    "v_fma_f32 v216, v216, s56, v243\n"
    "v_mfma_f32_16x16x32_bf16 v[212:215], v[188:191], a[72:75], v[212:215]\n"
    "s_add_u32 s61, s61, 1\n"
    "ds_read_b128 v[148:151], v233 offset:0\n"
    "v_fma_f32 v217, v217, s56, v243\n"
    "v_mfma_f32_16x16x32_bf16 v[212:215], v[192:195], a[76:79], v[212:215]\n"
    "ds_read_b64 v[254:255], v238 offset:4800\n"
    "v_fma_f32 v218, v218, s56, v243\n"
    "v_mfma_f32_16x16x32_bf16 v[212:215], v[196:199], a[80:83], v[212:215]\n"
    "s_add_u32 m0, s59, 1056\n"
    "ds_read_b128 v[152:155], v233 offset:64\n"
    "v_fma_f32 v219, v219, s56, v243\n"
    "v_mfma_f32_16x16x32_bf16 v[212:215], v[200:203], a[84:87], v[212:215]\n"
    "buffer_load_dwordx4 v230, s[44:47], s58 offen nt lds\n"
    "v_mfma_f32_16x16x32_bf16 v[212:215], v[204:207], a[88:91], v[212:215]\n"
    "v_exp_f32 v216, v216\n"
    "v_exp_f32 v217, v217\n"
    "v_exp_f32 v218, v218\n"
    "v_exp_f32 v219, v219\n"
    "v_mfma_f32_16x16x32_bf16 v[212:215], v[208:211], a[92:95], v[212:215]\n"
    "s_add_u32 s58, s58, 1024\n"
    "v_cvt_pk_bf16_f32 v248, v216, v217\n"
    "v_cvt_pk_bf16_f32 v249, v218, v219\n"
    "v_mfma_f32_16x16x32_bf16 v[216:219], v[180:183], a[96:99], 0\n"
    "ds_read_b128 v[156:159], v233 offset:128\n"
    "v_accvgpr_write_b32 a160, v248\n"
    "v_mfma_f32_16x16x32_bf16 v[216:219], v[184:187], a[100:103], v[216:219]\n"
    "ds_read_b32 v229, v230\n"
    "v_accvgpr_write_b32 a161, v249\n"
    "v_mfma_f32_16x16x32_bf16 v[216:219], v[188:191], a[104:107], v[216:219]\n"
    "s_waitcnt lgkmcnt(3)\n"
    "ds_read_b128 v[160:163], v233 offset:192\n"
    "v_fma_f32 v220, v220, s56, v241\n"
    "v_mfma_f32_16x16x32_bf16 v[216:219], v[192:195], a[108:111], v[216:219]\n"
    "buffer_store_dwordx4 v[252:255], v232, s[48:51], s29 offen offset:0 sc1\n"
    "v_mfma_f32_16x16x32_bf16 v[216:219], v[196:199], a[112:115], v[216:219]\n"
    "v_fma_f32 v221, v221, s56, v241\n"
    "v_fma_f32 v222, v222, s56, v241\n"
    "v_mfma_f32_16x16x32_bf16 v[216:219], v[200:203], a[116:119], v[216:219]\n"
    "s_waitcnt lgkmcnt(1)\n"
    "v_fma_f32 v223, v223, s56, v241\n"
    "v_cmp_gt_u32 vcc, s61, v229\n"
    "v_mfma_f32_16x16x32_bf16 v[216:219], v[204:207], a[120:123], v[216:219]\n"
    "v_exp_f32 v220, v220\n"
    "v_exp_f32 v221, v221\n"
    "v_exp_f32 v222, v222\n"
    "v_exp_f32 v223, v223\n"
    "v_mfma_f32_16x16x32_bf16 v[216:219], v[208:211], a[124:127], v[216:219]\n"
    "v_cvt_pk_bf16_f32 v40, v220, v221\n"
    "v_cvt_pk_bf16_f32 v41, v222, v223\n"
    "v_mfma_f32_16x16x32_bf16 v[220:223], v[180:183], a[32:35], 0\n"
    "s_cbranch_vccnz L_sp3%=\n"
    "L_rt3%=:\n"
    "ds_read_b128 v[164:167], v233 offset:256\n"
    "v_mfma_f32_16x16x32_bf16 v[220:223], v[184:187], a[36:39], v[220:223]\n"
    "s_add_u32 m0, s55, 12672\n"
    "ds_read_b128 v[168:171], v233 offset:320\n"
    "v_fma_f32 v224, v224, s56, v240\n"
    "v_mfma_f32_16x16x32_bf16 v[220:223], v[188:191], a[40:43], v[220:223]\n"
    "buffer_load_dwordx4 v230, s[36:39], s52 offen lds\n"
    "v_mfma_f32_16x16x32_bf16 v[220:223], v[192:195], a[44:47], v[220:223]\n"
    "s_add_u32 s52, s52, 4096\n"
    "v_fma_f32 v225, v225, s56, v240\n"
    "v_fma_f32 v226, v226, s56, v240\n"
    "v_mfma_f32_16x16x32_bf16 v[220:223], v[196:199], a[48:51], v[220:223]\n"
    "ds_read_b128 v[172:175], v233 offset:384\n"
    "v_fma_f32 v227, v227, s56, v240\n"
    "v_mfma_f32_16x16x32_bf16 v[220:223], v[200:203], a[52:55], v[220:223]\n"
    "v_exp_f32 v224, v224\n"
    "v_mfma_f32_16x16x32_bf16 v[220:223], v[204:207], a[56:59], v[220:223]\n"
    "v_exp_f32 v225, v225\n"
    "v_exp_f32 v226, v226\n"
    "v_exp_f32 v227, v227\n"
    "v_mfma_f32_16x16x32_bf16 v[220:223], v[208:211], a[60:63], v[220:223]\n"
    "s_add_u32 m0, s96, 0\n"
    "v_cvt_pk_bf16_f32 v250, v224, v225\n"
    "v_cvt_pk_bf16_f32 v251, v226, v227\n"
    "v_mfma_f32_16x16x32_bf16 a[136:139], a[144:147], v[36:39], a[136:139]\n"
    "buffer_load_dwordx4 v230, s[36:39], s52 offen lds\n"
    "v_mfma_f32_16x16x32_bf16 v[224:227], v[180:183], a[0:3], 0\n"
    "s_add_u32 s52, s52, 4096\n"
    "ds_read_b128 v[176:179], v233 offset:448\n"
    "v_accvgpr_write_b32 a156, v250\n"
    "v_mfma_f32_16x16x32_bf16 v[224:227], v[184:187], a[4:7], v[224:227]\n"
    "v_accvgpr_write_b32 a157, v251\n"
    "v_fma_f32 v212, v212, s56, v242\n"
    "v_mfma_f32_16x16x32_bf16 v[224:227], v[188:191], a[8:11], v[224:227]\n"
    "v_fma_f32 v213, v213, s56, v242\n"
    "v_fma_f32 v214, v214, s56, v242\n"
    "v_mfma_f32_16x16x32_bf16 v[224:227], v[192:195], a[12:15], v[224:227]\n"
    "v_fma_f32 v215, v215, s56, v242\n"
    "v_fma_f32 v216, v216, s56, v243\n"
    "v_mfma_f32_16x16x32_bf16 v[224:227], v[196:199], a[16:19], v[224:227]\n"
    "s_add_u32 m0, s96, 4224\n"
    "v_exp_f32 v212, v212\n"
    "v_mfma_f32_16x16x32_bf16 v[224:227], v[200:203], a[20:23], v[224:227]\n"
    "buffer_load_dwordx4 v230, s[36:39], s52 offen lds\n"
    "v_mfma_f32_16x16x32_bf16 v[224:227], v[204:207], a[24:27], v[224:227]\n"
    "s_add_u32 s52, s52, 4096\n"
    "v_exp_f32 v213, v213\n"
    "v_mfma_f32_16x16x32_bf16 v[224:227], v[208:211], a[28:31], v[224:227]\n"
    "v_exp_f32 v214, v214\n"
    "v_exp_f32 v215, v215\n"
    "v_mfma_f32_16x16x32_bf16 a[140:143], a[144:147], a[152:155], a[140:143]\n"
    "s_waitcnt lgkmcnt(0)\n"
    "v_cvt_pk_bf16_f32 v46, v212, v213\n"
    "v_cvt_pk_bf16_f32 v47, v214, v215\n"
    "v_mfma_f32_16x16x32_bf16 v[212:215], v[148:151], a[64:67], 0\n"
    "ds_read_b128 v[180:183], v233 offset:512\n"
    "v_fma_f32 v217, v217, s56, v243\n"
    "v_mfma_f32_16x16x32_bf16 v[212:215], v[152:155], a[68:71], v[212:215]\n"
    "s_add_u32 m0, s96, 8448\n"
    "v_fma_f32 v218, v218, s56, v243\n"
    "v_fma_f32 v219, v219, s56, v243\n"
    "v_mfma_f32_16x16x32_bf16 v[212:215], v[156:159], a[72:75], v[212:215]\n"
    "buffer_load_dwordx4 v230, s[36:39], s52 offen lds\n"
    "v_mfma_f32_16x16x32_bf16 v[212:215], v[160:163], a[76:79], v[212:215]\n"
    "s_add_u32 s52, s52, 4096\n"
    "ds_read_b64 v[252:253], v238 offset:6912\n"
    "v_fma_f32 v220, v220, s56, v241\n"
    "v_mfma_f32_16x16x32_bf16 v[212:215], v[164:167], a[80:83], v[212:215]\n"
    "v_exp_f32 v216, v216\n"
    "v_mfma_f32_16x16x32_bf16 v[212:215], v[168:171], a[84:87], v[212:215]\n"
    "v_exp_f32 v217, v217\n"
    "v_mfma_f32_16x16x32_bf16 v[212:215], v[172:175], a[88:91], v[212:215]\n"
    "ds_read_b128 v[184:187], v233 offset:576\n"
    "v_exp_f32 v218, v218\n"
    "v_exp_f32 v219, v219\n"
    "v_mfma_f32_16x16x32_bf16 v[212:215], v[176:179], a[92:95], v[212:215]\n"
    "s_add_u32 m0, s96, 12672\n"
    "ds_read_b64 v[254:255], v238 offset:7040\n"
    "v_cvt_pk_bf16_f32 v248, v216, v217\n"
    "v_cvt_pk_bf16_f32 v249, v218, v219\n"
    "v_mfma_f32_16x16x32_bf16 v[216:219], v[148:151], a[96:99], 0\n"
    "buffer_load_dwordx4 v230, s[36:39], s52 offen lds\n"
    "v_mfma_f32_16x16x32_bf16 v[216:219], v[152:155], a[100:103], v[216:219]\n"
    "s_add_u32 s52, s52, 4096\n"
    "ds_read_b128 v[188:191], v233 offset:640\n"
    "v_fma_f32 v221, v221, s56, v241\n"
    "v_mfma_f32_16x16x32_bf16 v[216:219], v[156:159], a[104:107], v[216:219]\n"
    "v_fma_f32 v222, v222, s56, v241\n"
    "v_fma_f32 v223, v223, s56, v241\n"
    "v_mfma_f32_16x16x32_bf16 v[216:219], v[160:163], a[108:111], v[216:219]\n"
    "ds_read_b128 v[192:195], v233 offset:704\n"
    "v_accvgpr_write_b32 a162, v248\n"
    "v_mfma_f32_16x16x32_bf16 v[216:219], v[164:167], a[112:115], v[216:219]\n"
    "v_accvgpr_write_b32 a163, v249\n"
    "v_fma_f32 v224, v224, s56, v240\n"
    "v_mfma_f32_16x16x32_bf16 v[216:219], v[168:171], a[116:119], v[216:219]\n"
    "s_waitcnt lgkmcnt(2)\n"
    "v_exp_f32 v220, v220\n"
    "v_exp_f32 v221, v221\n"
    "v_exp_f32 v222, v222\n"
    "v_exp_f32 v223, v223\n"
    "v_mfma_f32_16x16x32_bf16 v[216:219], v[172:175], a[120:123], v[216:219]\n"
    "s_add_u32 s55, s54, s62\n"
    "buffer_store_dwordx4 v[252:255], v232, s[48:51], s30 offen offset:0 sc1\n"
    "v_mfma_f32_16x16x32_bf16 v[216:219], v[176:179], a[124:127], v[216:219]\n"
    "s_add_u32 s54, s54, 16896\n"
    "v_cvt_pk_bf16_f32 v42, v220, v221\n"
    "v_cvt_pk_bf16_f32 v43, v222, v223\n"
    "v_mfma_f32_16x16x32_bf16 v[220:223], v[148:151], a[32:35], 0\n"
    "s_sub_u32 s97, s54, 84480\n"
    "ds_read_b128 v[196:199], v233 offset:768\n"
    "v_fma_f32 v225, v225, s56, v240\n"
    "v_mfma_f32_16x16x32_bf16 v[220:223], v[152:155], a[36:39], v[220:223]\n"
    "s_min_u32 s54, s54, s97\n"
    "ds_read_b128 v[200:203], v233 offset:832\n"
    "v_fma_f32 v226, v226, s56, v240\n"
    "v_mfma_f32_16x16x32_bf16 v[220:223], v[156:159], a[40:43], v[220:223]\n"
    "s_sub_u32 s97, s54, s66\n"
    "v_fma_f32 v227, v227, s56, v240\n"
    "v_fma_f32 v212, v212, s56, v242\n"
    "v_mfma_f32_16x16x32_bf16 v[220:223], v[160:163], a[44:47], v[220:223]\n"
    "s_add_u32 m0, s59, 2112\n"
    "ds_read_b128 v[204:207], v233 offset:896\n"
    "v_fma_f32 v213, v213, s56, v242\n"
    "v_mfma_f32_16x16x32_bf16 v[220:223], v[164:167], a[48:51], v[220:223]\n"
    "s_mov_b32 s66, s54\n"
    "buffer_load_dwordx4 v230, s[44:47], s58 offen nt lds\n"
    "v_mfma_f32_16x16x32_bf16 v[220:223], v[168:171], a[52:55], v[220:223]\n"
    "s_add_u32 s58, s58, 1024\n"
    "v_exp_f32 v224, v224\n"
    "v_mfma_f32_16x16x32_bf16 v[220:223], v[172:175], a[56:59], v[220:223]\n"
    "v_exp_f32 v225, v225\n"
    "v_mfma_f32_16x16x32_bf16 v[220:223], v[176:179], a[60:63], v[220:223]\n"
    "v_exp_f32 v226, v226\n"
    "v_exp_f32 v227, v227\n"
    "v_mfma_f32_16x16x32_bf16 a[132:135], a[144:147], v[32:35], a[132:135]\n"
    "v_cvt_pk_bf16_f32 v250, v224, v225\n"
    "v_cvt_pk_bf16_f32 v251, v226, v227\n"
    "v_mfma_f32_16x16x32_bf16 v[224:227], v[148:151], a[0:3], 0\n"
    "ds_read_b128 v[208:211], v233 offset:960\n"
    "v_accvgpr_write_b32 a158, v250\n"
    "v_mfma_f32_16x16x32_bf16 v[224:227], v[152:155], a[4:7], v[224:227]\n"
    "v_accvgpr_write_b32 a159, v251\n"
    "v_fma_f32 v214, v214, s56, v242\n"
    "v_mfma_f32_16x16x32_bf16 v[224:227], v[156:159], a[8:11], v[224:227]\n"
    "v_fma_f32 v215, v215, s56, v242\n"
    "v_add_u32 v233, s97, v233\n"
    "v_mfma_f32_16x16x32_bf16 v[224:227], v[160:163], a[12:15], v[224:227]\n"
    "v_exp_f32 v212, v212\n"
    "v_mfma_f32_16x16x32_bf16 v[224:227], v[164:167], a[16:19], v[224:227]\n"
    "v_exp_f32 v213, v213\n"
    "v_mfma_f32_16x16x32_bf16 v[224:227], v[168:171], a[20:23], v[224:227]\n"
    "v_exp_f32 v214, v214\n"
    "v_mfma_f32_16x16x32_bf16 v[224:227], v[172:175], a[24:27], v[224:227]\n"
    "v_exp_f32 v215, v215\n"
    "v_mfma_f32_16x16x32_bf16 v[224:227], v[176:179], a[28:31], v[224:227]\n"
    "v_cvt_pk_bf16_f32 v52, v212, v213\n"
    "v_cvt_pk_bf16_f32 v53, v214, v215\n"
    "v_mfma_f32_16x16x32_bf16 a[128:131], a[144:147], a[148:151], a[128:131]\n"
    "s_waitcnt vmcnt(14) lgkmcnt(0)\n"
    "ds_inc_u32 v228, v236\n"
    "v_mfma_f32_16x16x32_bf16 v[212:215], v[180:183], a[64:67], 0\n"
    "ds_read_b64 v[252:253], v238 offset:6976\n"
    "v_fma_f32 v216, v216, s56, v243\n"
    "v_mfma_f32_16x16x32_bf16 v[212:215], v[184:187], a[68:71], v[212:215]\n"
    "s_add_u32 m0, s59, 3168\n"
    "ds_read_b128 v[148:151], v233 offset:0\n"
    "v_fma_f32 v217, v217, s56, v243\n"
    "v_mfma_f32_16x16x32_bf16 v[212:215], v[188:191], a[72:75], v[212:215]\n"
    "s_add_u32 s61, s61, 1\n"
    "buffer_load_dwordx4 v230, s[44:47], s58 offen nt lds\n"
    "v_mfma_f32_16x16x32_bf16 v[212:215], v[192:195], a[76:79], v[212:215]\n"
    "s_add_u32 s58, s58, 1024\n"
    "ds_read_b64 v[254:255], v238 offset:7104\n"
    "v_fma_f32 v218, v218, s56, v243\n"
    "v_mfma_f32_16x16x32_bf16 v[212:215], v[196:199], a[80:83], v[212:215]\n"
    "ds_read_b128 v[152:155], v233 offset:64\n"
    "v_fma_f32 v219, v219, s56, v243\n"
    "v_mfma_f32_16x16x32_bf16 v[212:215], v[200:203], a[84:87], v[212:215]\n"
    "v_exp_f32 v216, v216\n"
    "v_mfma_f32_16x16x32_bf16 v[212:215], v[204:207], a[88:91], v[212:215]\n"
    "v_exp_f32 v217, v217\n"
    "v_exp_f32 v218, v218\n"
    "v_exp_f32 v219, v219\n"
    "v_mfma_f32_16x16x32_bf16 v[212:215], v[208:211], a[92:95], v[212:215]\n"
    "v_cvt_pk_bf16_f32 v248, v216, v217\n"
    "v_cvt_pk_bf16_f32 v249, v218, v219\n"
    "v_mfma_f32_16x16x32_bf16 v[216:219], v[180:183], a[96:99], 0\n"
    "ds_read_b128 v[156:159], v233 offset:128\n"
    "v_accvgpr_write_b32 a168, v248\n"
    "v_mfma_f32_16x16x32_bf16 v[216:219], v[184:187], a[100:103], v[216:219]\n"
    "s_waitcnt lgkmcnt(2)\n"
    "ds_read_b32 v229, v230\n"
    "v_accvgpr_write_b32 a169, v249\n"
    "v_mfma_f32_16x16x32_bf16 v[216:219], v[188:191], a[104:107], v[216:219]\n"
    "buffer_store_dwordx4 v[252:255], v232, s[48:51], s31 offen offset:0 sc1\n"
    "v_mfma_f32_16x16x32_bf16 v[216:219], v[192:195], a[108:111], v[216:219]\n"
    "s_xor_b32 s35, s35, s33\n"
    "ds_read_b128 v[160:163], v233 offset:192\n"
    "v_fma_f32 v220, v220, s56, v241\n"
    "v_mfma_f32_16x16x32_bf16 v[216:219], v[196:199], a[112:115], v[216:219]\n"
    "s_add_u32 s24, s68, 0\n"
    "v_fma_f32 v221, v221, s56, v241\n"
    "v_fma_f32 v222, v222, s56, v241\n"
    "v_mfma_f32_16x16x32_bf16 v[216:219], v[200:203], a[116:119], v[216:219]\n"
    "s_waitcnt lgkmcnt(1)\n"
    "v_fma_f32 v223, v223, s56, v241\n"
    "v_cmp_gt_u32 vcc, s61, v229\n"
    "v_mfma_f32_16x16x32_bf16 v[216:219], v[204:207], a[120:123], v[216:219]\n"
    "v_exp_f32 v220, v220\n"
    "v_exp_f32 v221, v221\n"
    "v_exp_f32 v222, v222\n"
    "v_exp_f32 v223, v223\n"
    "v_mfma_f32_16x16x32_bf16 v[216:219], v[208:211], a[124:127], v[216:219]\n"
    "s_add_u32 s25, s68, 4096\n"
    "v_cvt_pk_bf16_f32 v48, v220, v221\n"
    "v_cvt_pk_bf16_f32 v49, v222, v223\n"
    "v_mfma_f32_16x16x32_bf16 v[220:223], v[180:183], a[32:35], 0\n"
    "s_cbranch_vccnz L_sp4%=\n"
    "L_rt4%=:\n"
    "ds_read_b128 v[164:167], v233 offset:256\n"
    "v_mfma_f32_16x16x32_bf16 v[220:223], v[184:187], a[36:39], v[220:223]\n"
    "s_add_u32 m0, s55, 0\n"
    "ds_read_b128 v[168:171], v233 offset:320\n"
    "v_fma_f32 v224, v224, s56, v240\n"
    "v_mfma_f32_16x16x32_bf16 v[220:223], v[188:191], a[40:43], v[220:223]\n"
    "s_add_u32 s26, s68, 8192\n"
    "buffer_load_dwordx4 v230, s[36:39], s52 offen lds\n"
    "v_mfma_f32_16x16x32_bf16 v[220:223], v[192:195], a[44:47], v[220:223]\n"
    "s_add_u32 s52, s52, 4096\n"
    "v_fma_f32 v225, v225, s56, v240\n"
    "v_fma_f32 v226, v226, s56, v240\n"
    "v_mfma_f32_16x16x32_bf16 v[220:223], v[196:199], a[48:51], v[220:223]\n"
    "s_add_u32 s27, s68, 12288\n"
    "ds_read_b128 v[172:175], v233 offset:384\n"
    "v_fma_f32 v227, v227, s56, v240\n"
    "v_mfma_f32_16x16x32_bf16 v[220:223], v[200:203], a[52:55], v[220:223]\n"
    "s_add_u32 s28, s68, 16384\n"
    "v_exp_f32 v224, v224\n"
    "v_mfma_f32_16x16x32_bf16 v[220:223], v[204:207], a[56:59], v[220:223]\n"
    "s_add_u32 s29, s68, 20480\n"
    "v_exp_f32 v225, v225\n"
    "v_mfma_f32_16x16x32_bf16 v[220:223], v[208:211], a[60:63], v[220:223]\n"
    "v_exp_f32 v226, v226\n"
    "v_exp_f32 v227, v227\n"
    "v_mfma_f32_16x16x32_bf16 a[136:139], a[144:147], v[44:47], a[136:139]\n"
    "s_add_u32 s30, s68, 24576\n"
    "v_cvt_pk_bf16_f32 v250, v224, v225\n"
    "v_cvt_pk_bf16_f32 v251, v226, v227\n"
    "v_mfma_f32_16x16x32_bf16 v[224:227], v[180:183], a[0:3], 0\n"
    "s_add_u32 m0, s55, 4224\n"
    "ds_read_b128 v[176:179], v233 offset:448\n"
    "v_accvgpr_write_b32 a164, v250\n"
    "v_mfma_f32_16x16x32_bf16 v[224:227], v[184:187], a[4:7], v[224:227]\n"
    "s_add_u32 s31, s68, 28672\n"
    "buffer_load_dwordx4 v230, s[36:39], s52 offen lds\n"
    "v_mfma_f32_16x16x32_bf16 v[224:227], v[188:191], a[8:11], v[224:227]\n"
    "s_add_u32 s52, s52, 4096\n"
    "v_accvgpr_write_b32 a165, v251\n"
    "v_fma_f32 v212, v212, s56, v242\n"
    "v_mfma_f32_16x16x32_bf16 v[224:227], v[192:195], a[12:15], v[224:227]\n"
    "s_mov_b32 s50, s69\n"
    "v_fma_f32 v213, v213, s56, v242\n"
    "v_fma_f32 v214, v214, s56, v242\n"
    "v_mfma_f32_16x16x32_bf16 v[224:227], v[196:199], a[16:19], v[224:227]\n"
    "v_fma_f32 v215, v215, s56, v242\n"
    "v_fma_f32 v216, v216, s56, v243\n"
    "v_mfma_f32_16x16x32_bf16 v[224:227], v[200:203], a[20:23], v[224:227]\n"
    "v_exp_f32 v212, v212\n"
    "v_mfma_f32_16x16x32_bf16 v[224:227], v[204:207], a[24:27], v[224:227]\n"
    "v_exp_f32 v213, v213\n"
    "v_mfma_f32_16x16x32_bf16 v[224:227], v[208:211], a[28:31], v[224:227]\n"
    "v_exp_f32 v214, v214\n"
    "v_exp_f32 v215, v215\n"
    "v_mfma_f32_16x16x32_bf16 a[140:143], a[144:147], a[160:163], a[140:143]\n"
    "s_waitcnt lgkmcnt(0)\n"
    "v_cvt_pk_bf16_f32 v54, v212, v213\n"
    "v_cvt_pk_bf16_f32 v55, v214, v215\n"
    "v_mfma_f32_16x16x32_bf16 v[212:215], v[148:151], a[64:67], 0\n"
    "s_add_u32 m0, s55, 8448\n"
    "ds_read_b128 v[180:183], v233 offset:512\n"
    "v_fma_f32 v217, v217, s56, v243\n"
    "v_mfma_f32_16x16x32_bf16 v[212:215], v[152:155], a[68:71], v[212:215]\n"
    "buffer_load_dwordx4 v230, s[36:39], s52 offen lds\n"
    "v_mfma_f32_16x16x32_bf16 v[212:215], v[156:159], a[72:75], v[212:215]\n"
    "s_add_u32 s52, s52, 4096\n"
    "v_fma_f32 v218, v218, s56, v243\n"
    "v_fma_f32 v219, v219, s56, v243\n"
    "v_mfma_f32_16x16x32_bf16 v[212:215], v[160:163], a[76:79], v[212:215]\n"
    "v_exp_f32 v216, v216\n"
    "v_mfma_f32_16x16x32_bf16 v[212:215], v[164:167], a[80:83], v[212:215]\n"
    "v_exp_f32 v217, v217\n"
    "v_mfma_f32_16x16x32_bf16 v[212:215], v[168:171], a[84:87], v[212:215]\n"
    "v_exp_f32 v218, v218\n"
    "v_exp_f32 v219, v219\n"
    "v_mfma_f32_16x16x32_bf16 v[212:215], v[172:175], a[88:91], v[212:215]\n"
    "s_add_u32 m0, s59, 4224\n"
    "ds_read_b128 v[184:187], v233 offset:576\n"
    "v_cvt_pk_bf16_f32 v248, v216, v217\n"
    "v_cvt_pk_bf16_f32 v249, v218, v219\n"
    "v_mfma_f32_16x16x32_bf16 v[212:215], v[176:179], a[92:95], v[212:215]\n"
    "buffer_load_dwordx4 v230, s[44:47], s58 offen nt lds\n"
    "v_mfma_f32_16x16x32_bf16 v[216:219], v[148:151], a[96:99], 0\n"
    "s_add_u32 s58, s58, 1024\n"
    "ds_read_b128 v[188:191], v233 offset:640\n"
    "v_accvgpr_write_b32 a170, v248\n"
    "v_mfma_f32_16x16x32_bf16 v[216:219], v[152:155], a[100:103], v[216:219]\n"
    "v_accvgpr_write_b32 a171, v249\n"
    "v_fma_f32 v220, v220, s56, v241\n"
    "v_mfma_f32_16x16x32_bf16 v[216:219], v[156:159], a[104:107], v[216:219]\n"
    "v_fma_f32 v221, v221, s56, v241\n"
    "v_fma_f32 v222, v222, s56, v241\n"
    "v_mfma_f32_16x16x32_bf16 v[216:219], v[160:163], a[108:111], v[216:219]\n"
    "ds_read_b128 v[192:195], v233 offset:704\n"
    "v_fma_f32 v223, v223, s56, v241\n"
    "v_mfma_f32_16x16x32_bf16 v[216:219], v[164:167], a[112:115], v[216:219]\n"
    "s_add_u32 m0, s55, 12672\n"
    "v_exp_f32 v220, v220\n"
    "v_mfma_f32_16x16x32_bf16 v[216:219], v[168:171], a[116:119], v[216:219]\n"
    "buffer_load_dwordx4 v230, s[36:39], s52 offen lds\n"
    "v_mfma_f32_16x16x32_bf16 v[216:219], v[172:175], a[120:123], v[216:219]\n"
    "v_exp_f32 v221, v221\n"
    "v_exp_f32 v222, v222\n"
    "v_exp_f32 v223, v223\n"
    "v_mfma_f32_16x16x32_bf16 v[216:219], v[176:179], a[124:127], v[216:219]\n"
    "s_add_u32 s52, s52, 4096\n"
    "v_cvt_pk_bf16_f32 v50, v220, v221\n"
    "v_cvt_pk_bf16_f32 v51, v222, v223\n"
    "v_mfma_f32_16x16x32_bf16 v[220:223], v[148:151], a[32:35], 0\n"
    "s_add_u32 s96, s54, s62\n"
    "ds_read_b128 v[196:199], v233 offset:768\n"
    "v_fma_f32 v224, v224, s56, v240\n"
    "v_mfma_f32_16x16x32_bf16 v[220:223], v[152:155], a[36:39], v[220:223]\n"
    "s_add_u32 s54, s54, 16896\n"
    "ds_read_b128 v[200:203], v233 offset:832\n"
    "v_fma_f32 v225, v225, s56, v240\n"
    "v_mfma_f32_16x16x32_bf16 v[220:223], v[156:159], a[40:43], v[220:223]\n"
    "s_sub_u32 s97, s54, 84480\n"
    "v_fma_f32 v226, v226, s56, v240\n"
    "v_fma_f32 v227, v227, s56, v240\n"
    "v_mfma_f32_16x16x32_bf16 v[220:223], v[160:163], a[44:47], v[220:223]\n"
    "s_min_u32 s54, s54, s97\n"
    "ds_read_b128 v[204:207], v233 offset:896\n"
    "v_fma_f32 v212, v212, s56, v242\n"
    "v_mfma_f32_16x16x32_bf16 v[220:223], v[164:167], a[48:51], v[220:223]\n"
    "s_sub_u32 s97, s54, s66\n"
    "v_exp_f32 v224, v224\n"
    "v_mfma_f32_16x16x32_bf16 v[220:223], v[168:171], a[52:55], v[220:223]\n"
    "s_mov_b32 s66, s54\n"
    "v_exp_f32 v225, v225\n"
    "v_mfma_f32_16x16x32_bf16 v[220:223], v[172:175], a[56:59], v[220:223]\n"
    "v_exp_f32 v226, v226\n"
    "v_mfma_f32_16x16x32_bf16 v[220:223], v[176:179], a[60:63], v[220:223]\n"
    "v_exp_f32 v227, v227\n"
    "v_mfma_f32_16x16x32_bf16 a[132:135], a[144:147], v[40:43], a[132:135]\n"
    "v_cvt_pk_bf16_f32 v250, v224, v225\n"
    "v_cvt_pk_bf16_f32 v251, v226, v227\n"
    "v_mfma_f32_16x16x32_bf16 v[224:227], v[148:151], a[0:3], 0\n"
    "ds_read_b128 v[208:211], v233 offset:960\n"
    "v_accvgpr_write_b32 a166, v250\n"
    "v_mfma_f32_16x16x32_bf16 v[224:227], v[152:155], a[4:7], v[224:227]\n"
    "v_accvgpr_write_b32 a167, v251\n"
    "v_fma_f32 v213, v213, s56, v242\n"
    "v_mfma_f32_16x16x32_bf16 v[224:227], v[156:159], a[8:11], v[224:227]\n"
    "v_fma_f32 v214, v214, s56, v242\n"
    "v_fma_f32 v215, v215, s56, v242\n"
    "v_mfma_f32_16x16x32_bf16 v[224:227], v[160:163], a[12:15], v[224:227]\n"
    "v_exp_f32 v212, v212\n"
    "v_mfma_f32_16x16x32_bf16 v[224:227], v[164:167], a[16:19], v[224:227]\n"
    "v_exp_f32 v213, v213\n"
    "v_mfma_f32_16x16x32_bf16 v[224:227], v[168:171], a[20:23], v[224:227]\n"
    "v_exp_f32 v214, v214\n"
    "v_mfma_f32_16x16x32_bf16 v[224:227], v[172:175], a[24:27], v[224:227]\n"
    "v_exp_f32 v215, v215\n"
    "v_mfma_f32_16x16x32_bf16 v[224:227], v[176:179], a[28:31], v[224:227]\n"
    "v_cvt_pk_bf16_f32 v60, v212, v213\n"
    "v_cvt_pk_bf16_f32 v61, v214, v215\n"
    "v_mfma_f32_16x16x32_bf16 a[128:131], a[144:147], a[156:159], a[128:131]\n"
    "s_waitcnt vmcnt(13) lgkmcnt(0)\n"
    "v_add_u32 v233, s97, v233\n"
    "v_fma_f32 v216, v216, s56, v243\n"
    "v_mfma_f32_16x16x32_bf16 v[212:215], v[180:183], a[64:67], 0\n"
    "ds_inc_u32 v228, v236\n"
    "v_mfma_f32_16x16x32_bf16 v[212:215], v[184:187], a[68:71], v[212:215]\n"
    "s_add_u32 m0, s59, 5280\n"
    "ds_read_b128 v[148:151], v233 offset:0\n"
    "v_fma_f32 v217, v217, s56, v243\n"
    "v_mfma_f32_16x16x32_bf16 v[212:215], v[188:191], a[72:75], v[212:215]\n"
    "s_add_u32 s61, s61, 1\n"
    "buffer_load_dwordx4 v230, s[44:47], s58 offen nt lds\n"
    "v_mfma_f32_16x16x32_bf16 v[212:215], v[192:195], a[76:79], v[212:215]\n"
    "s_add_u32 s58, s58, 1024\n"
    "v_fma_f32 v218, v218, s56, v243\n"
    "v_fma_f32 v219, v219, s56, v243\n"
    "v_mfma_f32_16x16x32_bf16 v[212:215], v[196:199], a[80:83], v[212:215]\n"
    "v_exp_f32 v216, v216\n"
    "v_mfma_f32_16x16x32_bf16 v[212:215], v[200:203], a[84:87], v[212:215]\n"
    "v_exp_f32 v217, v217\n"
    "v_mfma_f32_16x16x32_bf16 v[212:215], v[204:207], a[88:91], v[212:215]\n"
    "ds_read_b128 v[152:155], v233 offset:64\n"
    "v_exp_f32 v218, v218\n"
    "v_exp_f32 v219, v219\n"
    "v_mfma_f32_16x16x32_bf16 v[212:215], v[208:211], a[92:95], v[212:215]\n"
    "v_cvt_pk_bf16_f32 v248, v216, v217\n"
    "v_cvt_pk_bf16_f32 v249, v218, v219\n"
    "v_mfma_f32_16x16x32_bf16 v[216:219], v[180:183], a[96:99], 0\n"
    "ds_read_b128 v[156:159], v233 offset:128\n"
    "v_accvgpr_write_b32 a176, v248\n"
    "v_mfma_f32_16x16x32_bf16 v[216:219], v[184:187], a[100:103], v[216:219]\n"
    "ds_read_b32 v229, v230\n"
    "v_accvgpr_write_b32 a177, v249\n"
    "v_mfma_f32_16x16x32_bf16 v[216:219], v[188:191], a[104:107], v[216:219]\n"
    "ds_read_b128 v[160:163], v233 offset:192\n"
    "v_fma_f32 v220, v220, s56, v241\n"
    "v_mfma_f32_16x16x32_bf16 v[216:219], v[192:195], a[108:111], v[216:219]\n"
    "v_fma_f32 v221, v221, s56, v241\n"
    "v_fma_f32 v222, v222, s56, v241\n"
    "v_mfma_f32_16x16x32_bf16 v[216:219], v[196:199], a[112:115], v[216:219]\n"
    "v_fma_f32 v223, v223, s56, v241\n"
    "v_fma_f32 v224, v224, s56, v240\n"
    "v_mfma_f32_16x16x32_bf16 v[216:219], v[200:203], a[116:119], v[216:219]\n"
    "s_waitcnt lgkmcnt(1)\n"
    "ds_read_b128 v[164:167], v233 offset:256\n"
    "v_cmp_gt_u32 vcc, s61, v229\n"
    "v_mfma_f32_16x16x32_bf16 v[216:219], v[204:207], a[120:123], v[216:219]\n"
    "v_exp_f32 v220, v220\n"
    "v_exp_f32 v221, v221\n"
    "v_exp_f32 v222, v222\n"
    "v_exp_f32 v223, v223\n"
    "v_mfma_f32_16x16x32_bf16 v[216:219], v[208:211], a[124:127], v[216:219]\n"
    "v_cvt_pk_bf16_f32 v56, v220, v221\n"
    "v_cvt_pk_bf16_f32 v57, v222, v223\n"
    "v_mfma_f32_16x16x32_bf16 v[220:223], v[180:183], a[32:35], 0\n"
    "s_cbranch_vccnz L_sp5%=\n"
    "L_rt5%=:\n"
    "ds_read_b128 v[168:171], v233 offset:320\n"
    "v_mfma_f32_16x16x32_bf16 v[220:223], v[184:187], a[36:39], v[220:223]\n"
    "s_add_u32 m0, s96, 0\n"
    "v_fma_f32 v225, v225, s56, v240\n"
    "v_fma_f32 v226, v226, s56, v240\n"
    "v_mfma_f32_16x16x32_bf16 v[220:223], v[188:191], a[40:43], v[220:223]\n"
    "buffer_load_dwordx4 v230, s[36:39], s52 offen lds\n"
    "v_mfma_f32_16x16x32_bf16 v[220:223], v[192:195], a[44:47], v[220:223]\n"
    "s_add_u32 s52, s52, 4096\n"
    "ds_read_b128 v[172:175], v233 offset:384\n"
    "v_fma_f32 v227, v227, s56, v240\n"
    "v_mfma_f32_16x16x32_bf16 v[220:223], v[196:199], a[48:51], v[220:223]\n"
    "v_exp_f32 v224, v224\n"
    "v_mfma_f32_16x16x32_bf16 v[220:223], v[200:203], a[52:55], v[220:223]\n"
    "v_exp_f32 v225, v225\n"
    "v_mfma_f32_16x16x32_bf16 v[220:223], v[204:207], a[56:59], v[220:223]\n"
    "v_exp_f32 v226, v226\n"
    "v_mfma_f32_16x16x32_bf16 v[220:223], v[208:211], a[60:63], v[220:223]\n"
    "v_exp_f32 v227, v227\n"
    "v_mfma_f32_16x16x32_bf16 a[136:139], a[144:147], v[52:55], a[136:139]\n"
    "v_cvt_pk_bf16_f32 v250, v224, v225\n"
    "v_cvt_pk_bf16_f32 v251, v226, v227\n"
    "v_mfma_f32_16x16x32_bf16 v[224:227], v[180:183], a[0:3], 0\n"
    "s_add_u32 m0, s96, 4224\n"
    "ds_read_b128 v[176:179], v233 offset:448\n"
    "v_accvgpr_write_b32 a172, v250\n"
    "v_mfma_f32_16x16x32_bf16 v[224:227], v[184:187], a[4:7], v[224:227]\n"
    "buffer_load_dwordx4 v230, s[36:39], s52 offen lds\n"
    "v_mfma_f32_16x16x32_bf16 v[224:227], v[188:191], a[8:11], v[224:227]\n"
    "s_add_u32 s52, s52, 4096\n"
    "v_accvgpr_write_b32 a173, v251\n"
    "v_fma_f32 v212, v212, s56, v242\n"
    "v_mfma_f32_16x16x32_bf16 v[224:227], v[192:195], a[12:15], v[224:227]\n"
    "v_fma_f32 v213, v213, s56, v242\n"
    "v_fma_f32 v214, v214, s56, v242\n"
    "v_mfma_f32_16x16x32_bf16 v[224:227], v[196:199], a[16:19], v[224:227]\n"
    "v_fma_f32 v215, v215, s56, v242\n"
    "v_fma_f32 v216, v216, s56, v243\n"
    "v_mfma_f32_16x16x32_bf16 v[224:227], v[200:203], a[20:23], v[224:227]\n"
    "v_exp_f32 v212, v212\n"
    "v_mfma_f32_16x16x32_bf16 v[224:227], v[204:207], a[24:27], v[224:227]\n"
    "v_exp_f32 v213, v213\n"
    "v_mfma_f32_16x16x32_bf16 v[224:227], v[208:211], a[28:31], v[224:227]\n"
    "v_exp_f32 v214, v214\n"
    "v_exp_f32 v215, v215\n"
    "v_mfma_f32_16x16x32_bf16 a[140:143], a[144:147], a[168:171], a[140:143]\n"
    "s_waitcnt lgkmcnt(0)\n"
    "v_cvt_pk_bf16_f32 v62, v212, v213\n"
    "v_cvt_pk_bf16_f32 v63, v214, v215\n"
    "v_mfma_f32_16x16x32_bf16 v[212:215], v[148:151], a[64:67], 0\n"
    "s_add_u32 m0, s96, 8448\n"
    "ds_read_b128 v[180:183], v233 offset:512\n"
    "v_fma_f32 v217, v217, s56, v243\n"
    "v_mfma_f32_16x16x32_bf16 v[212:215], v[152:155], a[68:71], v[212:215]\n"
    "buffer_load_dwordx4 v230, s[36:39], s52 offen lds\n"
    "v_mfma_f32_16x16x32_bf16 v[212:215], v[156:159], a[72:75], v[212:215]\n"
    "s_add_u32 s52, s52, 4096\n"
    "v_fma_f32 v218, v218, s56, v243\n"
    "v_fma_f32 v219, v219, s56, v243\n"
    "v_mfma_f32_16x16x32_bf16 v[212:215], v[160:163], a[76:79], v[212:215]\n"
    "v_exp_f32 v216, v216\n"
    "v_mfma_f32_16x16x32_bf16 v[212:215], v[164:167], a[80:83], v[212:215]\n"
    "v_exp_f32 v217, v217\n"
    "v_mfma_f32_16x16x32_bf16 v[212:215], v[168:171], a[84:87], v[212:215]\n"
    "v_exp_f32 v218, v218\n"
    "v_mfma_f32_16x16x32_bf16 v[212:215], v[172:175], a[88:91], v[212:215]\n"
    "ds_read_b128 v[184:187], v233 offset:576\n"
    "v_exp_f32 v219, v219\n"
    "v_mfma_f32_16x16x32_bf16 v[212:215], v[176:179], a[92:95], v[212:215]\n"
    "s_add_u32 m0, s96, 12672\n"
    "v_cvt_pk_bf16_f32 v248, v216, v217\n"
    "v_cvt_pk_bf16_f32 v249, v218, v219\n"
    "v_mfma_f32_16x16x32_bf16 v[216:219], v[148:151], a[96:99], 0\n"
    "buffer_load_dwordx4 v230, s[36:39], s52 offen lds\n"
    "v_mfma_f32_16x16x32_bf16 v[216:219], v[152:155], a[100:103], v[216:219]\n"
    "s_add_u32 s52, s52, 4096\n"
    "ds_read_b128 v[188:191], v233 offset:640\n"
    "v_accvgpr_write_b32 a178, v248\n"
    "v_mfma_f32_16x16x32_bf16 v[216:219], v[156:159], a[104:107], v[216:219]\n"
    "ds_read_b128 v[192:195], v233 offset:704\n"
    "v_accvgpr_write_b32 a179, v249\n"
    "v_mfma_f32_16x16x32_bf16 v[216:219], v[160:163], a[108:111], v[216:219]\n"
    "v_fma_f32 v220, v220, s56, v241\n"
    "v_fma_f32 v221, v221, s56, v241\n"
    "v_mfma_f32_16x16x32_bf16 v[216:219], v[164:167], a[112:115], v[216:219]\n"
    "v_fma_f32 v222, v222, s56, v241\n"
    "v_fma_f32 v223, v223, s56, v241\n"
    "v_mfma_f32_16x16x32_bf16 v[216:219], v[168:171], a[116:119], v[216:219]\n"
    "v_exp_f32 v220, v220\n"
    "v_mfma_f32_16x16x32_bf16 v[216:219], v[172:175], a[120:123], v[216:219]\n"
    "v_exp_f32 v221, v221\n"
    "v_exp_f32 v222, v222\n"
    "v_exp_f32 v223, v223\n"
    "v_mfma_f32_16x16x32_bf16 v[216:219], v[176:179], a[124:127], v[216:219]\n"
    "s_add_u32 s55, s54, s62\n"
    "v_cvt_pk_bf16_f32 v58, v220, v221\n"
    "v_cvt_pk_bf16_f32 v59, v222, v223\n"
    "v_mfma_f32_16x16x32_bf16 v[220:223], v[148:151], a[32:35], 0\n"
    "s_add_u32 s54, s54, 16896\n"
    "ds_read_b128 v[196:199], v233 offset:768\n"
    "v_fma_f32 v224, v224, s56, v240\n"
    "v_mfma_f32_16x16x32_bf16 v[220:223], v[152:155], a[36:39], v[220:223]\n"
    "s_sub_u32 s97, s54, 84480\n"
    "ds_read_b128 v[200:203], v233 offset:832\n"
    "v_fma_f32 v225, v225, s56, v240\n"
    "v_mfma_f32_16x16x32_bf16 v[220:223], v[156:159], a[40:43], v[220:223]\n"
    "s_min_u32 s54, s54, s97\n"
    "v_fma_f32 v226, v226, s56, v240\n"
    "v_fma_f32 v227, v227, s56, v240\n"
    "v_mfma_f32_16x16x32_bf16 v[220:223], v[160:163], a[44:47], v[220:223]\n"
    "s_sub_u32 s97, s54, s66\n"
    "ds_read_b128 v[204:207], v233 offset:896\n"
    "v_fma_f32 v212, v212, s56, v242\n"
    "v_mfma_f32_16x16x32_bf16 v[220:223], v[164:167], a[48:51], v[220:223]\n"
    "s_mov_b32 s66, s54\n"
    "v_exp_f32 v224, v224\n"
    "v_mfma_f32_16x16x32_bf16 v[220:223], v[168:171], a[52:55], v[220:223]\n"
    "v_exp_f32 v225, v225\n"
    "v_mfma_f32_16x16x32_bf16 v[220:223], v[172:175], a[56:59], v[220:223]\n"
    "v_exp_f32 v226, v226\n"
    "v_mfma_f32_16x16x32_bf16 v[220:223], v[176:179], a[60:63], v[220:223]\n"
    "v_exp_f32 v227, v227\n"
    "v_mfma_f32_16x16x32_bf16 a[132:135], a[144:147], v[48:51], a[132:135]\n"
    "v_cvt_pk_bf16_f32 v250, v224, v225\n"
    "v_cvt_pk_bf16_f32 v251, v226, v227\n"
    "v_mfma_f32_16x16x32_bf16 v[224:227], v[148:151], a[0:3], 0\n"
    "ds_read_b128 v[208:211], v233 offset:960\n"
    "v_accvgpr_write_b32 a174, v250\n"
    "v_mfma_f32_16x16x32_bf16 v[224:227], v[152:155], a[4:7], v[224:227]\n"
    "v_accvgpr_write_b32 a175, v251\n"
    "v_fma_f32 v213, v213, s56, v242\n"
    "v_mfma_f32_16x16x32_bf16 v[224:227], v[156:159], a[8:11], v[224:227]\n"
    "v_fma_f32 v214, v214, s56, v242\n"
    "v_fma_f32 v215, v215, s56, v242\n"
    "v_mfma_f32_16x16x32_bf16 v[224:227], v[160:163], a[12:15], v[224:227]\n"
    "v_exp_f32 v212, v212\n"
    "v_mfma_f32_16x16x32_bf16 v[224:227], v[164:167], a[16:19], v[224:227]\n"
    "v_exp_f32 v213, v213\n"
    "v_mfma_f32_16x16x32_bf16 v[224:227], v[168:171], a[20:23], v[224:227]\n"
    "v_exp_f32 v214, v214\n"
    "v_mfma_f32_16x16x32_bf16 v[224:227], v[172:175], a[24:27], v[224:227]\n"
    "v_exp_f32 v215, v215\n"
    "v_mfma_f32_16x16x32_bf16 v[224:227], v[176:179], a[28:31], v[224:227]\n"
    "v_cvt_pk_bf16_f32 v68, v212, v213\n"
    "v_cvt_pk_bf16_f32 v69, v214, v215\n"
    "v_mfma_f32_16x16x32_bf16 a[128:131], a[144:147], a[164:167], a[128:131]\n"
    "s_waitcnt vmcnt(14) lgkmcnt(0)\n"
    "v_add_u32 v233, s97, v233\n"
    "v_fma_f32 v216, v216, s56, v243\n"
    "v_mfma_f32_16x16x32_bf16 v[212:215], v[180:183], a[64:67], 0\n"
    "ds_inc_u32 v228, v236\n"
    "v_mfma_f32_16x16x32_bf16 v[212:215], v[184:187], a[68:71], v[212:215]\n"
    "s_add_u32 m0, s59, 6336\n"
    "ds_read_b128 v[148:151], v233 offset:0\n"
    "v_fma_f32 v217, v217, s56, v243\n"
    "v_mfma_f32_16x16x32_bf16 v[212:215], v[188:191], a[72:75], v[212:215]\n"
    "s_add_u32 s61, s61, 1\n"
    "buffer_load_dwordx4 v230, s[44:47], s58 offen nt lds\n"
    "v_mfma_f32_16x16x32_bf16 v[212:215], v[192:195], a[76:79], v[212:215]\n"
    "s_add_u32 s58, s58, 1024\n"
    "v_fma_f32 v218, v218, s56, v243\n"
    "v_fma_f32 v219, v219, s56, v243\n"
    "v_mfma_f32_16x16x32_bf16 v[212:215], v[196:199], a[80:83], v[212:215]\n"
    "v_exp_f32 v216, v216\n"
    "v_mfma_f32_16x16x32_bf16 v[212:215], v[200:203], a[84:87], v[212:215]\n"
    "v_exp_f32 v217, v217\n"
    "v_mfma_f32_16x16x32_bf16 v[212:215], v[204:207], a[88:91], v[212:215]\n"
    "ds_read_b128 v[152:155], v233 offset:64\n"
    "v_exp_f32 v218, v218\n"
    "v_exp_f32 v219, v219\n"
    "v_mfma_f32_16x16x32_bf16 v[212:215], v[208:211], a[92:95], v[212:215]\n"
    "v_cvt_pk_bf16_f32 v248, v216, v217\n"
    "v_cvt_pk_bf16_f32 v249, v218, v219\n"
    "v_mfma_f32_16x16x32_bf16 v[216:219], v[180:183], a[96:99], 0\n"
    "ds_read_b128 v[156:159], v233 offset:128\n"
    "v_accvgpr_write_b32 a184, v248\n"
    "v_mfma_f32_16x16x32_bf16 v[216:219], v[184:187], a[100:103], v[216:219]\n"
    "ds_read_b32 v229, v230\n"
    "v_accvgpr_write_b32 a185, v249\n"
    "v_mfma_f32_16x16x32_bf16 v[216:219], v[188:191], a[104:107], v[216:219]\n"
    "ds_read_b128 v[160:163], v233 offset:192\n"
    "v_fma_f32 v220, v220, s56, v241\n"
    "v_mfma_f32_16x16x32_bf16 v[216:219], v[192:195], a[108:111], v[216:219]\n"
    "v_fma_f32 v221, v221, s56, v241\n"
    "v_fma_f32 v222, v222, s56, v241\n"
    "v_mfma_f32_16x16x32_bf16 v[216:219], v[196:199], a[112:115], v[216:219]\n"
    "v_fma_f32 v223, v223, s56, v241\n"
    "v_fma_f32 v224, v224, s56, v240\n"
    "v_mfma_f32_16x16x32_bf16 v[216:219], v[200:203], a[116:119], v[216:219]\n"
    "s_waitcnt lgkmcnt(1)\n"
    "ds_read_b128 v[164:167], v233 offset:256\n"
    "v_cmp_gt_u32 vcc, s61, v229\n"
    "v_mfma_f32_16x16x32_bf16 v[216:219], v[204:207], a[120:123], v[216:219]\n"
    "v_exp_f32 v220, v220\n"
    "v_exp_f32 v221, v221\n"
    "v_exp_f32 v222, v222\n"
    "v_exp_f32 v223, v223\n"
    "v_mfma_f32_16x16x32_bf16 v[216:219], v[208:211], a[124:127], v[216:219]\n"
    "v_cvt_pk_bf16_f32 v64, v220, v221\n"
    "v_cvt_pk_bf16_f32 v65, v222, v223\n"
    "v_mfma_f32_16x16x32_bf16 v[220:223], v[180:183], a[32:35], 0\n"
    "s_cbranch_vccnz L_sp6%=\n"
    "L_rt6%=:\n"
    "ds_read_b128 v[168:171], v233 offset:320\n"
    "v_mfma_f32_16x16x32_bf16 v[220:223], v[184:187], a[36:39], v[220:223]\n"
    "s_add_u32 m0, s55, 0\n"
    "v_fma_f32 v225, v225, s56, v240\n"
    "v_fma_f32 v226, v226, s56, v240\n"
    "v_mfma_f32_16x16x32_bf16 v[220:223], v[188:191], a[40:43], v[220:223]\n"
    "buffer_load_dwordx4 v230, s[36:39], s52 offen lds\n"
    "v_mfma_f32_16x16x32_bf16 v[220:223], v[192:195], a[44:47], v[220:223]\n"
    "s_add_u32 s52, s52, 4096\n"
    "ds_read_b128 v[172:175], v233 offset:384\n"
    "v_fma_f32 v227, v227, s56, v240\n"
    "v_mfma_f32_16x16x32_bf16 v[220:223], v[196:199], a[48:51], v[220:223]\n"
    "v_exp_f32 v224, v224\n"
    "v_mfma_f32_16x16x32_bf16 v[220:223], v[200:203], a[52:55], v[220:223]\n"
    "v_exp_f32 v225, v225\n"
    "v_mfma_f32_16x16x32_bf16 v[220:223], v[204:207], a[56:59], v[220:223]\n"
    "v_exp_f32 v226, v226\n"
    "v_mfma_f32_16x16x32_bf16 v[220:223], v[208:211], a[60:63], v[220:223]\n"
    "v_exp_f32 v227, v227\n"
    "v_mfma_f32_16x16x32_bf16 a[136:139], a[144:147], v[60:63], a[136:139]\n"
    "v_cvt_pk_bf16_f32 v250, v224, v225\n"
    "v_cvt_pk_bf16_f32 v251, v226, v227\n"
    "v_mfma_f32_16x16x32_bf16 v[224:227], v[180:183], a[0:3], 0\n"
    "s_add_u32 m0, s55, 4224\n"
    "ds_read_b128 v[176:179], v233 offset:448\n"
    "v_accvgpr_write_b32 a180, v250\n"
    "v_mfma_f32_16x16x32_bf16 v[224:227], v[184:187], a[4:7], v[224:227]\n"
    "buffer_load_dwordx4 v230, s[36:39], s52 offen lds\n"
    "v_mfma_f32_16x16x32_bf16 v[224:227], v[188:191], a[8:11], v[224:227]\n"
    "s_add_u32 s52, s52, 4096\n"
    "v_accvgpr_write_b32 a181, v251\n"
    "v_fma_f32 v212, v212, s56, v242\n"
    "v_mfma_f32_16x16x32_bf16 v[224:227], v[192:195], a[12:15], v[224:227]\n"
    "v_fma_f32 v213, v213, s56, v242\n"
    "v_fma_f32 v214, v214, s56, v242\n"
    "v_mfma_f32_16x16x32_bf16 v[224:227], v[196:199], a[16:19], v[224:227]\n"
    "v_fma_f32 v215, v215, s56, v242\n"
    "v_fma_f32 v216, v216, s56, v243\n"
    "v_mfma_f32_16x16x32_bf16 v[224:227], v[200:203], a[20:23], v[224:227]\n"
    "v_exp_f32 v212, v212\n"
    "v_mfma_f32_16x16x32_bf16 v[224:227], v[204:207], a[24:27], v[224:227]\n"
    "v_exp_f32 v213, v213\n"
    "v_mfma_f32_16x16x32_bf16 v[224:227], v[208:211], a[28:31], v[224:227]\n"
    "v_exp_f32 v214, v214\n"
    "v_exp_f32 v215, v215\n"
    "v_mfma_f32_16x16x32_bf16 a[140:143], a[144:147], a[176:179], a[140:143]\n"
    "s_waitcnt lgkmcnt(0)\n"
    "v_cvt_pk_bf16_f32 v70, v212, v213\n"
    "v_cvt_pk_bf16_f32 v71, v214, v215\n"
    "v_mfma_f32_16x16x32_bf16 v[212:215], v[148:151], a[64:67], 0\n"
    "s_add_u32 m0, s55, 8448\n"
    "ds_read_b128 v[180:183], v233 offset:512\n"
    "v_fma_f32 v217, v217, s56, v243\n"
    "v_mfma_f32_16x16x32_bf16 v[212:215], v[152:155], a[68:71], v[212:215]\n"
    "buffer_load_dwordx4 v230, s[36:39], s52 offen lds\n"
    "v_mfma_f32_16x16x32_bf16 v[212:215], v[156:159], a[72:75], v[212:215]\n"
    "s_add_u32 s52, s52, 4096\n"
    "v_fma_f32 v218, v218, s56, v243\n"
    "v_fma_f32 v219, v219, s56, v243\n"
    "v_mfma_f32_16x16x32_bf16 v[212:215], v[160:163], a[76:79], v[212:215]\n"
    "v_exp_f32 v216, v216\n"
    "v_mfma_f32_16x16x32_bf16 v[212:215], v[164:167], a[80:83], v[212:215]\n"
    "v_exp_f32 v217, v217\n"
    "v_mfma_f32_16x16x32_bf16 v[212:215], v[168:171], a[84:87], v[212:215]\n"
    "v_exp_f32 v218, v218\n"
    "v_exp_f32 v219, v219\n"
    "v_mfma_f32_16x16x32_bf16 v[212:215], v[172:175], a[88:91], v[212:215]\n"
    "s_add_u32 m0, s59, 7392\n"
    "ds_read_b128 v[184:187], v233 offset:576\n"
    "v_cvt_pk_bf16_f32 v248, v216, v217\n"
    "v_cvt_pk_bf16_f32 v249, v218, v219\n"
    "v_mfma_f32_16x16x32_bf16 v[212:215], v[176:179], a[92:95], v[212:215]\n"
    "buffer_load_dwordx4 v230, s[44:47], s58 offen nt lds\n"
    "v_mfma_f32_16x16x32_bf16 v[216:219], v[148:151], a[96:99], 0\n"
    "s_add_u32 s58, s58, 1024\n"
    "ds_read_b128 v[188:191], v233 offset:640\n"
    "v_accvgpr_write_b32 a186, v248\n"
    "v_mfma_f32_16x16x32_bf16 v[216:219], v[152:155], a[100:103], v[216:219]\n"
    "v_accvgpr_write_b32 a187, v249\n"
    "v_fma_f32 v220, v220, s56, v241\n"
    "v_mfma_f32_16x16x32_bf16 v[216:219], v[156:159], a[104:107], v[216:219]\n"
    "v_fma_f32 v221, v221, s56, v241\n"
    "v_fma_f32 v222, v222, s56, v241\n"
    "v_mfma_f32_16x16x32_bf16 v[216:219], v[160:163], a[108:111], v[216:219]\n"
    "ds_read_b128 v[192:195], v233 offset:704\n"
    "v_fma_f32 v223, v223, s56, v241\n"
    "v_mfma_f32_16x16x32_bf16 v[216:219], v[164:167], a[112:115], v[216:219]\n"
    "s_add_u32 m0, s55, 12672\n"
    "v_exp_f32 v220, v220\n"
    "v_mfma_f32_16x16x32_bf16 v[216:219], v[168:171], a[116:119], v[216:219]\n"
    "buffer_load_dwordx4 v230, s[36:39], s52 offen lds\n"
    "v_mfma_f32_16x16x32_bf16 v[216:219], v[172:175], a[120:123], v[216:219]\n"
    "v_exp_f32 v221, v221\n"
    "v_exp_f32 v222, v222\n"
    "v_exp_f32 v223, v223\n"
    "v_mfma_f32_16x16x32_bf16 v[216:219], v[176:179], a[124:127], v[216:219]\n"
    "s_add_u32 s52, s52, 4096\n"
    "v_cvt_pk_bf16_f32 v66, v220, v221\n"
    "v_cvt_pk_bf16_f32 v67, v222, v223\n"
    "v_mfma_f32_16x16x32_bf16 v[220:223], v[148:151], a[32:35], 0\n"
    "s_add_u32 s96, s54, s62\n"
    "ds_read_b128 v[196:199], v233 offset:768\n"
    "v_fma_f32 v224, v224, s56, v240\n"
    "v_mfma_f32_16x16x32_bf16 v[220:223], v[152:155], a[36:39], v[220:223]\n"
    "s_add_u32 s54, s54, 16896\n"
    "ds_read_b128 v[200:203], v233 offset:832\n"
    "v_fma_f32 v225, v225, s56, v240\n"
    "v_mfma_f32_16x16x32_bf16 v[220:223], v[156:159], a[40:43], v[220:223]\n"
    "s_sub_u32 s97, s54, 84480\n"
    "v_fma_f32 v226, v226, s56, v240\n"
    "v_fma_f32 v227, v227, s56, v240\n"
    "v_mfma_f32_16x16x32_bf16 v[220:223], v[160:163], a[44:47], v[220:223]\n"
    "s_min_u32 s54, s54, s97\n"
    "ds_read_b128 v[204:207], v233 offset:896\n"
    "v_fma_f32 v212, v212, s56, v242\n"
    "v_mfma_f32_16x16x32_bf16 v[220:223], v[164:167], a[48:51], v[220:223]\n"
    "s_sub_u32 s97, s54, s66\n"
    "v_exp_f32 v224, v224\n"
    "v_mfma_f32_16x16x32_bf16 v[220:223], v[168:171], a[52:55], v[220:223]\n"
    "s_mov_b32 s66, s54\n"
    "v_exp_f32 v225, v225\n"
    "v_mfma_f32_16x16x32_bf16 v[220:223], v[172:175], a[56:59], v[220:223]\n"
    "v_exp_f32 v226, v226\n"
    "v_mfma_f32_16x16x32_bf16 v[220:223], v[176:179], a[60:63], v[220:223]\n"
    "v_exp_f32 v227, v227\n"
    "v_mfma_f32_16x16x32_bf16 a[132:135], a[144:147], v[56:59], a[132:135]\n"
    "v_cvt_pk_bf16_f32 v250, v224, v225\n"
    "v_cvt_pk_bf16_f32 v251, v226, v227\n"
    "v_mfma_f32_16x16x32_bf16 v[224:227], v[148:151], a[0:3], 0\n"
    "ds_read_b128 v[208:211], v233 offset:960\n"
    "v_accvgpr_write_b32 a182, v250\n"
    "v_mfma_f32_16x16x32_bf16 v[224:227], v[152:155], a[4:7], v[224:227]\n"
    "v_accvgpr_write_b32 a183, v251\n"
    "v_fma_f32 v213, v213, s56, v242\n"
    "v_mfma_f32_16x16x32_bf16 v[224:227], v[156:159], a[8:11], v[224:227]\n"
    "v_fma_f32 v214, v214, s56, v242\n"
    "v_fma_f32 v215, v215, s56, v242\n"
    "v_mfma_f32_16x16x32_bf16 v[224:227], v[160:163], a[12:15], v[224:227]\n"
    "v_exp_f32 v212, v212\n"
    "v_mfma_f32_16x16x32_bf16 v[224:227], v[164:167], a[16:19], v[224:227]\n"
    "v_exp_f32 v213, v213\n"
    "v_mfma_f32_16x16x32_bf16 v[224:227], v[168:171], a[20:23], v[224:227]\n"
    "v_exp_f32 v214, v214\n"
    "v_mfma_f32_16x16x32_bf16 v[224:227], v[172:175], a[24:27], v[224:227]\n"
    "v_exp_f32 v215, v215\n"
    "v_mfma_f32_16x16x32_bf16 v[224:227], v[176:179], a[28:31], v[224:227]\n"
    "v_cvt_pk_bf16_f32 v76, v212, v213\n"
    "v_cvt_pk_bf16_f32 v77, v214, v215\n"
    "v_mfma_f32_16x16x32_bf16 a[128:131], a[144:147], a[172:175], a[128:131]\n"
    "s_waitcnt vmcnt(11) lgkmcnt(0)\n"
    "v_add_u32 v233, s97, v233\n"
    "v_fma_f32 v216, v216, s56, v243\n"
    "v_mfma_f32_16x16x32_bf16 v[212:215], v[180:183], a[64:67], 0\n"
    "ds_inc_u32 v228, v236\n"
    "v_mfma_f32_16x16x32_bf16 v[212:215], v[184:187], a[68:71], v[212:215]\n"
    "s_add_u32 m0, s59, 8448\n"
    "ds_read_b128 v[148:151], v233 offset:0\n"
    "v_fma_f32 v217, v217, s56, v243\n"
    "v_mfma_f32_16x16x32_bf16 v[212:215], v[188:191], a[72:75], v[212:215]\n"
    "s_add_u32 s61, s61, 1\n"
    "buffer_load_dwordx4 v230, s[44:47], s58 offen nt lds\n"
    "v_mfma_f32_16x16x32_bf16 v[212:215], v[192:195], a[76:79], v[212:215]\n"
    "s_add_u32 s58, s58, 1024\n"
    "v_fma_f32 v218, v218, s56, v243\n"
    "v_fma_f32 v219, v219, s56, v243\n"
    "v_mfma_f32_16x16x32_bf16 v[212:215], v[196:199], a[80:83], v[212:215]\n"
    "ds_read_b128 v[152:155], v233 offset:64\n"
    "v_xor_b32 v232, s35, v232\n"
    "v_mfma_f32_16x16x32_bf16 v[212:215], v[200:203], a[84:87], v[212:215]\n"
    "s_xor_b32 s35, s33, 384\n"
    "v_exp_f32 v216, v216\n"
    "v_mfma_f32_16x16x32_bf16 v[212:215], v[204:207], a[88:91], v[212:215]\n"
    "v_exp_f32 v217, v217\n"
    "v_exp_f32 v218, v218\n"
    "v_exp_f32 v219, v219\n"
    "v_mfma_f32_16x16x32_bf16 v[212:215], v[208:211], a[92:95], v[212:215]\n"
    "v_cvt_pk_bf16_f32 v248, v216, v217\n"
    "v_cvt_pk_bf16_f32 v249, v218, v219\n"
    "v_mfma_f32_16x16x32_bf16 v[216:219], v[180:183], a[96:99], 0\n"
    "ds_read_b128 v[156:159], v233 offset:128\n"
    "v_accvgpr_write_b32 a192, v248\n"
    "v_mfma_f32_16x16x32_bf16 v[216:219], v[184:187], a[100:103], v[216:219]\n"
    "ds_read_b32 v229, v230\n"
    "v_accvgpr_write_b32 a193, v249\n"
    "v_mfma_f32_16x16x32_bf16 v[216:219], v[188:191], a[104:107], v[216:219]\n"
    "ds_read_b128 v[160:163], v233 offset:192\n"
    "v_fma_f32 v220, v220, s56, v241\n"
    "v_mfma_f32_16x16x32_bf16 v[216:219], v[192:195], a[108:111], v[216:219]\n"
    "v_fma_f32 v221, v221, s56, v241\n"
    "v_fma_f32 v222, v222, s56, v241\n"
    "v_mfma_f32_16x16x32_bf16 v[216:219], v[196:199], a[112:115], v[216:219]\n"
    "v_fma_f32 v223, v223, s56, v241\n"
    "v_fma_f32 v224, v224, s56, v240\n"
    "v_mfma_f32_16x16x32_bf16 v[216:219], v[200:203], a[116:119], v[216:219]\n"
    "s_waitcnt lgkmcnt(1)\n"
    "ds_read_b128 v[164:167], v233 offset:256\n"
    "v_cmp_gt_u32 vcc, s61, v229\n"
    "v_mfma_f32_16x16x32_bf16 v[216:219], v[204:207], a[120:123], v[216:219]\n"
    "v_exp_f32 v220, v220\n"
    "v_exp_f32 v221, v221\n"
    "v_exp_f32 v222, v222\n"
    "v_exp_f32 v223, v223\n"
    "v_mfma_f32_16x16x32_bf16 v[216:219], v[208:211], a[124:127], v[216:219]\n"
    "v_cvt_pk_bf16_f32 v72, v220, v221\n"
    "v_cvt_pk_bf16_f32 v73, v222, v223\n"
    "v_mfma_f32_16x16x32_bf16 v[220:223], v[180:183], a[32:35], 0\n"
    "s_cbranch_vccnz L_sp7%=\n"
    "L_rt7%=:\n"
    "ds_read_b128 v[168:171], v233 offset:320\n"
    "v_mfma_f32_16x16x32_bf16 v[220:223], v[184:187], a[36:39], v[220:223]\n"
    "s_add_u32 m0, s96, 0\n"
    "v_fma_f32 v225, v225, s56, v240\n"
    "v_fma_f32 v226, v226, s56, v240\n"
    "v_mfma_f32_16x16x32_bf16 v[220:223], v[188:191], a[40:43], v[220:223]\n"
    "buffer_load_dwordx4 v230, s[36:39], s52 offen lds\n"
    "v_mfma_f32_16x16x32_bf16 v[220:223], v[192:195], a[44:47], v[220:223]\n"
    "s_add_u32 s52, s52, 4096\n"
    "ds_read_b128 v[172:175], v233 offset:384\n"
    "v_fma_f32 v227, v227, s56, v240\n"
    "v_mfma_f32_16x16x32_bf16 v[220:223], v[196:199], a[48:51], v[220:223]\n"
    "v_exp_f32 v224, v224\n"
    "v_mfma_f32_16x16x32_bf16 v[220:223], v[200:203], a[52:55], v[220:223]\n"
    "v_exp_f32 v225, v225\n"
    "v_mfma_f32_16x16x32_bf16 v[220:223], v[204:207], a[56:59], v[220:223]\n"
    "v_exp_f32 v226, v226\n"
    "v_mfma_f32_16x16x32_bf16 v[220:223], v[208:211], a[60:63], v[220:223]\n"
    "v_exp_f32 v227, v227\n"
    "v_mfma_f32_16x16x32_bf16 a[136:139], a[144:147], v[68:71], a[136:139]\n"
    "v_cvt_pk_bf16_f32 v250, v224, v225\n"
    "v_cvt_pk_bf16_f32 v251, v226, v227\n"
    "v_mfma_f32_16x16x32_bf16 v[224:227], v[180:183], a[0:3], 0\n"
    "s_add_u32 m0, s96, 4224\n"
    "ds_read_b128 v[176:179], v233 offset:448\n"
    "v_accvgpr_write_b32 a188, v250\n"
    "v_mfma_f32_16x16x32_bf16 v[224:227], v[184:187], a[4:7], v[224:227]\n"
    "buffer_load_dwordx4 v230, s[36:39], s52 offen lds\n"
    "v_mfma_f32_16x16x32_bf16 v[224:227], v[188:191], a[8:11], v[224:227]\n"
    "s_add_u32 s52, s52, 4096\n"
    "v_accvgpr_write_b32 a189, v251\n"
    "v_fma_f32 v212, v212, s56, v242\n"
    "v_mfma_f32_16x16x32_bf16 v[224:227], v[192:195], a[12:15], v[224:227]\n"
    "v_fma_f32 v213, v213, s56, v242\n"
    "v_fma_f32 v214, v214, s56, v242\n"
    "v_mfma_f32_16x16x32_bf16 v[224:227], v[196:199], a[16:19], v[224:227]\n"
    "v_fma_f32 v215, v215, s56, v242\n"
    "v_fma_f32 v216, v216, s56, v243\n"
    "v_mfma_f32_16x16x32_bf16 v[224:227], v[200:203], a[20:23], v[224:227]\n"
    "v_exp_f32 v212, v212\n"
    "v_mfma_f32_16x16x32_bf16 v[224:227], v[204:207], a[24:27], v[224:227]\n"
    "v_exp_f32 v213, v213\n"
    "v_mfma_f32_16x16x32_bf16 v[224:227], v[208:211], a[28:31], v[224:227]\n"
    "v_exp_f32 v214, v214\n"
    "v_exp_f32 v215, v215\n"
    "v_mfma_f32_16x16x32_bf16 a[140:143], a[144:147], a[184:187], a[140:143]\n"
    "s_waitcnt lgkmcnt(0)\n"
    "v_cvt_pk_bf16_f32 v78, v212, v213\n"
    "v_cvt_pk_bf16_f32 v79, v214, v215\n"
    "v_mfma_f32_16x16x32_bf16 v[212:215], v[148:151], a[64:67], 0\n"
    "s_add_u32 m0, s96, 8448\n"
    "ds_read_b128 v[180:183], v233 offset:512\n"
    "v_fma_f32 v217, v217, s56, v243\n"
    "v_mfma_f32_16x16x32_bf16 v[212:215], v[152:155], a[68:71], v[212:215]\n"
    "buffer_load_dwordx4 v230, s[36:39], s52 offen lds\n"
    "v_mfma_f32_16x16x32_bf16 v[212:215], v[156:159], a[72:75], v[212:215]\n"
    "s_add_u32 s52, s52, 4096\n"
    "v_fma_f32 v218, v218, s56, v243\n"
    "v_fma_f32 v219, v219, s56, v243\n"
    "v_mfma_f32_16x16x32_bf16 v[212:215], v[160:163], a[76:79], v[212:215]\n"
    "v_exp_f32 v216, v216\n"
    "v_mfma_f32_16x16x32_bf16 v[212:215], v[164:167], a[80:83], v[212:215]\n"
    "v_exp_f32 v217, v217\n"
    "v_mfma_f32_16x16x32_bf16 v[212:215], v[168:171], a[84:87], v[212:215]\n"
    "v_exp_f32 v218, v218\n"
    "v_exp_f32 v219, v219\n"
    "v_mfma_f32_16x16x32_bf16 v[212:215], v[172:175], a[88:91], v[212:215]\n"
    "s_add_u32 m0, s59, 9504\n"
    "ds_read_b128 v[184:187], v233 offset:576\n"
    "v_cvt_pk_bf16_f32 v248, v216, v217\n"
    "v_cvt_pk_bf16_f32 v249, v218, v219\n"
    "v_mfma_f32_16x16x32_bf16 v[212:215], v[176:179], a[92:95], v[212:215]\n"
    "buffer_load_dwordx4 v230, s[44:47], s58 offen nt lds\n"
    "v_mfma_f32_16x16x32_bf16 v[216:219], v[148:151], a[96:99], 0\n"
    "s_add_u32 s58, s58, 1024\n"
    "ds_read_b128 v[188:191], v233 offset:640\n"
    "v_accvgpr_write_b32 a194, v248\n"
    "v_mfma_f32_16x16x32_bf16 v[216:219], v[152:155], a[100:103], v[216:219]\n"
    "v_accvgpr_write_b32 a195, v249\n"
    "v_fma_f32 v220, v220, s56, v241\n"
    "v_mfma_f32_16x16x32_bf16 v[216:219], v[156:159], a[104:107], v[216:219]\n"
    "v_fma_f32 v221, v221, s56, v241\n"
    "v_fma_f32 v222, v222, s56, v241\n"
    "v_mfma_f32_16x16x32_bf16 v[216:219], v[160:163], a[108:111], v[216:219]\n"
    "ds_read_b128 v[192:195], v233 offset:704\n"
    "v_fma_f32 v223, v223, s56, v241\n"
    "v_mfma_f32_16x16x32_bf16 v[216:219], v[164:167], a[112:115], v[216:219]\n"
    "s_add_u32 m0, s96, 12672\n"
    "v_exp_f32 v220, v220\n"
    "v_mfma_f32_16x16x32_bf16 v[216:219], v[168:171], a[116:119], v[216:219]\n"
    "buffer_load_dwordx4 v230, s[36:39], s52 offen lds\n"
    "v_mfma_f32_16x16x32_bf16 v[216:219], v[172:175], a[120:123], v[216:219]\n"
    "v_exp_f32 v221, v221\n"
    "v_exp_f32 v222, v222\n"
    "v_exp_f32 v223, v223\n"
    "v_mfma_f32_16x16x32_bf16 v[216:219], v[176:179], a[124:127], v[216:219]\n"
    "s_add_u32 s52, s52, 4096\n"
    "v_cvt_pk_bf16_f32 v74, v220, v221\n"
    "v_cvt_pk_bf16_f32 v75, v222, v223\n"
    "v_mfma_f32_16x16x32_bf16 v[220:223], v[148:151], a[32:35], 0\n"
    "s_add_u32 s55, s54, s62\n"
    "ds_read_b128 v[196:199], v233 offset:768\n"
    "v_fma_f32 v224, v224, s56, v240\n"
    "v_mfma_f32_16x16x32_bf16 v[220:223], v[152:155], a[36:39], v[220:223]\n"
    "s_add_u32 s54, s54, 16896\n"
    "ds_read_b128 v[200:203], v233 offset:832\n"
    "v_fma_f32 v225, v225, s56, v240\n"
    "v_mfma_f32_16x16x32_bf16 v[220:223], v[156:159], a[40:43], v[220:223]\n"
    "s_sub_u32 s97, s54, 84480\n"
    "v_fma_f32 v226, v226, s56, v240\n"
    "v_fma_f32 v227, v227, s56, v240\n"
    "v_mfma_f32_16x16x32_bf16 v[220:223], v[160:163], a[44:47], v[220:223]\n"
    "s_min_u32 s54, s54, s97\n"
    "ds_read_b128 v[204:207], v233 offset:896\n"
    "v_fma_f32 v212, v212, s56, v242\n"
    "v_mfma_f32_16x16x32_bf16 v[220:223], v[164:167], a[48:51], v[220:223]\n"
    "s_sub_u32 s97, s54, s66\n"
    "v_exp_f32 v224, v224\n"
    "v_mfma_f32_16x16x32_bf16 v[220:223], v[168:171], a[52:55], v[220:223]\n"
    "s_mov_b32 s66, s54\n"
    "v_exp_f32 v225, v225\n"
    "v_mfma_f32_16x16x32_bf16 v[220:223], v[172:175], a[56:59], v[220:223]\n"
    "v_exp_f32 v226, v226\n"
    "v_mfma_f32_16x16x32_bf16 v[220:223], v[176:179], a[60:63], v[220:223]\n"
    "v_exp_f32 v227, v227\n"
    "v_mfma_f32_16x16x32_bf16 a[132:135], a[144:147], v[64:67], a[132:135]\n"
    "v_cvt_pk_bf16_f32 v250, v224, v225\n"
    "v_cvt_pk_bf16_f32 v251, v226, v227\n"
    "v_mfma_f32_16x16x32_bf16 v[224:227], v[148:151], a[0:3], 0\n"
    "ds_read_b128 v[208:211], v233 offset:960\n"
    "v_accvgpr_write_b32 a190, v250\n"
    "v_mfma_f32_16x16x32_bf16 v[224:227], v[152:155], a[4:7], v[224:227]\n"
    "v_accvgpr_write_b32 a191, v251\n"
    "v_fma_f32 v213, v213, s56, v242\n"
    "v_mfma_f32_16x16x32_bf16 v[224:227], v[156:159], a[8:11], v[224:227]\n"
    "v_fma_f32 v214, v214, s56, v242\n"
    "v_fma_f32 v215, v215, s56, v242\n"
    "v_mfma_f32_16x16x32_bf16 v[224:227], v[160:163], a[12:15], v[224:227]\n"
    "v_exp_f32 v212, v212\n"
    "v_mfma_f32_16x16x32_bf16 v[224:227], v[164:167], a[16:19], v[224:227]\n"
    "v_exp_f32 v213, v213\n"
    "v_mfma_f32_16x16x32_bf16 v[224:227], v[168:171], a[20:23], v[224:227]\n"
    "v_exp_f32 v214, v214\n"
    "v_mfma_f32_16x16x32_bf16 v[224:227], v[172:175], a[24:27], v[224:227]\n"
    "v_exp_f32 v215, v215\n"
    "v_mfma_f32_16x16x32_bf16 v[224:227], v[176:179], a[28:31], v[224:227]\n"
    "v_cvt_pk_bf16_f32 v88, v212, v213\n"
    "v_cvt_pk_bf16_f32 v89, v214, v215\n"
    "v_mfma_f32_16x16x32_bf16 a[128:131], a[144:147], a[180:183], a[128:131]\n"
    "s_waitcnt vmcnt(12) lgkmcnt(0)\n"
    "v_add_u32 v233, s97, v233\n"
    "v_fma_f32 v216, v216, s56, v243\n"
    "v_mfma_f32_16x16x32_bf16 v[212:215], v[180:183], a[64:67], 0\n"
    "ds_inc_u32 v228, v236\n"
    "v_mfma_f32_16x16x32_bf16 v[212:215], v[184:187], a[68:71], v[212:215]\n"
    "s_add_u32 m0, s59, 10560\n"
    "ds_read_b128 v[148:151], v233 offset:0\n"
    "v_fma_f32 v217, v217, s56, v243\n"
    "v_mfma_f32_16x16x32_bf16 v[212:215], v[188:191], a[72:75], v[212:215]\n"
    "s_add_u32 s61, s61, 1\n"
    "buffer_load_dwordx4 v230, s[44:47], s58 offen nt lds\n"
    "v_mfma_f32_16x16x32_bf16 v[212:215], v[192:195], a[76:79], v[212:215]\n"
    "s_add_u32 s58, s58, 1024\n"
    "v_fma_f32 v218, v218, s56, v243\n"
    "v_fma_f32 v219, v219, s56, v243\n"
    "v_mfma_f32_16x16x32_bf16 v[212:215], v[196:199], a[80:83], v[212:215]\n"
    "v_exp_f32 v216, v216\n"
    "v_mfma_f32_16x16x32_bf16 v[212:215], v[200:203], a[84:87], v[212:215]\n"
    "v_exp_f32 v217, v217\n"
    "v_mfma_f32_16x16x32_bf16 v[212:215], v[204:207], a[88:91], v[212:215]\n"
    "ds_read_b128 v[152:155], v233 offset:64\n"
    "v_exp_f32 v218, v218\n"
    "v_exp_f32 v219, v219\n"
    "v_mfma_f32_16x16x32_bf16 v[212:215], v[208:211], a[92:95], v[212:215]\n"
    "v_cvt_pk_bf16_f32 v248, v216, v217\n"
    "v_cvt_pk_bf16_f32 v249, v218, v219\n"
    "v_mfma_f32_16x16x32_bf16 v[216:219], v[180:183], a[96:99], 0\n"
    "ds_read_b128 v[156:159], v233 offset:128\n"
    "v_accvgpr_write_b32 a196, v248\n"
    "v_mfma_f32_16x16x32_bf16 v[216:219], v[184:187], a[100:103], v[216:219]\n"
    "ds_read_b32 v229, v230\n"
    "v_accvgpr_write_b32 a197, v249\n"
    "v_mfma_f32_16x16x32_bf16 v[216:219], v[188:191], a[104:107], v[216:219]\n"
    "ds_read_b128 v[160:163], v233 offset:192\n"
    "v_fma_f32 v220, v220, s56, v241\n"
    "v_mfma_f32_16x16x32_bf16 v[216:219], v[192:195], a[108:111], v[216:219]\n"
    "v_fma_f32 v221, v221, s56, v241\n"
    "v_fma_f32 v222, v222, s56, v241\n"
    "v_mfma_f32_16x16x32_bf16 v[216:219], v[196:199], a[112:115], v[216:219]\n"
    "v_fma_f32 v223, v223, s56, v241\n"
    "v_fma_f32 v224, v224, s56, v240\n"
    "v_mfma_f32_16x16x32_bf16 v[216:219], v[200:203], a[116:119], v[216:219]\n"
    "s_waitcnt lgkmcnt(1)\n"
    "ds_read_b128 v[164:167], v233 offset:256\n"
    "v_cmp_gt_u32 vcc, s61, v229\n"
    "v_mfma_f32_16x16x32_bf16 v[216:219], v[204:207], a[120:123], v[216:219]\n"
    "v_exp_f32 v220, v220\n"
    "v_exp_f32 v221, v221\n"
    "v_exp_f32 v222, v222\n"
    "v_exp_f32 v223, v223\n"
    "v_mfma_f32_16x16x32_bf16 v[216:219], v[208:211], a[124:127], v[216:219]\n"
    "v_cvt_pk_bf16_f32 v84, v220, v221\n"
    "v_cvt_pk_bf16_f32 v85, v222, v223\n"
    "v_mfma_f32_16x16x32_bf16 v[220:223], v[180:183], a[32:35], 0\n"
    "s_cbranch_vccnz L_sp8%=\n"
    "L_rt8%=:\n"
    "ds_read_b128 v[168:171], v233 offset:320\n"
    "v_mfma_f32_16x16x32_bf16 v[220:223], v[184:187], a[36:39], v[220:223]\n"
    "s_add_u32 m0, s55, 0\n"
    "v_fma_f32 v225, v225, s56, v240\n"
    "v_fma_f32 v226, v226, s56, v240\n"
    "v_mfma_f32_16x16x32_bf16 v[220:223], v[188:191], a[40:43], v[220:223]\n"
    "buffer_load_dwordx4 v230, s[36:39], s52 offen lds\n"
    "v_mfma_f32_16x16x32_bf16 v[220:223], v[192:195], a[44:47], v[220:223]\n"
    "s_add_u32 s52, s52, 4096\n"
    "ds_read_b128 v[172:175], v233 offset:384\n"
    "v_fma_f32 v227, v227, s56, v240\n"
    "v_mfma_f32_16x16x32_bf16 v[220:223], v[196:199], a[48:51], v[220:223]\n"
    "v_exp_f32 v224, v224\n"
    "v_mfma_f32_16x16x32_bf16 v[220:223], v[200:203], a[52:55], v[220:223]\n"
    "v_exp_f32 v225, v225\n"
    "v_mfma_f32_16x16x32_bf16 v[220:223], v[204:207], a[56:59], v[220:223]\n"
    "v_exp_f32 v226, v226\n"
    "v_mfma_f32_16x16x32_bf16 v[220:223], v[208:211], a[60:63], v[220:223]\n"
    "v_exp_f32 v227, v227\n"
    "v_mfma_f32_16x16x32_bf16 a[136:139], a[144:147], v[76:79], a[136:139]\n"
    "v_cvt_pk_bf16_f32 v80, v224, v225\n"
    "v_cvt_pk_bf16_f32 v81, v226, v227\n"
    "v_mfma_f32_16x16x32_bf16 v[224:227], v[180:183], a[0:3], 0\n"
    "s_add_u32 m0, s55, 4224\n"
    "ds_read_b128 v[176:179], v233 offset:448\n"
    "v_fma_f32 v212, v212, s56, v242\n"
    "v_mfma_f32_16x16x32_bf16 v[224:227], v[184:187], a[4:7], v[224:227]\n"
    "buffer_load_dwordx4 v230, s[36:39], s52 offen lds\n"
    "v_mfma_f32_16x16x32_bf16 v[224:227], v[188:191], a[8:11], v[224:227]\n"
    "s_add_u32 s52, s52, 4096\n"
    "v_fma_f32 v213, v213, s56, v242\n"
    "v_fma_f32 v214, v214, s56, v242\n"
    "v_mfma_f32_16x16x32_bf16 v[224:227], v[192:195], a[12:15], v[224:227]\n"
    "v_fma_f32 v215, v215, s56, v242\n"
    "v_fma_f32 v216, v216, s56, v243\n"
    "v_mfma_f32_16x16x32_bf16 v[224:227], v[196:199], a[16:19], v[224:227]\n"
    "v_exp_f32 v212, v212\n"
    "v_mfma_f32_16x16x32_bf16 v[224:227], v[200:203], a[20:23], v[224:227]\n"
    "v_exp_f32 v213, v213\n"
    "v_mfma_f32_16x16x32_bf16 v[224:227], v[204:207], a[24:27], v[224:227]\n"
    "v_exp_f32 v214, v214\n"
    "v_mfma_f32_16x16x32_bf16 v[224:227], v[208:211], a[28:31], v[224:227]\n"
    "v_exp_f32 v215, v215\n"
    "v_mfma_f32_16x16x32_bf16 a[140:143], a[144:147], a[192:195], a[140:143]\n"
    "s_waitcnt lgkmcnt(0)\n"
    "v_cvt_pk_bf16_f32 v90, v212, v213\n"
    "v_cvt_pk_bf16_f32 v91, v214, v215\n"
    "v_mfma_f32_16x16x32_bf16 v[212:215], v[148:151], a[64:67], 0\n"
    "s_add_u32 m0, s55, 8448\n"
    "ds_read_b128 v[180:183], v233 offset:512\n"
    "v_fma_f32 v217, v217, s56, v243\n"
    "v_mfma_f32_16x16x32_bf16 v[212:215], v[152:155], a[68:71], v[212:215]\n"
    "buffer_load_dwordx4 v230, s[36:39], s52 offen lds\n"
    "v_mfma_f32_16x16x32_bf16 v[212:215], v[156:159], a[72:75], v[212:215]\n"
    "s_add_u32 s52, s52, 4096\n"
    "v_fma_f32 v218, v218, s56, v243\n"
    "v_fma_f32 v219, v219, s56, v243\n"
    "v_mfma_f32_16x16x32_bf16 v[212:215], v[160:163], a[76:79], v[212:215]\n"
    "v_exp_f32 v216, v216\n"
    "v_mfma_f32_16x16x32_bf16 v[212:215], v[164:167], a[80:83], v[212:215]\n"
    "v_exp_f32 v217, v217\n"
    "v_mfma_f32_16x16x32_bf16 v[212:215], v[168:171], a[84:87], v[212:215]\n"
    "v_exp_f32 v218, v218\n"
    "v_mfma_f32_16x16x32_bf16 v[212:215], v[172:175], a[88:91], v[212:215]\n"
    "ds_read_b128 v[184:187], v233 offset:576\n"
    "v_exp_f32 v219, v219\n"
    "v_mfma_f32_16x16x32_bf16 v[212:215], v[176:179], a[92:95], v[212:215]\n"
    "s_add_u32 m0, s55, 12672\n"
    "v_cvt_pk_bf16_f32 v250, v216, v217\n"
    "v_cvt_pk_bf16_f32 v251, v218, v219\n"
    "v_mfma_f32_16x16x32_bf16 v[216:219], v[148:151], a[96:99], 0\n"
    "buffer_load_dwordx4 v230, s[36:39], s52 offen lds\n"
    "v_mfma_f32_16x16x32_bf16 v[216:219], v[152:155], a[100:103], v[216:219]\n"
    "s_add_u32 s52, s52, 4096\n"
    "ds_read_b128 v[188:191], v233 offset:640\n"
    "v_accvgpr_write_b32 a198, v250\n"
    "v_mfma_f32_16x16x32_bf16 v[216:219], v[156:159], a[104:107], v[216:219]\n"
    "ds_read_b128 v[192:195], v233 offset:704\n"
    "v_accvgpr_write_b32 a199, v251\n"
    "v_mfma_f32_16x16x32_bf16 v[216:219], v[160:163], a[108:111], v[216:219]\n"
    "v_fma_f32 v220, v220, s56, v241\n"
    "v_fma_f32 v221, v221, s56, v241\n"
    "v_mfma_f32_16x16x32_bf16 v[216:219], v[164:167], a[112:115], v[216:219]\n"
    "v_fma_f32 v222, v222, s56, v241\n"
    "v_fma_f32 v223, v223, s56, v241\n"
    "v_mfma_f32_16x16x32_bf16 v[216:219], v[168:171], a[116:119], v[216:219]\n"
    "v_exp_f32 v220, v220\n"
    "v_mfma_f32_16x16x32_bf16 v[216:219], v[172:175], a[120:123], v[216:219]\n"
    "v_exp_f32 v221, v221\n"
    "v_exp_f32 v222, v222\n"
    "v_exp_f32 v223, v223\n"
    "v_mfma_f32_16x16x32_bf16 v[216:219], v[176:179], a[124:127], v[216:219]\n"
    "s_add_u32 s96, s54, s62\n"
    "v_cvt_pk_bf16_f32 v86, v220, v221\n"
    "v_cvt_pk_bf16_f32 v87, v222, v223\n"
    "v_mfma_f32_16x16x32_bf16 v[220:223], v[148:151], a[32:35], 0\n"
    "s_add_u32 s54, s54, 16896\n"
    "ds_read_b128 v[196:199], v233 offset:768\n"
    "v_fma_f32 v224, v224, s56, v240\n"
    "v_mfma_f32_16x16x32_bf16 v[220:223], v[152:155], a[36:39], v[220:223]\n"
    "s_sub_u32 s97, s54, 84480\n"
    "ds_read_b128 v[200:203], v233 offset:832\n"
    "v_fma_f32 v225, v225, s56, v240\n"
    "v_mfma_f32_16x16x32_bf16 v[220:223], v[156:159], a[40:43], v[220:223]\n"
    "s_min_u32 s54, s54, s97\n"
    "v_fma_f32 v226, v226, s56, v240\n"
    "v_fma_f32 v227, v227, s56, v240\n"
    "v_mfma_f32_16x16x32_bf16 v[220:223], v[160:163], a[44:47], v[220:223]\n"
    "s_sub_u32 s97, s54, s66\n"
    "ds_read_b128 v[204:207], v233 offset:896\n"
    "v_fma_f32 v212, v212, s56, v242\n"
    "v_mfma_f32_16x16x32_bf16 v[220:223], v[164:167], a[48:51], v[220:223]\n"
    "s_mov_b32 s66, s54\n"
    "v_exp_f32 v224, v224\n"
    "v_mfma_f32_16x16x32_bf16 v[220:223], v[168:171], a[52:55], v[220:223]\n"
    "v_exp_f32 v225, v225\n"
    "v_mfma_f32_16x16x32_bf16 v[220:223], v[172:175], a[56:59], v[220:223]\n"
    "v_exp_f32 v226, v226\n"
    "v_mfma_f32_16x16x32_bf16 v[220:223], v[176:179], a[60:63], v[220:223]\n"
    "v_exp_f32 v227, v227\n"
    "v_mfma_f32_16x16x32_bf16 a[132:135], a[144:147], v[72:75], a[132:135]\n"
    "v_cvt_pk_bf16_f32 v82, v224, v225\n"
    "v_cvt_pk_bf16_f32 v83, v226, v227\n"
    "v_mfma_f32_16x16x32_bf16 v[224:227], v[148:151], a[0:3], 0\n"
    "ds_read_b128 v[208:211], v233 offset:960\n"
    "v_fma_f32 v213, v213, s56, v242\n"
    "v_mfma_f32_16x16x32_bf16 v[224:227], v[152:155], a[4:7], v[224:227]\n"
    "v_fma_f32 v214, v214, s56, v242\n"
    "v_fma_f32 v215, v215, s56, v242\n"
    "v_mfma_f32_16x16x32_bf16 v[224:227], v[156:159], a[8:11], v[224:227]\n"
    "v_exp_f32 v212, v212\n"
    "v_mfma_f32_16x16x32_bf16 v[224:227], v[160:163], a[12:15], v[224:227]\n"
    "v_exp_f32 v213, v213\n"
    "v_mfma_f32_16x16x32_bf16 v[224:227], v[164:167], a[16:19], v[224:227]\n"
    "v_exp_f32 v214, v214\n"
    "v_mfma_f32_16x16x32_bf16 v[224:227], v[168:171], a[20:23], v[224:227]\n"
    "v_exp_f32 v215, v215\n"
    "v_mfma_f32_16x16x32_bf16 v[224:227], v[172:175], a[24:27], v[224:227]\n"
    "v_cvt_pk_bf16_f32 v248, v212, v213\n"
    "v_cvt_pk_bf16_f32 v249, v214, v215\n"
    "v_mfma_f32_16x16x32_bf16 v[224:227], v[176:179], a[28:31], v[224:227]\n"
    "v_add_u32 v233, s97, v233\n"
    "v_accvgpr_write_b32 a204, v248\n"
    "v_mfma_f32_16x16x32_bf16 a[128:131], a[144:147], a[188:191], a[128:131]\n"
    "s_waitcnt vmcnt(11) lgkmcnt(0)\n"
    "ds_inc_u32 v228, v236\n"
    "v_mfma_f32_16x16x32_bf16 v[212:215], v[180:183], a[64:67], 0\n"
    "ds_read_b128 v[148:151], v233 offset:0\n"
    "v_accvgpr_write_b32 a205, v249\n"
    "v_mfma_f32_16x16x32_bf16 v[212:215], v[184:187], a[68:71], v[212:215]\n"
    "s_add_u32 m0, s59, 11616\n"
    "v_fma_f32 v216, v216, s56, v243\n"
    "v_fma_f32 v217, v217, s56, v243\n"
    "v_mfma_f32_16x16x32_bf16 v[212:215], v[188:191], a[72:75], v[212:215]\n"
    "s_add_u32 s61, s61, 1\n"
    "buffer_load_dwordx4 v230, s[44:47], s58 offen nt lds\n"
    "v_mfma_f32_16x16x32_bf16 v[212:215], v[192:195], a[76:79], v[212:215]\n"
    "s_add_u32 s58, s58, 1024\n"
    "v_fma_f32 v218, v218, s56, v243\n"
    "v_fma_f32 v219, v219, s56, v243\n"
    "v_mfma_f32_16x16x32_bf16 v[212:215], v[196:199], a[80:83], v[212:215]\n"
    "v_exp_f32 v216, v216\n"
    "v_mfma_f32_16x16x32_bf16 v[212:215], v[200:203], a[84:87], v[212:215]\n"
    "v_exp_f32 v217, v217\n"
    "v_mfma_f32_16x16x32_bf16 v[212:215], v[204:207], a[88:91], v[212:215]\n"
    "ds_read_b128 v[152:155], v233 offset:64\n"
    "v_exp_f32 v218, v218\n"
    "v_exp_f32 v219, v219\n"
    "v_mfma_f32_16x16x32_bf16 v[212:215], v[208:211], a[92:95], v[212:215]\n"
    "v_cvt_pk_bf16_f32 v96, v216, v217\n"
    "v_cvt_pk_bf16_f32 v97, v218, v219\n"
    "v_mfma_f32_16x16x32_bf16 v[216:219], v[180:183], a[96:99], 0\n"
    "ds_read_b128 v[156:159], v233 offset:128\n"
    "v_fma_f32 v220, v220, s56, v241\n"
    "v_mfma_f32_16x16x32_bf16 v[216:219], v[184:187], a[100:103], v[216:219]\n"
    "ds_read_b32 v229, v230\n"
    "v_fma_f32 v221, v221, s56, v241\n"
    "v_mfma_f32_16x16x32_bf16 v[216:219], v[188:191], a[104:107], v[216:219]\n"
    "ds_read_b128 v[160:163], v233 offset:192\n"
    "v_fma_f32 v222, v222, s56, v241\n"
    "v_mfma_f32_16x16x32_bf16 v[216:219], v[192:195], a[108:111], v[216:219]\n"
    "v_fma_f32 v223, v223, s56, v241\n"
    "v_fma_f32 v224, v224, s56, v240\n"
    "v_mfma_f32_16x16x32_bf16 v[216:219], v[196:199], a[112:115], v[216:219]\n"
    "v_exp_f32 v220, v220\n"
    "v_mfma_f32_16x16x32_bf16 v[216:219], v[200:203], a[116:119], v[216:219]\n"
    "s_waitcnt lgkmcnt(1)\n"
    "ds_read_b128 v[164:167], v233 offset:256\n"
    "v_cmp_gt_u32 vcc, s61, v229\n"
    "v_mfma_f32_16x16x32_bf16 v[216:219], v[204:207], a[120:123], v[216:219]\n"
    "v_exp_f32 v221, v221\n"
    "v_exp_f32 v222, v222\n"
    "v_exp_f32 v223, v223\n"
    "v_mfma_f32_16x16x32_bf16 v[216:219], v[208:211], a[124:127], v[216:219]\n"
    "v_cvt_pk_bf16_f32 v250, v220, v221\n"
    "v_cvt_pk_bf16_f32 v251, v222, v223\n"
    "v_mfma_f32_16x16x32_bf16 v[220:223], v[180:183], a[32:35], 0\n"
    "s_cbranch_vccnz L_sp9%=\n"
    "L_rt9%=:\n"
    "ds_read_b128 v[168:171], v233 offset:320\n"
    "v_mfma_f32_16x16x32_bf16 v[220:223], v[184:187], a[36:39], v[220:223]\n"
    "s_add_u32 m0, s96, 0\n"
    "v_accvgpr_write_b32 a200, v250\n"
    "v_accvgpr_write_b32 a201, v251\n"
    "v_mfma_f32_16x16x32_bf16 v[220:223], v[188:191], a[40:43], v[220:223]\n"
    "buffer_load_dwordx4 v230, s[36:39], s52 offen lds\n"
    "v_mfma_f32_16x16x32_bf16 v[220:223], v[192:195], a[44:47], v[220:223]\n"
    "s_add_u32 s52, s52, 4096\n"
    "v_fma_f32 v225, v225, s56, v240\n"
    "v_fma_f32 v226, v226, s56, v240\n"
    "v_mfma_f32_16x16x32_bf16 v[220:223], v[196:199], a[48:51], v[220:223]\n"
    "ds_read_b128 v[172:175], v233 offset:384\n"
    "v_fma_f32 v227, v227, s56, v240\n"
    "v_mfma_f32_16x16x32_bf16 v[220:223], v[200:203], a[52:55], v[220:223]\n"
    "v_exp_f32 v224, v224\n"
    "v_mfma_f32_16x16x32_bf16 v[220:223], v[204:207], a[56:59], v[220:223]\n"
    "v_exp_f32 v225, v225\n"
    "v_mfma_f32_16x16x32_bf16 v[220:223], v[208:211], a[60:63], v[220:223]\n"
    "v_exp_f32 v226, v226\n"
    "v_exp_f32 v227, v227\n"
    "v_mfma_f32_16x16x32_bf16 a[136:139], a[144:147], v[88:91], a[136:139]\n"
    "v_cvt_pk_bf16_f32 v92, v224, v225\n"
    "v_cvt_pk_bf16_f32 v93, v226, v227\n"
    "v_mfma_f32_16x16x32_bf16 v[224:227], v[180:183], a[0:3], 0\n"
    "s_add_u32 m0, s96, 4224\n"
    "ds_read_b128 v[176:179], v233 offset:448\n"
    "v_fma_f32 v212, v212, s56, v242\n"
    "v_mfma_f32_16x16x32_bf16 v[224:227], v[184:187], a[4:7], v[224:227]\n"
    "buffer_load_dwordx4 v230, s[36:39], s52 offen lds\n"
    "v_mfma_f32_16x16x32_bf16 v[224:227], v[188:191], a[8:11], v[224:227]\n"
    "s_add_u32 s52, s52, 4096\n"
    "v_fma_f32 v213, v213, s56, v242\n"
    "v_fma_f32 v214, v214, s56, v242\n"
    "v_mfma_f32_16x16x32_bf16 v[224:227], v[192:195], a[12:15], v[224:227]\n"
    "v_fma_f32 v215, v215, s56, v242\n"
    "v_fma_f32 v216, v216, s56, v243\n"
    "v_mfma_f32_16x16x32_bf16 v[224:227], v[196:199], a[16:19], v[224:227]\n"
    "v_exp_f32 v212, v212\n"
    "v_mfma_f32_16x16x32_bf16 v[224:227], v[200:203], a[20:23], v[224:227]\n"
    "v_exp_f32 v213, v213\n"
    "v_mfma_f32_16x16x32_bf16 v[224:227], v[204:207], a[24:27], v[224:227]\n"
    "v_exp_f32 v214, v214\n"
    "v_mfma_f32_16x16x32_bf16 v[224:227], v[208:211], a[28:31], v[224:227]\n"
    "v_exp_f32 v215, v215\n"
    "v_mfma_f32_16x16x32_bf16 a[140:143], a[144:147], a[196:199], a[140:143]\n"
    "s_waitcnt lgkmcnt(0)\n"
    "v_cvt_pk_bf16_f32 v248, v212, v213\n"
    "v_cvt_pk_bf16_f32 v249, v214, v215\n"
    "v_mfma_f32_16x16x32_bf16 v[212:215], v[148:151], a[64:67], 0\n"
    "s_add_u32 m0, s96, 8448\n"
    "ds_read_b128 v[180:183], v233 offset:512\n"
    "v_fma_f32 v217, v217, s56, v243\n"
    "v_mfma_f32_16x16x32_bf16 v[212:215], v[152:155], a[68:71], v[212:215]\n"
    "buffer_load_dwordx4 v230, s[36:39], s52 offen lds\n"
    "v_mfma_f32_16x16x32_bf16 v[212:215], v[156:159], a[72:75], v[212:215]\n"
    "s_add_u32 s52, s52, 4096\n"
    "v_fma_f32 v218, v218, s56, v243\n"
    "v_fma_f32 v219, v219, s56, v243\n"
    "v_mfma_f32_16x16x32_bf16 v[212:215], v[160:163], a[76:79], v[212:215]\n"
    "v_accvgpr_write_b32 a206, v248\n"
    "v_accvgpr_write_b32 a207, v249\n"
    "v_mfma_f32_16x16x32_bf16 v[212:215], v[164:167], a[80:83], v[212:215]\n"
    "v_exp_f32 v216, v216\n"
    "v_mfma_f32_16x16x32_bf16 v[212:215], v[168:171], a[84:87], v[212:215]\n"
    "v_exp_f32 v217, v217\n"
    "v_exp_f32 v218, v218\n"
    "v_exp_f32 v219, v219\n"
    "v_mfma_f32_16x16x32_bf16 v[212:215], v[172:175], a[88:91], v[212:215]\n"
    "s_add_u32 m0, s59, 12672\n"
    "ds_read_b128 v[184:187], v233 offset:576\n"
    "v_cvt_pk_bf16_f32 v98, v216, v217\n"
    "v_cvt_pk_bf16_f32 v99, v218, v219\n"
    "v_mfma_f32_16x16x32_bf16 v[212:215], v[176:179], a[92:95], v[212:215]\n"
    "buffer_load_dwordx4 v230, s[44:47], s58 offen nt lds\n"
    "v_mfma_f32_16x16x32_bf16 v[216:219], v[148:151], a[96:99], 0\n"
    "s_add_u32 s58, s58, 1024\n"
    "ds_read_b128 v[188:191], v233 offset:640\n"
    "v_fma_f32 v220, v220, s56, v241\n"
    "v_mfma_f32_16x16x32_bf16 v[216:219], v[152:155], a[100:103], v[216:219]\n"
    "v_fma_f32 v221, v221, s56, v241\n"
    "v_fma_f32 v222, v222, s56, v241\n"
    "v_mfma_f32_16x16x32_bf16 v[216:219], v[156:159], a[104:107], v[216:219]\n"
    "ds_read_b128 v[192:195], v233 offset:704\n"
    "v_fma_f32 v223, v223, s56, v241\n"
    "v_mfma_f32_16x16x32_bf16 v[216:219], v[160:163], a[108:111], v[216:219]\n"
    "v_exp_f32 v220, v220\n"
    "v_mfma_f32_16x16x32_bf16 v[216:219], v[164:167], a[112:115], v[216:219]\n"
    "s_add_u32 m0, s96, 12672\n"
    "v_exp_f32 v221, v221\n"
    "v_mfma_f32_16x16x32_bf16 v[216:219], v[168:171], a[116:119], v[216:219]\n"
    "buffer_load_dwordx4 v230, s[36:39], s52 offen lds\n"
    "v_mfma_f32_16x16x32_bf16 v[216:219], v[172:175], a[120:123], v[216:219]\n"
    "v_exp_f32 v222, v222\n"
    "v_exp_f32 v223, v223\n"
    "v_mfma_f32_16x16x32_bf16 v[216:219], v[176:179], a[124:127], v[216:219]\n"
    "s_add_u32 s52, s52, 4096\n"
    "v_cvt_pk_bf16_f32 v250, v220, v221\n"
    "v_cvt_pk_bf16_f32 v251, v222, v223\n"
    "v_mfma_f32_16x16x32_bf16 v[220:223], v[148:151], a[32:35], 0\n"
    "s_add_u32 s55, s54, s62\n"
    "ds_read_b128 v[196:199], v233 offset:768\n"
    "v_accvgpr_write_b32 a202, v250\n"
    "v_mfma_f32_16x16x32_bf16 v[220:223], v[152:155], a[36:39], v[220:223]\n"
    "s_add_u32 s54, s54, 16896\n"
    "ds_read_b128 v[200:203], v233 offset:832\n"
    "v_accvgpr_write_b32 a203, v251\n"
    "v_mfma_f32_16x16x32_bf16 v[220:223], v[156:159], a[40:43], v[220:223]\n"
    "s_sub_u32 s97, s54, 84480\n"
    "v_fma_f32 v224, v224, s56, v240\n"
    "v_fma_f32 v225, v225, s56, v240\n"
    "v_mfma_f32_16x16x32_bf16 v[220:223], v[160:163], a[44:47], v[220:223]\n"
    "s_min_u32 s54, s54, s97\n"
    "v_fma_f32 v226, v226, s56, v240\n"
    "v_fma_f32 v227, v227, s56, v240\n"
    "v_mfma_f32_16x16x32_bf16 v[220:223], v[164:167], a[48:51], v[220:223]\n"
    "s_sub_u32 s97, s54, s66\n"
    "ds_read_b128 v[204:207], v233 offset:896\n"
    "v_fma_f32 v212, v212, s56, v242\n"
    "v_mfma_f32_16x16x32_bf16 v[220:223], v[168:171], a[52:55], v[220:223]\n"
    "s_mov_b32 s66, s54\n"
    "v_exp_f32 v224, v224\n"
    "v_mfma_f32_16x16x32_bf16 v[220:223], v[172:175], a[56:59], v[220:223]\n"
    "v_exp_f32 v225, v225\n"
    "v_mfma_f32_16x16x32_bf16 v[220:223], v[176:179], a[60:63], v[220:223]\n"
    "v_exp_f32 v226, v226\n"
    "v_exp_f32 v227, v227\n"
    "v_mfma_f32_16x16x32_bf16 a[132:135], a[144:147], v[84:87], a[132:135]\n"
    "v_cvt_pk_bf16_f32 v94, v224, v225\n"
    "v_cvt_pk_bf16_f32 v95, v226, v227\n"
    "v_mfma_f32_16x16x32_bf16 v[224:227], v[148:151], a[0:3], 0\n"
    "ds_read_b128 v[208:211], v233 offset:960\n"
    "v_fma_f32 v213, v213, s56, v242\n"
    "v_mfma_f32_16x16x32_bf16 v[224:227], v[152:155], a[4:7], v[224:227]\n"
    "v_fma_f32 v214, v214, s56, v242\n"
    "v_fma_f32 v215, v215, s56, v242\n"
    "v_mfma_f32_16x16x32_bf16 v[224:227], v[156:159], a[8:11], v[224:227]\n"
    "v_exp_f32 v212, v212\n"
    "v_mfma_f32_16x16x32_bf16 v[224:227], v[160:163], a[12:15], v[224:227]\n"
    "v_exp_f32 v213, v213\n"
    "v_mfma_f32_16x16x32_bf16 v[224:227], v[164:167], a[16:19], v[224:227]\n"
    "v_exp_f32 v214, v214\n"
    "v_mfma_f32_16x16x32_bf16 v[224:227], v[168:171], a[20:23], v[224:227]\n"
    "v_exp_f32 v215, v215\n"
    "v_mfma_f32_16x16x32_bf16 v[224:227], v[172:175], a[24:27], v[224:227]\n"
    "v_cvt_pk_bf16_f32 v248, v212, v213\n"
    "v_cvt_pk_bf16_f32 v249, v214, v215\n"
    "v_mfma_f32_16x16x32_bf16 v[224:227], v[176:179], a[28:31], v[224:227]\n"
    "v_add_u32 v233, s97, v233\n"
    "v_accvgpr_write_b32 a212, v248\n"
    "v_mfma_f32_16x16x32_bf16 a[128:131], a[144:147], v[80:83], a[128:131]\n"
    "s_waitcnt vmcnt(11) lgkmcnt(0)\n"
    "ds_inc_u32 v228, v236\n"
    "v_mfma_f32_16x16x32_bf16 v[212:215], v[180:183], a[64:67], 0\n"
    "ds_read_b128 v[148:151], v233 offset:0\n"
    "v_accvgpr_write_b32 a213, v249\n"
    "v_mfma_f32_16x16x32_bf16 v[212:215], v[184:187], a[68:71], v[212:215]\n"
    "s_add_u32 m0, s59, 13728\n"
    "v_fma_f32 v216, v216, s56, v243\n"
    "v_fma_f32 v217, v217, s56, v243\n"
    "v_mfma_f32_16x16x32_bf16 v[212:215], v[188:191], a[72:75], v[212:215]\n"
    "s_add_u32 s61, s61, 1\n"
    "buffer_load_dwordx4 v230, s[44:47], s58 offen nt lds\n"
    "v_mfma_f32_16x16x32_bf16 v[212:215], v[192:195], a[76:79], v[212:215]\n"
    "s_add_u32 s58, s58, 1024\n"
    "v_fma_f32 v218, v218, s56, v243\n"
    "v_fma_f32 v219, v219, s56, v243\n"
    "v_mfma_f32_16x16x32_bf16 v[212:215], v[196:199], a[80:83], v[212:215]\n"
    "v_exp_f32 v216, v216\n"
    "v_mfma_f32_16x16x32_bf16 v[212:215], v[200:203], a[84:87], v[212:215]\n"
    "v_exp_f32 v217, v217\n"
    "v_mfma_f32_16x16x32_bf16 v[212:215], v[204:207], a[88:91], v[212:215]\n"
    "ds_read_b128 v[152:155], v233 offset:64\n"
    "v_exp_f32 v218, v218\n"
    "v_exp_f32 v219, v219\n"
    "v_mfma_f32_16x16x32_bf16 v[212:215], v[208:211], a[92:95], v[212:215]\n"
    "v_cvt_pk_bf16_f32 v104, v216, v217\n"
    "v_cvt_pk_bf16_f32 v105, v218, v219\n"
    "v_mfma_f32_16x16x32_bf16 v[216:219], v[180:183], a[96:99], 0\n"
    "ds_read_b128 v[156:159], v233 offset:128\n"
    "v_fma_f32 v220, v220, s56, v241\n"
    "v_mfma_f32_16x16x32_bf16 v[216:219], v[184:187], a[100:103], v[216:219]\n"
    "ds_read_b32 v229, v230\n"
    "v_fma_f32 v221, v221, s56, v241\n"
    "v_mfma_f32_16x16x32_bf16 v[216:219], v[188:191], a[104:107], v[216:219]\n"
    "ds_read_b128 v[160:163], v233 offset:192\n"
    "v_fma_f32 v222, v222, s56, v241\n"
    "v_mfma_f32_16x16x32_bf16 v[216:219], v[192:195], a[108:111], v[216:219]\n"
    "v_fma_f32 v223, v223, s56, v241\n"
    "v_fma_f32 v224, v224, s56, v240\n"
    "v_mfma_f32_16x16x32_bf16 v[216:219], v[196:199], a[112:115], v[216:219]\n"
    "v_exp_f32 v220, v220\n"
    "v_mfma_f32_16x16x32_bf16 v[216:219], v[200:203], a[116:119], v[216:219]\n"
    "s_waitcnt lgkmcnt(1)\n"
    "ds_read_b128 v[164:167], v233 offset:256\n"
    "v_cmp_gt_u32 vcc, s61, v229\n"
    "v_mfma_f32_16x16x32_bf16 v[216:219], v[204:207], a[120:123], v[216:219]\n"
    "v_exp_f32 v221, v221\n"
    "v_exp_f32 v222, v222\n"
    "v_exp_f32 v223, v223\n"
    "v_mfma_f32_16x16x32_bf16 v[216:219], v[208:211], a[124:127], v[216:219]\n"
    "v_cvt_pk_bf16_f32 v250, v220, v221\n"
    "v_cvt_pk_bf16_f32 v251, v222, v223\n"
    "v_mfma_f32_16x16x32_bf16 v[220:223], v[180:183], a[32:35], 0\n"
    "s_cbranch_vccnz L_sp10%=\n"
    "L_rt10%=:\n"
    "ds_read_b128 v[168:171], v233 offset:320\n"
    "v_mfma_f32_16x16x32_bf16 v[220:223], v[184:187], a[36:39], v[220:223]\n"
    "s_add_u32 m0, s55, 0\n"
    "v_accvgpr_write_b32 a208, v250\n"
    "v_accvgpr_write_b32 a209, v251\n"
    "v_mfma_f32_16x16x32_bf16 v[220:223], v[188:191], a[40:43], v[220:223]\n"
    "buffer_load_dwordx4 v230, s[36:39], s52 offen lds\n"
    "v_mfma_f32_16x16x32_bf16 v[220:223], v[192:195], a[44:47], v[220:223]\n"
    "s_add_u32 s52, s52, 4096\n"
    "v_fma_f32 v225, v225, s56, v240\n"
    "v_fma_f32 v226, v226, s56, v240\n"
    "v_mfma_f32_16x16x32_bf16 v[220:223], v[196:199], a[48:51], v[220:223]\n"
    "ds_read_b128 v[172:175], v233 offset:384\n"
    "v_fma_f32 v227, v227, s56, v240\n"
    "v_mfma_f32_16x16x32_bf16 v[220:223], v[200:203], a[52:55], v[220:223]\n"
    "v_exp_f32 v224, v224\n"
    "v_mfma_f32_16x16x32_bf16 v[220:223], v[204:207], a[56:59], v[220:223]\n"
    "v_exp_f32 v225, v225\n"
    "v_mfma_f32_16x16x32_bf16 v[220:223], v[208:211], a[60:63], v[220:223]\n"
    "v_exp_f32 v226, v226\n"
    "v_exp_f32 v227, v227\n"
    "v_mfma_f32_16x16x32_bf16 a[136:139], a[144:147], a[204:207], a[136:139]\n"
    "v_cvt_pk_bf16_f32 v100, v224, v225\n"
    "v_cvt_pk_bf16_f32 v101, v226, v227\n"
    "v_mfma_f32_16x16x32_bf16 v[224:227], v[180:183], a[0:3], 0\n"
    "s_add_u32 m0, s55, 4224\n"
    "ds_read_b128 v[176:179], v233 offset:448\n"
    "v_fma_f32 v212, v212, s56, v242\n"
    "v_mfma_f32_16x16x32_bf16 v[224:227], v[184:187], a[4:7], v[224:227]\n"
    "buffer_load_dwordx4 v230, s[36:39], s52 offen lds\n"
    "v_mfma_f32_16x16x32_bf16 v[224:227], v[188:191], a[8:11], v[224:227]\n"
    "s_add_u32 s52, s52, 4096\n"
    "v_fma_f32 v213, v213, s56, v242\n"
    "v_fma_f32 v214, v214, s56, v242\n"
    "v_mfma_f32_16x16x32_bf16 v[224:227], v[192:195], a[12:15], v[224:227]\n"
    "v_fma_f32 v215, v215, s56, v242\n"
    "v_fma_f32 v216, v216, s56, v243\n"
    "v_mfma_f32_16x16x32_bf16 v[224:227], v[196:199], a[16:19], v[224:227]\n"
    "v_exp_f32 v212, v212\n"
    "v_mfma_f32_16x16x32_bf16 v[224:227], v[200:203], a[20:23], v[224:227]\n"
    "v_exp_f32 v213, v213\n"
    "v_mfma_f32_16x16x32_bf16 v[224:227], v[204:207], a[24:27], v[224:227]\n"
    "v_exp_f32 v214, v214\n"
    "v_mfma_f32_16x16x32_bf16 v[224:227], v[208:211], a[28:31], v[224:227]\n"
    "v_exp_f32 v215, v215\n"
    "v_mfma_f32_16x16x32_bf16 a[140:143], a[144:147], v[96:99], a[140:143]\n"
    "s_waitcnt lgkmcnt(0)\n"
    "v_cvt_pk_bf16_f32 v248, v212, v213\n"
    "v_cvt_pk_bf16_f32 v249, v214, v215\n"
    "v_mfma_f32_16x16x32_bf16 v[212:215], v[148:151], a[64:67], 0\n"
    "s_add_u32 m0, s55, 8448\n"
    "ds_read_b128 v[180:183], v233 offset:512\n"
    "v_fma_f32 v217, v217, s56, v243\n"
    "v_mfma_f32_16x16x32_bf16 v[212:215], v[152:155], a[68:71], v[212:215]\n"
    "buffer_load_dwordx4 v230, s[36:39], s52 offen lds\n"
    "v_mfma_f32_16x16x32_bf16 v[212:215], v[156:159], a[72:75], v[212:215]\n"
    "s_add_u32 s52, s52, 4096\n"
    "v_fma_f32 v218, v218, s56, v243\n"
    "v_fma_f32 v219, v219, s56, v243\n"
    "v_mfma_f32_16x16x32_bf16 v[212:215], v[160:163], a[76:79], v[212:215]\n"
    "v_accvgpr_write_b32 a214, v248\n"
    "v_accvgpr_write_b32 a215, v249\n"
    "v_mfma_f32_16x16x32_bf16 v[212:215], v[164:167], a[80:83], v[212:215]\n"
    "v_exp_f32 v216, v216\n"
    "v_mfma_f32_16x16x32_bf16 v[212:215], v[168:171], a[84:87], v[212:215]\n"
    "v_exp_f32 v217, v217\n"
    "v_exp_f32 v218, v218\n"
    "v_exp_f32 v219, v219\n"
    "v_mfma_f32_16x16x32_bf16 v[212:215], v[172:175], a[88:91], v[212:215]\n"
    "s_add_u32 m0, s59, 14784\n"
    "ds_read_b128 v[184:187], v233 offset:576\n"
    "v_cvt_pk_bf16_f32 v106, v216, v217\n"
    "v_cvt_pk_bf16_f32 v107, v218, v219\n"
    "v_mfma_f32_16x16x32_bf16 v[212:215], v[176:179], a[92:95], v[212:215]\n"
    "buffer_load_dwordx4 v230, s[44:47], s58 offen nt lds\n"
    "v_mfma_f32_16x16x32_bf16 v[216:219], v[148:151], a[96:99], 0\n"
    "s_add_u32 s58, s58, 1024\n"
    "ds_read_b128 v[188:191], v233 offset:640\n"
    "v_fma_f32 v220, v220, s56, v241\n"
    "v_mfma_f32_16x16x32_bf16 v[216:219], v[152:155], a[100:103], v[216:219]\n"
    "v_fma_f32 v221, v221, s56, v241\n"
    "v_fma_f32 v222, v222, s56, v241\n"
    "v_mfma_f32_16x16x32_bf16 v[216:219], v[156:159], a[104:107], v[216:219]\n"
    "ds_read_b128 v[192:195], v233 offset:704\n"
    "v_fma_f32 v223, v223, s56, v241\n"
    "v_mfma_f32_16x16x32_bf16 v[216:219], v[160:163], a[108:111], v[216:219]\n"
    "v_exp_f32 v220, v220\n"
    "v_mfma_f32_16x16x32_bf16 v[216:219], v[164:167], a[112:115], v[216:219]\n"
    "s_add_u32 m0, s55, 12672\n"
    "v_exp_f32 v221, v221\n"
    "v_mfma_f32_16x16x32_bf16 v[216:219], v[168:171], a[116:119], v[216:219]\n"
    "buffer_load_dwordx4 v230, s[36:39], s52 offen lds\n"
    "v_mfma_f32_16x16x32_bf16 v[216:219], v[172:175], a[120:123], v[216:219]\n"
    "v_exp_f32 v222, v222\n"
    "v_exp_f32 v223, v223\n"
    "v_mfma_f32_16x16x32_bf16 v[216:219], v[176:179], a[124:127], v[216:219]\n"
    "s_add_u32 s52, s52, 4096\n"
    "v_cvt_pk_bf16_f32 v250, v220, v221\n"
    "v_cvt_pk_bf16_f32 v251, v222, v223\n"
    "v_mfma_f32_16x16x32_bf16 v[220:223], v[148:151], a[32:35], 0\n"
    "s_add_u32 s96, s54, s63\n"
    "ds_read_b128 v[196:199], v233 offset:768\n"
    "v_accvgpr_write_b32 a210, v250\n"
    "v_mfma_f32_16x16x32_bf16 v[220:223], v[152:155], a[36:39], v[220:223]\n"
    "s_add_u32 s54, s54, 16896\n"
    "ds_read_b128 v[200:203], v233 offset:832\n"
    "v_accvgpr_write_b32 a211, v251\n"
    "v_mfma_f32_16x16x32_bf16 v[220:223], v[156:159], a[40:43], v[220:223]\n"
    "s_sub_u32 s97, s54, 84480\n"
    "v_fma_f32 v224, v224, s56, v240\n"
    "v_fma_f32 v225, v225, s56, v240\n"
    "v_mfma_f32_16x16x32_bf16 v[220:223], v[160:163], a[44:47], v[220:223]\n"
    "s_min_u32 s54, s54, s97\n"
    "v_fma_f32 v226, v226, s56, v240\n"
    "v_fma_f32 v227, v227, s56, v240\n"
    "v_mfma_f32_16x16x32_bf16 v[220:223], v[164:167], a[48:51], v[220:223]\n"
    "s_sub_u32 s97, s54, s66\n"
    "ds_read_b128 v[204:207], v233 offset:896\n"
    "v_fma_f32 v212, v212, s56, v242\n"
    "v_mfma_f32_16x16x32_bf16 v[220:223], v[168:171], a[52:55], v[220:223]\n"
    "s_mov_b32 s66, s54\n"
    "v_exp_f32 v224, v224\n"
    "v_mfma_f32_16x16x32_bf16 v[220:223], v[172:175], a[56:59], v[220:223]\n"
    "v_exp_f32 v225, v225\n"
    "v_mfma_f32_16x16x32_bf16 v[220:223], v[176:179], a[60:63], v[220:223]\n"
    "v_exp_f32 v226, v226\n"
    "v_exp_f32 v227, v227\n"
    "v_mfma_f32_16x16x32_bf16 a[132:135], a[144:147], a[200:203], a[132:135]\n"
    "v_cvt_pk_bf16_f32 v102, v224, v225\n"
    "v_cvt_pk_bf16_f32 v103, v226, v227\n"
    "v_mfma_f32_16x16x32_bf16 v[224:227], v[148:151], a[0:3], 0\n"
    "ds_read_b128 v[208:211], v233 offset:960\n"
    "v_fma_f32 v213, v213, s56, v242\n"
    "v_mfma_f32_16x16x32_bf16 v[224:227], v[152:155], a[4:7], v[224:227]\n"
    "v_fma_f32 v214, v214, s56, v242\n"
    "v_fma_f32 v215, v215, s56, v242\n"
    "v_mfma_f32_16x16x32_bf16 v[224:227], v[156:159], a[8:11], v[224:227]\n"
    "v_exp_f32 v212, v212\n"
    "v_mfma_f32_16x16x32_bf16 v[224:227], v[160:163], a[12:15], v[224:227]\n"
    "v_exp_f32 v213, v213\n"
    "v_mfma_f32_16x16x32_bf16 v[224:227], v[164:167], a[16:19], v[224:227]\n"
    "v_exp_f32 v214, v214\n"
    "v_mfma_f32_16x16x32_bf16 v[224:227], v[168:171], a[20:23], v[224:227]\n"
    "v_exp_f32 v215, v215\n"
    "v_mfma_f32_16x16x32_bf16 v[224:227], v[172:175], a[24:27], v[224:227]\n"
    "v_cvt_pk_bf16_f32 v248, v212, v213\n"
    "v_cvt_pk_bf16_f32 v249, v214, v215\n"
    "v_mfma_f32_16x16x32_bf16 v[224:227], v[176:179], a[28:31], v[224:227]\n"
    "v_add_u32 v233, s97, v233\n"
    "v_accvgpr_write_b32 a220, v248\n"
    "v_mfma_f32_16x16x32_bf16 a[128:131], a[144:147], v[92:95], a[128:131]\n"
    "s_waitcnt vmcnt(12) lgkmcnt(0)\n"
    "ds_inc_u32 v228, v236\n"
    "v_mfma_f32_16x16x32_bf16 v[212:215], v[180:183], a[64:67], 0\n"
    "ds_read_b128 v[148:151], v233 offset:0\n"
    "v_accvgpr_write_b32 a221, v249\n"
    "v_mfma_f32_16x16x32_bf16 v[212:215], v[184:187], a[68:71], v[212:215]\n"
    "s_add_u32 m0, s59, 15840\n"
    "v_fma_f32 v216, v216, s56, v243\n"
    "v_fma_f32 v217, v217, s56, v243\n"
    "v_mfma_f32_16x16x32_bf16 v[212:215], v[188:191], a[72:75], v[212:215]\n"
    "s_add_u32 s61, s61, 1\n"
    "buffer_load_dwordx4 v230, s[44:47], s58 offen nt lds\n"
    "v_mfma_f32_16x16x32_bf16 v[212:215], v[192:195], a[76:79], v[212:215]\n"
    "s_add_u32 s58, s58, 1024\n"
    "v_fma_f32 v218, v218, s56, v243\n"
    "v_fma_f32 v219, v219, s56, v243\n"
    "v_mfma_f32_16x16x32_bf16 v[212:215], v[196:199], a[80:83], v[212:215]\n"
    "v_exp_f32 v216, v216\n"
    "v_mfma_f32_16x16x32_bf16 v[212:215], v[200:203], a[84:87], v[212:215]\n"
    "v_exp_f32 v217, v217\n"
    "v_mfma_f32_16x16x32_bf16 v[212:215], v[204:207], a[88:91], v[212:215]\n"
    "ds_read_b128 v[152:155], v233 offset:64\n"
    "v_exp_f32 v218, v218\n"
    "v_exp_f32 v219, v219\n"
    "v_mfma_f32_16x16x32_bf16 v[212:215], v[208:211], a[92:95], v[212:215]\n"
    "v_cvt_pk_bf16_f32 v112, v216, v217\n"
    "v_cvt_pk_bf16_f32 v113, v218, v219\n"
    "v_mfma_f32_16x16x32_bf16 v[216:219], v[180:183], a[96:99], 0\n"
    "ds_read_b128 v[156:159], v233 offset:128\n"
    "v_fma_f32 v220, v220, s56, v241\n"
    "v_mfma_f32_16x16x32_bf16 v[216:219], v[184:187], a[100:103], v[216:219]\n"
    "ds_read_b32 v229, v230\n"
    "v_fma_f32 v221, v221, s56, v241\n"
    "v_mfma_f32_16x16x32_bf16 v[216:219], v[188:191], a[104:107], v[216:219]\n"
    "ds_read_b128 v[160:163], v233 offset:192\n"
    "v_fma_f32 v222, v222, s56, v241\n"
    "v_mfma_f32_16x16x32_bf16 v[216:219], v[192:195], a[108:111], v[216:219]\n"
    "v_fma_f32 v223, v223, s56, v241\n"
    "v_fma_f32 v224, v224, s56, v240\n"
    "v_mfma_f32_16x16x32_bf16 v[216:219], v[196:199], a[112:115], v[216:219]\n"
    "v_exp_f32 v220, v220\n"
    "v_mfma_f32_16x16x32_bf16 v[216:219], v[200:203], a[116:119], v[216:219]\n"
    "s_waitcnt lgkmcnt(1)\n"
    "ds_read_b128 v[164:167], v233 offset:256\n"
    "v_cmp_gt_u32 vcc, s61, v229\n"
    "v_mfma_f32_16x16x32_bf16 v[216:219], v[204:207], a[120:123], v[216:219]\n"
    "v_exp_f32 v221, v221\n"
    "v_exp_f32 v222, v222\n"
    "v_exp_f32 v223, v223\n"
    "v_mfma_f32_16x16x32_bf16 v[216:219], v[208:211], a[124:127], v[216:219]\n"
    "v_cvt_pk_bf16_f32 v250, v220, v221\n"
    "v_cvt_pk_bf16_f32 v251, v222, v223\n"
    "v_mfma_f32_16x16x32_bf16 v[220:223], v[180:183], a[32:35], 0\n"
    "s_cbranch_vccnz L_sp11%=\n"
    "L_rt11%=:\n"
    "ds_read_b128 v[168:171], v233 offset:320\n"
    "v_mfma_f32_16x16x32_bf16 v[220:223], v[184:187], a[36:39], v[220:223]\n"
    "s_add_u32 m0, s96, 0\n"
    "v_accvgpr_write_b32 a216, v250\n"
    "v_accvgpr_write_b32 a217, v251\n"
    "v_mfma_f32_16x16x32_bf16 v[220:223], v[188:191], a[40:43], v[220:223]\n"
    "buffer_load_dwordx4 v231, s[40:43], s53 offen lds\n"
    "v_mfma_f32_16x16x32_bf16 v[220:223], v[192:195], a[44:47], v[220:223]\n"
    "s_add_u32 s53, s53, 0x4000\n"
    "v_fma_f32 v225, v225, s56, v240\n"
    "v_fma_f32 v226, v226, s56, v240\n"
    "v_mfma_f32_16x16x32_bf16 v[220:223], v[196:199], a[48:51], v[220:223]\n"
    "ds_read_b128 v[172:175], v233 offset:384\n"
    "v_fma_f32 v227, v227, s56, v240\n"
    "v_mfma_f32_16x16x32_bf16 v[220:223], v[200:203], a[52:55], v[220:223]\n"
    "v_exp_f32 v224, v224\n"
    "v_mfma_f32_16x16x32_bf16 v[220:223], v[204:207], a[56:59], v[220:223]\n"
    "v_exp_f32 v225, v225\n"
    "v_mfma_f32_16x16x32_bf16 v[220:223], v[208:211], a[60:63], v[220:223]\n"
    "v_exp_f32 v226, v226\n"
    "v_exp_f32 v227, v227\n"
    "v_mfma_f32_16x16x32_bf16 a[136:139], a[144:147], a[212:215], a[136:139]\n"
    "v_cvt_pk_bf16_f32 v108, v224, v225\n"
    "v_cvt_pk_bf16_f32 v109, v226, v227\n"
    "v_mfma_f32_16x16x32_bf16 v[224:227], v[180:183], a[0:3], 0\n"
    "s_add_u32 m0, s96, 4096\n"
    "ds_read_b128 v[176:179], v233 offset:448\n"
    "v_fma_f32 v212, v212, s56, v242\n"
    "v_mfma_f32_16x16x32_bf16 v[224:227], v[184:187], a[4:7], v[224:227]\n"
    "buffer_load_dwordx4 v231, s[40:43], s53 offen lds\n"
    "v_mfma_f32_16x16x32_bf16 v[224:227], v[188:191], a[8:11], v[224:227]\n"
    "s_add_u32 s53, s53, 0x4000\n"
    "v_fma_f32 v213, v213, s56, v242\n"
    "v_fma_f32 v214, v214, s56, v242\n"
    "v_mfma_f32_16x16x32_bf16 v[224:227], v[192:195], a[12:15], v[224:227]\n"
    "v_fma_f32 v215, v215, s56, v242\n"
    "v_fma_f32 v216, v216, s56, v243\n"
    "v_mfma_f32_16x16x32_bf16 v[224:227], v[196:199], a[16:19], v[224:227]\n"
    "v_exp_f32 v212, v212\n"
    "v_mfma_f32_16x16x32_bf16 v[224:227], v[200:203], a[20:23], v[224:227]\n"
    "v_exp_f32 v213, v213\n"
    "v_mfma_f32_16x16x32_bf16 v[224:227], v[204:207], a[24:27], v[224:227]\n"
    "v_exp_f32 v214, v214\n"
    "v_mfma_f32_16x16x32_bf16 v[224:227], v[208:211], a[28:31], v[224:227]\n"
    "v_exp_f32 v215, v215\n"
    "v_mfma_f32_16x16x32_bf16 a[140:143], a[144:147], v[104:107], a[140:143]\n"
    "s_waitcnt lgkmcnt(0)\n"
    "v_cvt_pk_bf16_f32 v248, v212, v213\n"
    "v_cvt_pk_bf16_f32 v249, v214, v215\n"
    "v_mfma_f32_16x16x32_bf16 v[212:215], v[148:151], a[64:67], 0\n"
    "s_add_u32 m0, s96, 8192\n"
    "ds_read_b128 v[180:183], v233 offset:512\n"
    "v_accvgpr_write_b32 a222, v248\n"
    "v_mfma_f32_16x16x32_bf16 v[212:215], v[152:155], a[68:71], v[212:215]\n"
    "buffer_load_dwordx4 v231, s[40:43], s53 offen lds\n"
    "v_mfma_f32_16x16x32_bf16 v[212:215], v[156:159], a[72:75], v[212:215]\n"
    "s_add_u32 s53, s53, 0x4000\n"
    "v_accvgpr_write_b32 a223, v249\n"
    "v_fma_f32 v217, v217, s56, v243\n"
    "v_mfma_f32_16x16x32_bf16 v[212:215], v[160:163], a[76:79], v[212:215]\n"
    "v_fma_f32 v218, v218, s56, v243\n"
    "v_fma_f32 v219, v219, s56, v243\n"
    "v_mfma_f32_16x16x32_bf16 v[212:215], v[164:167], a[80:83], v[212:215]\n"
    "v_exp_f32 v216, v216\n"
    "v_mfma_f32_16x16x32_bf16 v[212:215], v[168:171], a[84:87], v[212:215]\n"
    "v_exp_f32 v217, v217\n"
    "v_mfma_f32_16x16x32_bf16 v[212:215], v[172:175], a[88:91], v[212:215]\n"
    "ds_read_b128 v[184:187], v233 offset:576\n"
    "v_exp_f32 v218, v218\n"
    "v_exp_f32 v219, v219\n"
    "v_mfma_f32_16x16x32_bf16 v[212:215], v[176:179], a[92:95], v[212:215]\n"
    "s_add_u32 m0, s96, 12288\n"
    "v_cvt_pk_bf16_f32 v114, v216, v217\n"
    "v_cvt_pk_bf16_f32 v115, v218, v219\n"
    "v_mfma_f32_16x16x32_bf16 v[216:219], v[148:151], a[96:99], 0\n"
    "buffer_load_dwordx4 v231, s[40:43], s53 offen lds\n"
    "v_mfma_f32_16x16x32_bf16 v[216:219], v[152:155], a[100:103], v[216:219]\n"
    "s_add_u32 s53, s53, 0x4000\n"
    "ds_read_b128 v[188:191], v233 offset:640\n"
    "v_fma_f32 v220, v220, s56, v241\n"
    "v_mfma_f32_16x16x32_bf16 v[216:219], v[156:159], a[104:107], v[216:219]\n"
    "ds_read_b128 v[192:195], v233 offset:704\n"
    "v_fma_f32 v221, v221, s56, v241\n"
    "v_mfma_f32_16x16x32_bf16 v[216:219], v[160:163], a[108:111], v[216:219]\n"
    "v_fma_f32 v222, v222, s56, v241\n"
    "v_fma_f32 v223, v223, s56, v241\n"
    "v_mfma_f32_16x16x32_bf16 v[216:219], v[164:167], a[112:115], v[216:219]\n"
    "v_exp_f32 v220, v220\n"
    "v_mfma_f32_16x16x32_bf16 v[216:219], v[168:171], a[116:119], v[216:219]\n"
    "v_exp_f32 v221, v221\n"
    "v_mfma_f32_16x16x32_bf16 v[216:219], v[172:175], a[120:123], v[216:219]\n"
    "v_exp_f32 v222, v222\n"
    "v_exp_f32 v223, v223\n"
    "v_mfma_f32_16x16x32_bf16 v[216:219], v[176:179], a[124:127], v[216:219]\n"
    "s_add_u32 s55, s54, s63\n"
    "v_cvt_pk_bf16_f32 v250, v220, v221\n"
    "v_cvt_pk_bf16_f32 v251, v222, v223\n"
    "v_mfma_f32_16x16x32_bf16 v[220:223], v[148:151], a[32:35], 0\n"
    "s_add_u32 s54, s54, 16896\n"
    "ds_read_b128 v[196:199], v233 offset:768\n"
    "v_accvgpr_write_b32 a218, v250\n"
    "v_mfma_f32_16x16x32_bf16 v[220:223], v[152:155], a[36:39], v[220:223]\n"
    "s_sub_u32 s97, s54, 84480\n"
    "ds_read_b128 v[200:203], v233 offset:832\n"
    "v_accvgpr_write_b32 a219, v251\n"
    "v_mfma_f32_16x16x32_bf16 v[220:223], v[156:159], a[40:43], v[220:223]\n"
    "s_min_u32 s54, s54, s97\n"
    "v_fma_f32 v224, v224, s56, v240\n"
    "v_fma_f32 v225, v225, s56, v240\n"
    "v_mfma_f32_16x16x32_bf16 v[220:223], v[160:163], a[44:47], v[220:223]\n"
    "s_sub_u32 s97, s54, s66\n"
    "v_fma_f32 v226, v226, s56, v240\n"
    "v_fma_f32 v227, v227, s56, v240\n"
    "v_mfma_f32_16x16x32_bf16 v[220:223], v[164:167], a[48:51], v[220:223]\n"
    "s_mov_b32 s66, s54\n"
    "ds_read_b128 v[204:207], v233 offset:896\n"
    "v_fma_f32 v212, v212, s56, v242\n"
    "v_mfma_f32_16x16x32_bf16 v[220:223], v[168:171], a[52:55], v[220:223]\n"
    "v_exp_f32 v224, v224\n"
    "v_mfma_f32_16x16x32_bf16 v[220:223], v[172:175], a[56:59], v[220:223]\n"
    "v_exp_f32 v225, v225\n"
    "v_mfma_f32_16x16x32_bf16 v[220:223], v[176:179], a[60:63], v[220:223]\n"
    "v_exp_f32 v226, v226\n"
    "v_exp_f32 v227, v227\n"
    "v_mfma_f32_16x16x32_bf16 a[132:135], a[144:147], a[208:211], a[132:135]\n"
    "v_cvt_pk_bf16_f32 v110, v224, v225\n"
    "v_cvt_pk_bf16_f32 v111, v226, v227\n"
    "v_mfma_f32_16x16x32_bf16 v[224:227], v[148:151], a[0:3], 0\n"
    "ds_read_b128 v[208:211], v233 offset:960\n"
    "v_fma_f32 v213, v213, s56, v242\n"
    "v_mfma_f32_16x16x32_bf16 v[224:227], v[152:155], a[4:7], v[224:227]\n"
    "v_fma_f32 v214, v214, s56, v242\n"
    "v_fma_f32 v215, v215, s56, v242\n"
    "v_mfma_f32_16x16x32_bf16 v[224:227], v[156:159], a[8:11], v[224:227]\n"
    "v_exp_f32 v212, v212\n"
    "v_mfma_f32_16x16x32_bf16 v[224:227], v[160:163], a[12:15], v[224:227]\n"
    "v_exp_f32 v213, v213\n"
    "v_mfma_f32_16x16x32_bf16 v[224:227], v[164:167], a[16:19], v[224:227]\n"
    "v_exp_f32 v214, v214\n"
    "v_mfma_f32_16x16x32_bf16 v[224:227], v[168:171], a[20:23], v[224:227]\n"
    "v_exp_f32 v215, v215\n"
    "v_mfma_f32_16x16x32_bf16 v[224:227], v[172:175], a[24:27], v[224:227]\n"
    "v_cvt_pk_bf16_f32 v248, v212, v213\n"
    "v_cvt_pk_bf16_f32 v249, v214, v215\n"
    "v_mfma_f32_16x16x32_bf16 v[224:227], v[176:179], a[28:31], v[224:227]\n"
    "v_add_u32 v233, s97, v233\n"
    "v_accvgpr_write_b32 a228, v248\n"
    "v_mfma_f32_16x16x32_bf16 a[128:131], a[144:147], v[100:103], a[128:131]\n"
    "s_waitcnt vmcnt(11) lgkmcnt(0)\n"
    "ds_inc_u32 v228, v236\n"
    "v_mfma_f32_16x16x32_bf16 v[212:215], v[180:183], a[64:67], 0\n"
    "ds_read_b128 v[148:151], v233 offset:0\n"
    "v_accvgpr_write_b32 a229, v249\n"
    "v_mfma_f32_16x16x32_bf16 v[212:215], v[184:187], a[68:71], v[212:215]\n"
    "v_fma_f32 v216, v216, s56, v243\n"
    "v_fma_f32 v217, v217, s56, v243\n"
    "v_mfma_f32_16x16x32_bf16 v[212:215], v[188:191], a[72:75], v[212:215]\n"
    "s_add_u32 s61, s61, 1\n"
    "v_fma_f32 v218, v218, s56, v243\n"
    "v_fma_f32 v219, v219, s56, v243\n"
    "v_mfma_f32_16x16x32_bf16 v[212:215], v[192:195], a[76:79], v[212:215]\n"
    "v_exp_f32 v216, v216\n"
    "v_mfma_f32_16x16x32_bf16 v[212:215], v[196:199], a[80:83], v[212:215]\n"
    "v_exp_f32 v217, v217\n"
    "v_mfma_f32_16x16x32_bf16 v[212:215], v[200:203], a[84:87], v[212:215]\n"
    "v_exp_f32 v218, v218\n"
    "v_mfma_f32_16x16x32_bf16 v[212:215], v[204:207], a[88:91], v[212:215]\n"
    "ds_read_b128 v[152:155], v233 offset:64\n"
    "v_exp_f32 v219, v219\n"
    "v_mfma_f32_16x16x32_bf16 v[212:215], v[208:211], a[92:95], v[212:215]\n"
    "v_cvt_pk_bf16_f32 v120, v216, v217\n"
    "v_cvt_pk_bf16_f32 v121, v218, v219\n"
    "v_mfma_f32_16x16x32_bf16 v[216:219], v[180:183], a[96:99], 0\n"
    "ds_read_b128 v[156:159], v233 offset:128\n"
    "v_fma_f32 v220, v220, s56, v241\n"
    "v_mfma_f32_16x16x32_bf16 v[216:219], v[184:187], a[100:103], v[216:219]\n"
    "ds_read_b32 v229, v230\n"
    "v_fma_f32 v221, v221, s56, v241\n"
    "v_mfma_f32_16x16x32_bf16 v[216:219], v[188:191], a[104:107], v[216:219]\n"
    "ds_read_b128 v[160:163], v233 offset:192\n"
    "v_fma_f32 v222, v222, s56, v241\n"
    "v_mfma_f32_16x16x32_bf16 v[216:219], v[192:195], a[108:111], v[216:219]\n"
    "v_fma_f32 v223, v223, s56, v241\n"
    "v_fma_f32 v224, v224, s56, v240\n"
    "v_mfma_f32_16x16x32_bf16 v[216:219], v[196:199], a[112:115], v[216:219]\n"
    "v_exp_f32 v220, v220\n"
    "v_mfma_f32_16x16x32_bf16 v[216:219], v[200:203], a[116:119], v[216:219]\n"
    "s_waitcnt lgkmcnt(1)\n"
    "ds_read_b128 v[164:167], v233 offset:256\n"
    "v_cmp_gt_u32 vcc, s61, v229\n"
    "v_mfma_f32_16x16x32_bf16 v[216:219], v[204:207], a[120:123], v[216:219]\n"
    "v_exp_f32 v221, v221\n"
    "v_exp_f32 v222, v222\n"
    "v_exp_f32 v223, v223\n"
    "v_mfma_f32_16x16x32_bf16 v[216:219], v[208:211], a[124:127], v[216:219]\n"
    "v_cvt_pk_bf16_f32 v250, v220, v221\n"
    "v_cvt_pk_bf16_f32 v251, v222, v223\n"
    "v_mfma_f32_16x16x32_bf16 v[220:223], v[180:183], a[32:35], 0\n"
    "s_cbranch_vccnz L_sp12%=\n"
    "L_rt12%=:\n"
    "ds_read_b128 v[168:171], v233 offset:320\n"
    "v_mfma_f32_16x16x32_bf16 v[220:223], v[184:187], a[36:39], v[220:223]\n"
    "s_add_u32 m0, s55, 0\n"
    "v_accvgpr_write_b32 a224, v250\n"
    "v_accvgpr_write_b32 a225, v251\n"
    "v_mfma_f32_16x16x32_bf16 v[220:223], v[188:191], a[40:43], v[220:223]\n"
    "buffer_load_dwordx4 v231, s[40:43], s53 offen lds\n"
    "v_mfma_f32_16x16x32_bf16 v[220:223], v[192:195], a[44:47], v[220:223]\n"
    "s_add_u32 s53, s53, 0x4000\n"
    "v_fma_f32 v225, v225, s56, v240\n"
    "v_fma_f32 v226, v226, s56, v240\n"
    "v_mfma_f32_16x16x32_bf16 v[220:223], v[196:199], a[48:51], v[220:223]\n"
    "ds_read_b128 v[172:175], v233 offset:384\n"
    "v_fma_f32 v227, v227, s56, v240\n"
    "v_mfma_f32_16x16x32_bf16 v[220:223], v[200:203], a[52:55], v[220:223]\n"
    "v_exp_f32 v224, v224\n"
    "v_mfma_f32_16x16x32_bf16 v[220:223], v[204:207], a[56:59], v[220:223]\n"
    "v_exp_f32 v225, v225\n"
    "v_mfma_f32_16x16x32_bf16 v[220:223], v[208:211], a[60:63], v[220:223]\n"
    "v_exp_f32 v226, v226\n"
    "v_exp_f32 v227, v227\n"
    "v_mfma_f32_16x16x32_bf16 a[136:139], a[144:147], a[220:223], a[136:139]\n"
    "v_cvt_pk_bf16_f32 v116, v224, v225\n"
    "v_cvt_pk_bf16_f32 v117, v226, v227\n"
    "v_mfma_f32_16x16x32_bf16 v[224:227], v[180:183], a[0:3], 0\n"
    "s_add_u32 m0, s55, 4096\n"
    "ds_read_b128 v[176:179], v233 offset:448\n"
    "v_fma_f32 v212, v212, s56, v242\n"
    "v_mfma_f32_16x16x32_bf16 v[224:227], v[184:187], a[4:7], v[224:227]\n"
    "buffer_load_dwordx4 v231, s[40:43], s53 offen lds\n"
    "v_mfma_f32_16x16x32_bf16 v[224:227], v[188:191], a[8:11], v[224:227]\n"
    "s_add_u32 s53, s53, 0x4000\n"
    "v_fma_f32 v213, v213, s56, v242\n"
    "v_fma_f32 v214, v214, s56, v242\n"
    "v_mfma_f32_16x16x32_bf16 v[224:227], v[192:195], a[12:15], v[224:227]\n"
    "v_fma_f32 v215, v215, s56, v242\n"
    "v_fma_f32 v216, v216, s56, v243\n"
    "v_mfma_f32_16x16x32_bf16 v[224:227], v[196:199], a[16:19], v[224:227]\n"
    "v_exp_f32 v212, v212\n"
    "v_mfma_f32_16x16x32_bf16 v[224:227], v[200:203], a[20:23], v[224:227]\n"
    "v_exp_f32 v213, v213\n"
    "v_mfma_f32_16x16x32_bf16 v[224:227], v[204:207], a[24:27], v[224:227]\n"
    "v_exp_f32 v214, v214\n"
    "v_mfma_f32_16x16x32_bf16 v[224:227], v[208:211], a[28:31], v[224:227]\n"
    "v_exp_f32 v215, v215\n"
    "v_mfma_f32_16x16x32_bf16 a[140:143], a[144:147], v[112:115], a[140:143]\n"
    "s_waitcnt lgkmcnt(0)\n"
    "v_cvt_pk_bf16_f32 v248, v212, v213\n"
    "v_cvt_pk_bf16_f32 v249, v214, v215\n"
    "v_mfma_f32_16x16x32_bf16 v[212:215], v[148:151], a[64:67], 0\n"
    "s_add_u32 m0, s55, 8192\n"
    "ds_read_b128 v[180:183], v233 offset:512\n"
    "v_accvgpr_write_b32 a230, v248\n"
    "v_mfma_f32_16x16x32_bf16 v[212:215], v[152:155], a[68:71], v[212:215]\n"
    "buffer_load_dwordx4 v231, s[40:43], s53 offen lds\n"
    "v_mfma_f32_16x16x32_bf16 v[212:215], v[156:159], a[72:75], v[212:215]\n"
    "s_add_u32 s53, s53, 0x4000\n"
    "v_accvgpr_write_b32 a231, v249\n"
    "v_fma_f32 v217, v217, s56, v243\n"
    "v_mfma_f32_16x16x32_bf16 v[212:215], v[160:163], a[76:79], v[212:215]\n"
    "v_fma_f32 v218, v218, s56, v243\n"
    "v_fma_f32 v219, v219, s56, v243\n"
    "v_mfma_f32_16x16x32_bf16 v[212:215], v[164:167], a[80:83], v[212:215]\n"
    "v_exp_f32 v216, v216\n"
    "v_mfma_f32_16x16x32_bf16 v[212:215], v[168:171], a[84:87], v[212:215]\n"
    "v_exp_f32 v217, v217\n"
    "v_mfma_f32_16x16x32_bf16 v[212:215], v[172:175], a[88:91], v[212:215]\n"
    "ds_read_b128 v[184:187], v233 offset:576\n"
    "v_exp_f32 v218, v218\n"
    "v_exp_f32 v219, v219\n"
    "v_mfma_f32_16x16x32_bf16 v[212:215], v[176:179], a[92:95], v[212:215]\n"
    "s_add_u32 m0, s55, 12288\n"
    "v_cvt_pk_bf16_f32 v122, v216, v217\n"
    "v_cvt_pk_bf16_f32 v123, v218, v219\n"
    "v_mfma_f32_16x16x32_bf16 v[216:219], v[148:151], a[96:99], 0\n"
    "buffer_load_dwordx4 v231, s[40:43], s53 offen lds\n"
    "v_mfma_f32_16x16x32_bf16 v[216:219], v[152:155], a[100:103], v[216:219]\n"
    "s_add_u32 s53, s53, 0x4000\n"
    "ds_read_b128 v[188:191], v233 offset:640\n"
    "v_fma_f32 v220, v220, s56, v241\n"
    "v_mfma_f32_16x16x32_bf16 v[216:219], v[156:159], a[104:107], v[216:219]\n"
    "ds_read_b128 v[192:195], v233 offset:704\n"
    "v_fma_f32 v221, v221, s56, v241\n"
    "v_mfma_f32_16x16x32_bf16 v[216:219], v[160:163], a[108:111], v[216:219]\n"
    "v_fma_f32 v222, v222, s56, v241\n"
    "v_fma_f32 v223, v223, s56, v241\n"
    "v_mfma_f32_16x16x32_bf16 v[216:219], v[164:167], a[112:115], v[216:219]\n"
    "v_exp_f32 v220, v220\n"
    "v_mfma_f32_16x16x32_bf16 v[216:219], v[168:171], a[116:119], v[216:219]\n"
    "v_exp_f32 v221, v221\n"
    "v_mfma_f32_16x16x32_bf16 v[216:219], v[172:175], a[120:123], v[216:219]\n"
    "v_exp_f32 v222, v222\n"
    "v_exp_f32 v223, v223\n"
    "v_mfma_f32_16x16x32_bf16 v[216:219], v[176:179], a[124:127], v[216:219]\n"
    "s_add_u32 s96, s54, s63\n"
    "v_cvt_pk_bf16_f32 v250, v220, v221\n"
    "v_cvt_pk_bf16_f32 v251, v222, v223\n"
    "v_mfma_f32_16x16x32_bf16 v[220:223], v[148:151], a[32:35], 0\n"
    "s_add_u32 s54, s54, 16896\n"
    "ds_read_b128 v[196:199], v233 offset:768\n"
    "v_accvgpr_write_b32 a226, v250\n"
    "v_mfma_f32_16x16x32_bf16 v[220:223], v[152:155], a[36:39], v[220:223]\n"
    "s_sub_u32 s97, s54, 84480\n"
    "ds_read_b128 v[200:203], v233 offset:832\n"
    "v_accvgpr_write_b32 a227, v251\n"
    "v_mfma_f32_16x16x32_bf16 v[220:223], v[156:159], a[40:43], v[220:223]\n"
    "s_min_u32 s54, s54, s97\n"
    "v_fma_f32 v224, v224, s56, v240\n"
    "v_fma_f32 v225, v225, s56, v240\n"
    "v_mfma_f32_16x16x32_bf16 v[220:223], v[160:163], a[44:47], v[220:223]\n"
    "s_sub_u32 s97, s54, s66\n"
    "v_fma_f32 v226, v226, s56, v240\n"
    "v_fma_f32 v227, v227, s56, v240\n"
    "v_mfma_f32_16x16x32_bf16 v[220:223], v[164:167], a[48:51], v[220:223]\n"
    "s_mov_b32 s66, s54\n"
    "ds_read_b128 v[204:207], v233 offset:896\n"
    "v_fma_f32 v212, v212, s56, v242\n"
    "v_mfma_f32_16x16x32_bf16 v[220:223], v[168:171], a[52:55], v[220:223]\n"
    "v_exp_f32 v224, v224\n"
    "v_mfma_f32_16x16x32_bf16 v[220:223], v[172:175], a[56:59], v[220:223]\n"
    "v_exp_f32 v225, v225\n"
    "v_mfma_f32_16x16x32_bf16 v[220:223], v[176:179], a[60:63], v[220:223]\n"
    "v_exp_f32 v226, v226\n"
    "v_exp_f32 v227, v227\n"
    "v_mfma_f32_16x16x32_bf16 a[132:135], a[144:147], a[216:219], a[132:135]\n"
    "v_cvt_pk_bf16_f32 v118, v224, v225\n"
    "v_cvt_pk_bf16_f32 v119, v226, v227\n"
    "v_mfma_f32_16x16x32_bf16 v[224:227], v[148:151], a[0:3], 0\n"
    "ds_read_b128 v[208:211], v233 offset:960\n"
    "v_fma_f32 v213, v213, s56, v242\n"
    "v_mfma_f32_16x16x32_bf16 v[224:227], v[152:155], a[4:7], v[224:227]\n"
    "v_fma_f32 v214, v214, s56, v242\n"
    "v_fma_f32 v215, v215, s56, v242\n"
    "v_mfma_f32_16x16x32_bf16 v[224:227], v[156:159], a[8:11], v[224:227]\n"
    "v_exp_f32 v212, v212\n"
    "v_mfma_f32_16x16x32_bf16 v[224:227], v[160:163], a[12:15], v[224:227]\n"
    "v_exp_f32 v213, v213\n"
    "v_mfma_f32_16x16x32_bf16 v[224:227], v[164:167], a[16:19], v[224:227]\n"
    "v_exp_f32 v214, v214\n"
    "v_mfma_f32_16x16x32_bf16 v[224:227], v[168:171], a[20:23], v[224:227]\n"
    "v_exp_f32 v215, v215\n"
    "v_mfma_f32_16x16x32_bf16 v[224:227], v[172:175], a[24:27], v[224:227]\n"
    "v_cvt_pk_bf16_f32 v248, v212, v213\n"
    "v_cvt_pk_bf16_f32 v249, v214, v215\n"
    "v_mfma_f32_16x16x32_bf16 v[224:227], v[176:179], a[28:31], v[224:227]\n"
    "v_add_u32 v233, s97, v233\n"
    "v_accvgpr_write_b32 a236, v248\n"
    "v_mfma_f32_16x16x32_bf16 a[128:131], a[144:147], v[108:111], a[128:131]\n"
    "s_waitcnt vmcnt(9) lgkmcnt(0)\n"
    "ds_inc_u32 v228, v236\n"
    "v_mfma_f32_16x16x32_bf16 v[212:215], v[180:183], a[64:67], 0\n"
    "ds_read_b128 v[148:151], v233 offset:0\n"
    "v_accvgpr_write_b32 a237, v249\n"
    "v_mfma_f32_16x16x32_bf16 v[212:215], v[184:187], a[68:71], v[212:215]\n"
    "v_fma_f32 v216, v216, s56, v243\n"
    "v_fma_f32 v217, v217, s56, v243\n"
    "v_mfma_f32_16x16x32_bf16 v[212:215], v[188:191], a[72:75], v[212:215]\n"
    "s_add_u32 s61, s61, 1\n"
    "v_fma_f32 v218, v218, s56, v243\n"
    "v_fma_f32 v219, v219, s56, v243\n"
    "v_mfma_f32_16x16x32_bf16 v[212:215], v[192:195], a[76:79], v[212:215]\n"
    "v_exp_f32 v216, v216\n"
    "v_mfma_f32_16x16x32_bf16 v[212:215], v[196:199], a[80:83], v[212:215]\n"
    "v_exp_f32 v217, v217\n"
    "v_mfma_f32_16x16x32_bf16 v[212:215], v[200:203], a[84:87], v[212:215]\n"
    "v_exp_f32 v218, v218\n"
    "v_mfma_f32_16x16x32_bf16 v[212:215], v[204:207], a[88:91], v[212:215]\n"
    "ds_read_b128 v[152:155], v233 offset:64\n"
    "v_exp_f32 v219, v219\n"
    "v_mfma_f32_16x16x32_bf16 v[212:215], v[208:211], a[92:95], v[212:215]\n"
    "v_cvt_pk_bf16_f32 v128, v216, v217\n"
    "v_cvt_pk_bf16_f32 v129, v218, v219\n"
    "v_mfma_f32_16x16x32_bf16 v[216:219], v[180:183], a[96:99], 0\n"
    "ds_read_b128 v[156:159], v233 offset:128\n"
    "v_fma_f32 v220, v220, s56, v241\n"
    "v_mfma_f32_16x16x32_bf16 v[216:219], v[184:187], a[100:103], v[216:219]\n"
    "ds_read_b32 v229, v230\n"
    "v_fma_f32 v221, v221, s56, v241\n"
    "v_mfma_f32_16x16x32_bf16 v[216:219], v[188:191], a[104:107], v[216:219]\n"
    "ds_read_b128 v[160:163], v233 offset:192\n"
    "v_fma_f32 v222, v222, s56, v241\n"
    "v_mfma_f32_16x16x32_bf16 v[216:219], v[192:195], a[108:111], v[216:219]\n"
    "v_fma_f32 v223, v223, s56, v241\n"
    "v_fma_f32 v224, v224, s56, v240\n"
    "v_mfma_f32_16x16x32_bf16 v[216:219], v[196:199], a[112:115], v[216:219]\n"
    "v_exp_f32 v220, v220\n"
    "v_mfma_f32_16x16x32_bf16 v[216:219], v[200:203], a[116:119], v[216:219]\n"
    "s_waitcnt lgkmcnt(1)\n"
    "ds_read_b128 v[164:167], v233 offset:256\n"
    "v_cmp_gt_u32 vcc, s61, v229\n"
    "v_mfma_f32_16x16x32_bf16 v[216:219], v[204:207], a[120:123], v[216:219]\n"
    "v_exp_f32 v221, v221\n"
    "v_exp_f32 v222, v222\n"
    "v_exp_f32 v223, v223\n"
    "v_mfma_f32_16x16x32_bf16 v[216:219], v[208:211], a[124:127], v[216:219]\n"
    "v_cvt_pk_bf16_f32 v250, v220, v221\n"
    "v_cvt_pk_bf16_f32 v251, v222, v223\n"
    "v_mfma_f32_16x16x32_bf16 v[220:223], v[180:183], a[32:35], 0\n"
    "s_cbranch_vccnz L_sp13%=\n"
    "L_rt13%=:\n"
    "ds_read_b128 v[168:171], v233 offset:320\n"
    "v_mfma_f32_16x16x32_bf16 v[220:223], v[184:187], a[36:39], v[220:223]\n"
    "s_add_u32 m0, s96, 0\n"
    "v_accvgpr_write_b32 a232, v250\n"
    "v_accvgpr_write_b32 a233, v251\n"
    "v_mfma_f32_16x16x32_bf16 v[220:223], v[188:191], a[40:43], v[220:223]\n"
    "buffer_load_dwordx4 v231, s[40:43], s53 offen lds\n"
    "v_mfma_f32_16x16x32_bf16 v[220:223], v[192:195], a[44:47], v[220:223]\n"
    "s_add_u32 s53, s53, 0x4000\n"
    "v_fma_f32 v225, v225, s56, v240\n"
    "v_fma_f32 v226, v226, s56, v240\n"
    "v_mfma_f32_16x16x32_bf16 v[220:223], v[196:199], a[48:51], v[220:223]\n"
    "ds_read_b128 v[172:175], v233 offset:384\n"
    "v_fma_f32 v227, v227, s56, v240\n"
    "v_mfma_f32_16x16x32_bf16 v[220:223], v[200:203], a[52:55], v[220:223]\n"
    "v_exp_f32 v224, v224\n"
    "v_mfma_f32_16x16x32_bf16 v[220:223], v[204:207], a[56:59], v[220:223]\n"
    "v_exp_f32 v225, v225\n"
    "v_mfma_f32_16x16x32_bf16 v[220:223], v[208:211], a[60:63], v[220:223]\n"
    "v_exp_f32 v226, v226\n"
    "v_exp_f32 v227, v227\n"
    "v_mfma_f32_16x16x32_bf16 a[136:139], a[144:147], a[228:231], a[136:139]\n"
    "v_cvt_pk_bf16_f32 v124, v224, v225\n"
    "v_cvt_pk_bf16_f32 v125, v226, v227\n"
    "v_mfma_f32_16x16x32_bf16 v[224:227], v[180:183], a[0:3], 0\n"
    "s_add_u32 m0, s96, 4096\n"
    "ds_read_b128 v[176:179], v233 offset:448\n"
    "v_fma_f32 v212, v212, s56, v242\n"
    "v_mfma_f32_16x16x32_bf16 v[224:227], v[184:187], a[4:7], v[224:227]\n"
    "buffer_load_dwordx4 v231, s[40:43], s53 offen lds\n"
    "v_mfma_f32_16x16x32_bf16 v[224:227], v[188:191], a[8:11], v[224:227]\n"
    "s_add_u32 s53, s53, 0x4000\n"
    "v_fma_f32 v213, v213, s56, v242\n"
    "v_fma_f32 v214, v214, s56, v242\n"
    "v_mfma_f32_16x16x32_bf16 v[224:227], v[192:195], a[12:15], v[224:227]\n"
    "v_fma_f32 v215, v215, s56, v242\n"
    "v_fma_f32 v216, v216, s56, v243\n"
    "v_mfma_f32_16x16x32_bf16 v[224:227], v[196:199], a[16:19], v[224:227]\n"
    "v_exp_f32 v212, v212\n"
    "v_mfma_f32_16x16x32_bf16 v[224:227], v[200:203], a[20:23], v[224:227]\n"
    "v_exp_f32 v213, v213\n"
    "v_mfma_f32_16x16x32_bf16 v[224:227], v[204:207], a[24:27], v[224:227]\n"
    "v_exp_f32 v214, v214\n"
    "v_mfma_f32_16x16x32_bf16 v[224:227], v[208:211], a[28:31], v[224:227]\n"
    "v_exp_f32 v215, v215\n"
    "v_mfma_f32_16x16x32_bf16 a[140:143], a[144:147], v[120:123], a[140:143]\n"
    "s_waitcnt lgkmcnt(0)\n"
    "v_cvt_pk_bf16_f32 v248, v212, v213\n"
    "v_cvt_pk_bf16_f32 v249, v214, v215\n"
    "v_mfma_f32_16x16x32_bf16 v[212:215], v[148:151], a[64:67], 0\n"
    "s_add_u32 m0, s96, 8192\n"
    "ds_read_b128 v[180:183], v233 offset:512\n"
    "v_fma_f32 v217, v217, s56, v243\n"
    "v_mfma_f32_16x16x32_bf16 v[212:215], v[152:155], a[68:71], v[212:215]\n"
    "buffer_load_dwordx4 v231, s[40:43], s53 offen lds\n"
    "v_mfma_f32_16x16x32_bf16 v[212:215], v[156:159], a[72:75], v[212:215]\n"
    "s_add_u32 s53, s53, 0x4000\n"
    "v_fma_f32 v218, v218, s56, v243\n"
    "v_fma_f32 v219, v219, s56, v243\n"
    "v_mfma_f32_16x16x32_bf16 v[212:215], v[160:163], a[76:79], v[212:215]\n"
    "v_lshrrev_b32 v252, 8, v230\n"
    "v_accvgpr_write_b32 a238, v248\n"
    "v_mfma_f32_16x16x32_bf16 v[212:215], v[164:167], a[80:83], v[212:215]\n"
    "v_accvgpr_write_b32 a239, v249\n"
    "v_lshl_add_u32 v252, v252, 7, s22\n"
    "v_mfma_f32_16x16x32_bf16 v[212:215], v[168:171], a[84:87], v[212:215]\n"
    "v_exp_f32 v216, v216\n"
    "v_exp_f32 v217, v217\n"
    "v_exp_f32 v218, v218\n"
    "v_exp_f32 v219, v219\n"
    "v_mfma_f32_16x16x32_bf16 v[212:215], v[172:175], a[88:91], v[212:215]\n"
    "ds_read_b128 v[184:187], v233 offset:576\n"
    "v_cvt_pk_bf16_f32 v130, v216, v217\n"
    "v_cvt_pk_bf16_f32 v131, v218, v219\n"
    "v_min_u32 v252, L_end%=-L_pf%=-4, v252\n"
    "v_mfma_f32_16x16x32_bf16 v[212:215], v[176:179], a[92:95], v[212:215]\n"
    "global_load_dword v253, v252, s[20:21]\n"
    "v_mfma_f32_16x16x32_bf16 v[216:219], v[148:151], a[96:99], 0\n"
    "ds_read_b128 v[188:191], v233 offset:640\n"
    "v_fma_f32 v220, v220, s56, v241\n"
    "v_mfma_f32_16x16x32_bf16 v[216:219], v[152:155], a[100:103], v[216:219]\n"
    "v_fma_f32 v221, v221, s56, v241\n"
    "v_fma_f32 v222, v222, s56, v241\n"
    "v_mfma_f32_16x16x32_bf16 v[216:219], v[156:159], a[104:107], v[216:219]\n"
    "ds_read_b128 v[192:195], v233 offset:704\n"
    "v_fma_f32 v223, v223, s56, v241\n"
    "v_mfma_f32_16x16x32_bf16 v[216:219], v[160:163], a[108:111], v[216:219]\n"
    "v_exp_f32 v220, v220\n"
    "v_mfma_f32_16x16x32_bf16 v[216:219], v[164:167], a[112:115], v[216:219]\n"
    "s_add_u32 m0, s96, 12288\n"
    "v_exp_f32 v221, v221\n"
    "v_mfma_f32_16x16x32_bf16 v[216:219], v[168:171], a[116:119], v[216:219]\n"
    "buffer_load_dwordx4 v231, s[40:43], s53 offen lds\n"
    "v_mfma_f32_16x16x32_bf16 v[216:219], v[172:175], a[120:123], v[216:219]\n"
    "v_exp_f32 v222, v222\n"
    "v_exp_f32 v223, v223\n"
    "v_mfma_f32_16x16x32_bf16 v[216:219], v[176:179], a[124:127], v[216:219]\n"
    "s_add_u32 s53, s53, 0x4000\n"
    "v_cvt_pk_bf16_f32 v250, v220, v221\n"
    "v_cvt_pk_bf16_f32 v251, v222, v223\n"
    "v_mfma_f32_16x16x32_bf16 v[220:223], v[148:151], a[32:35], 0\n"
    "s_add_u32 s55, s54, s63\n"
    "ds_read_b128 v[196:199], v233 offset:768\n"
    "v_accvgpr_write_b32 a234, v250\n"
    "v_mfma_f32_16x16x32_bf16 v[220:223], v[152:155], a[36:39], v[220:223]\n"
    "s_add_u32 s54, s54, 16896\n"
    "ds_read_b128 v[200:203], v233 offset:832\n"
    "v_accvgpr_write_b32 a235, v251\n"
    "v_mfma_f32_16x16x32_bf16 v[220:223], v[156:159], a[40:43], v[220:223]\n"
    "s_sub_u32 s97, s54, 84480\n"
    "v_fma_f32 v224, v224, s56, v240\n"
    "v_fma_f32 v225, v225, s56, v240\n"
    "v_mfma_f32_16x16x32_bf16 v[220:223], v[160:163], a[44:47], v[220:223]\n"
    "s_min_u32 s54, s54, s97\n"
    "v_fma_f32 v226, v226, s56, v240\n"
    "v_fma_f32 v227, v227, s56, v240\n"
    "v_mfma_f32_16x16x32_bf16 v[220:223], v[164:167], a[48:51], v[220:223]\n"
    "s_sub_u32 s97, s54, s66\n"
    "ds_read_b128 v[204:207], v233 offset:896\n"
    "v_fma_f32 v212, v212, s56, v242\n"
    "v_mfma_f32_16x16x32_bf16 v[220:223], v[168:171], a[52:55], v[220:223]\n"
    "s_mov_b32 s66, s54\n"
    "v_exp_f32 v224, v224\n"
    "v_mfma_f32_16x16x32_bf16 v[220:223], v[172:175], a[56:59], v[220:223]\n"
    "v_exp_f32 v225, v225\n"
    "v_mfma_f32_16x16x32_bf16 v[220:223], v[176:179], a[60:63], v[220:223]\n"
    "v_exp_f32 v226, v226\n"
    "v_exp_f32 v227, v227\n"
    "v_mfma_f32_16x16x32_bf16 a[132:135], a[144:147], a[224:227], a[132:135]\n"
    "v_cvt_pk_bf16_f32 v126, v224, v225\n"
    "v_cvt_pk_bf16_f32 v127, v226, v227\n"
    "v_mfma_f32_16x16x32_bf16 v[224:227], v[148:151], a[0:3], 0\n"
    "ds_read_b128 v[208:211], v233 offset:960\n"
    "v_fma_f32 v213, v213, s56, v242\n"
    "v_mfma_f32_16x16x32_bf16 v[224:227], v[152:155], a[4:7], v[224:227]\n"
    "v_fma_f32 v214, v214, s56, v242\n"
    "v_fma_f32 v215, v215, s56, v242\n"
    "v_mfma_f32_16x16x32_bf16 v[224:227], v[156:159], a[8:11], v[224:227]\n"
    "v_exp_f32 v212, v212\n"
    "v_mfma_f32_16x16x32_bf16 v[224:227], v[160:163], a[12:15], v[224:227]\n"
    "v_exp_f32 v213, v213\n"
    "v_mfma_f32_16x16x32_bf16 v[224:227], v[164:167], a[16:19], v[224:227]\n"
    "v_exp_f32 v214, v214\n"
    "v_mfma_f32_16x16x32_bf16 v[224:227], v[168:171], a[20:23], v[224:227]\n"
    "v_exp_f32 v215, v215\n"
    "v_mfma_f32_16x16x32_bf16 v[224:227], v[172:175], a[24:27], v[224:227]\n"
    "v_cvt_pk_bf16_f32 v248, v212, v213\n"
    "v_cvt_pk_bf16_f32 v249, v214, v215\n"
    "v_mfma_f32_16x16x32_bf16 v[224:227], v[176:179], a[28:31], v[224:227]\n"
    "v_add_u32 v233, s97, v233\n"
    "v_accvgpr_write_b32 a244, v248\n"
    "v_mfma_f32_16x16x32_bf16 a[128:131], a[144:147], v[116:119], a[128:131]\n"
    "s_waitcnt vmcnt(9) lgkmcnt(0)\n"
    "ds_inc_u32 v228, v236\n"
    "v_mfma_f32_16x16x32_bf16 v[212:215], v[180:183], a[64:67], 0\n"
    "ds_read_b128 v[148:151], v233 offset:0\n"
    "v_accvgpr_write_b32 a245, v249\n"
    "v_mfma_f32_16x16x32_bf16 v[212:215], v[184:187], a[68:71], v[212:215]\n"
    "v_fma_f32 v216, v216, s56, v243\n"
    "v_fma_f32 v217, v217, s56, v243\n"
    "v_mfma_f32_16x16x32_bf16 v[212:215], v[188:191], a[72:75], v[212:215]\n"
    "s_add_u32 s61, s61, 1\n"
    "v_fma_f32 v218, v218, s56, v243\n"
    "v_fma_f32 v219, v219, s56, v243\n"
    "v_mfma_f32_16x16x32_bf16 v[212:215], v[192:195], a[76:79], v[212:215]\n"
    "v_exp_f32 v216, v216\n"
    "v_mfma_f32_16x16x32_bf16 v[212:215], v[196:199], a[80:83], v[212:215]\n"
    "v_exp_f32 v217, v217\n"
    "v_mfma_f32_16x16x32_bf16 v[212:215], v[200:203], a[84:87], v[212:215]\n"
    "v_exp_f32 v218, v218\n"
    "v_mfma_f32_16x16x32_bf16 v[212:215], v[204:207], a[88:91], v[212:215]\n"
    "ds_read_b128 v[152:155], v233 offset:64\n"
    "v_exp_f32 v219, v219\n"
    "v_mfma_f32_16x16x32_bf16 v[212:215], v[208:211], a[92:95], v[212:215]\n"
    "v_cvt_pk_bf16_f32 v136, v216, v217\n"
    "v_cvt_pk_bf16_f32 v137, v218, v219\n"
    "v_mfma_f32_16x16x32_bf16 v[216:219], v[180:183], a[96:99], 0\n"
    "ds_read_b128 v[156:159], v233 offset:128\n"
    "v_fma_f32 v220, v220, s56, v241\n"
    "v_mfma_f32_16x16x32_bf16 v[216:219], v[184:187], a[100:103], v[216:219]\n"
    "ds_read_b32 v229, v230\n"
    "v_fma_f32 v221, v221, s56, v241\n"
    "v_mfma_f32_16x16x32_bf16 v[216:219], v[188:191], a[104:107], v[216:219]\n"
    "ds_read_b128 v[160:163], v233 offset:192\n"
    "v_fma_f32 v222, v222, s56, v241\n"
    "v_mfma_f32_16x16x32_bf16 v[216:219], v[192:195], a[108:111], v[216:219]\n"
    "v_fma_f32 v223, v223, s56, v241\n"
    "v_fma_f32 v224, v224, s56, v240\n"
    "v_mfma_f32_16x16x32_bf16 v[216:219], v[196:199], a[112:115], v[216:219]\n"
    "v_exp_f32 v220, v220\n"
    "v_mfma_f32_16x16x32_bf16 v[216:219], v[200:203], a[116:119], v[216:219]\n"
    "s_waitcnt lgkmcnt(1)\n"
    "ds_read_b128 v[164:167], v233 offset:256\n"
    "v_cmp_gt_u32 vcc, s61, v229\n"
    "v_mfma_f32_16x16x32_bf16 v[216:219], v[204:207], a[120:123], v[216:219]\n"
    "v_exp_f32 v221, v221\n"
    "v_exp_f32 v222, v222\n"
    "v_exp_f32 v223, v223\n"
    "v_mfma_f32_16x16x32_bf16 v[216:219], v[208:211], a[124:127], v[216:219]\n"
    "v_cvt_pk_bf16_f32 v250, v220, v221\n"
    "v_cvt_pk_bf16_f32 v251, v222, v223\n"
    "v_mfma_f32_16x16x32_bf16 v[220:223], v[180:183], a[32:35], 0\n"
    "s_cbranch_vccnz L_sp14%=\n"
    "L_rt14%=:\n"
    "ds_read_b128 v[168:171], v233 offset:320\n"
    "v_mfma_f32_16x16x32_bf16 v[220:223], v[184:187], a[36:39], v[220:223]\n"
    "s_add_u32 m0, s55, 0\n"
    "v_accvgpr_write_b32 a240, v250\n"
    "v_accvgpr_write_b32 a241, v251\n"
    "v_mfma_f32_16x16x32_bf16 v[220:223], v[188:191], a[40:43], v[220:223]\n"
    "buffer_load_dwordx4 v231, s[40:43], s53 offen lds\n"
    "v_mfma_f32_16x16x32_bf16 v[220:223], v[192:195], a[44:47], v[220:223]\n"
    "s_add_u32 s53, s53, 0x4000\n"
    "v_fma_f32 v225, v225, s56, v240\n"
    "v_fma_f32 v226, v226, s56, v240\n"
    "v_mfma_f32_16x16x32_bf16 v[220:223], v[196:199], a[48:51], v[220:223]\n"
    "ds_read_b128 v[172:175], v233 offset:384\n"
    "v_fma_f32 v227, v227, s56, v240\n"
    "v_mfma_f32_16x16x32_bf16 v[220:223], v[200:203], a[52:55], v[220:223]\n"
    "v_exp_f32 v224, v224\n"
    "v_mfma_f32_16x16x32_bf16 v[220:223], v[204:207], a[56:59], v[220:223]\n"
    "v_exp_f32 v225, v225\n"
    "v_mfma_f32_16x16x32_bf16 v[220:223], v[208:211], a[60:63], v[220:223]\n"
    "v_exp_f32 v226, v226\n"
    "v_exp_f32 v227, v227\n"
    "v_mfma_f32_16x16x32_bf16 a[136:139], a[144:147], a[236:239], a[136:139]\n"
    "v_cvt_pk_bf16_f32 v132, v224, v225\n"
    "v_cvt_pk_bf16_f32 v133, v226, v227\n"
    "v_mfma_f32_16x16x32_bf16 v[224:227], v[180:183], a[0:3], 0\n"
    "s_add_u32 m0, s55, 4096\n"
    "ds_read_b128 v[176:179], v233 offset:448\n"
    "v_fma_f32 v212, v212, s56, v242\n"
    "v_mfma_f32_16x16x32_bf16 v[224:227], v[184:187], a[4:7], v[224:227]\n"
    "buffer_load_dwordx4 v231, s[40:43], s53 offen lds\n"
    "v_mfma_f32_16x16x32_bf16 v[224:227], v[188:191], a[8:11], v[224:227]\n"
    "s_add_u32 s53, s53, 0x4000\n"
    "v_fma_f32 v213, v213, s56, v242\n"
    "v_fma_f32 v214, v214, s56, v242\n"
    "v_mfma_f32_16x16x32_bf16 v[224:227], v[192:195], a[12:15], v[224:227]\n"
    "v_fma_f32 v215, v215, s56, v242\n"
    "v_fma_f32 v216, v216, s56, v243\n"
    "v_mfma_f32_16x16x32_bf16 v[224:227], v[196:199], a[16:19], v[224:227]\n"
    "v_exp_f32 v212, v212\n"
    "v_mfma_f32_16x16x32_bf16 v[224:227], v[200:203], a[20:23], v[224:227]\n"
    "v_exp_f32 v213, v213\n"
    "v_mfma_f32_16x16x32_bf16 v[224:227], v[204:207], a[24:27], v[224:227]\n"
    "v_exp_f32 v214, v214\n"
    "v_mfma_f32_16x16x32_bf16 v[224:227], v[208:211], a[28:31], v[224:227]\n"
    "v_exp_f32 v215, v215\n"
    "v_mfma_f32_16x16x32_bf16 a[140:143], a[144:147], v[128:131], a[140:143]\n"
    "s_waitcnt lgkmcnt(0)\n"
    "v_cvt_pk_bf16_f32 v248, v212, v213\n"
    "v_cvt_pk_bf16_f32 v249, v214, v215\n"
    "v_mfma_f32_16x16x32_bf16 v[212:215], v[148:151], a[64:67], 0\n"
    "s_add_u32 m0, s55, 8192\n"
    "ds_read_b128 v[180:183], v233 offset:512\n"
    "v_accvgpr_write_b32 a246, v248\n"
    "v_mfma_f32_16x16x32_bf16 v[212:215], v[152:155], a[68:71], v[212:215]\n"
    "buffer_load_dwordx4 v231, s[40:43], s53 offen lds\n"
    "v_mfma_f32_16x16x32_bf16 v[212:215], v[156:159], a[72:75], v[212:215]\n"
    "s_add_u32 s53, s53, 0x4000\n"
    "v_accvgpr_write_b32 a247, v249\n"
    "v_fma_f32 v217, v217, s56, v243\n"
    "v_mfma_f32_16x16x32_bf16 v[212:215], v[160:163], a[76:79], v[212:215]\n"
    "v_fma_f32 v218, v218, s56, v243\n"
    "v_fma_f32 v219, v219, s56, v243\n"
    "v_mfma_f32_16x16x32_bf16 v[212:215], v[164:167], a[80:83], v[212:215]\n"
    "v_exp_f32 v216, v216\n"
    "v_mfma_f32_16x16x32_bf16 v[212:215], v[168:171], a[84:87], v[212:215]\n"
    "v_exp_f32 v217, v217\n"
    "v_mfma_f32_16x16x32_bf16 v[212:215], v[172:175], a[88:91], v[212:215]\n"
    "ds_read_b128 v[184:187], v233 offset:576\n"
    "v_exp_f32 v218, v218\n"
    "v_exp_f32 v219, v219\n"
    "v_mfma_f32_16x16x32_bf16 v[212:215], v[176:179], a[92:95], v[212:215]\n"
    "s_add_u32 m0, s55, 12288\n"
    "v_cvt_pk_bf16_f32 v138, v216, v217\n"
    "v_cvt_pk_bf16_f32 v139, v218, v219\n"
    "v_mfma_f32_16x16x32_bf16 v[216:219], v[148:151], a[96:99], 0\n"
    "buffer_load_dwordx4 v231, s[40:43], s53 offen lds\n"
    "v_mfma_f32_16x16x32_bf16 v[216:219], v[152:155], a[100:103], v[216:219]\n"
    "ds_read_b128 v[188:191], v233 offset:640\n"
    "v_fma_f32 v220, v220, s56, v241\n"
    "v_mfma_f32_16x16x32_bf16 v[216:219], v[156:159], a[104:107], v[216:219]\n"
    "ds_read_b128 v[192:195], v233 offset:704\n"
    "v_fma_f32 v221, v221, s56, v241\n"
    "v_mfma_f32_16x16x32_bf16 v[216:219], v[160:163], a[108:111], v[216:219]\n"
    "s_add_u32 s53, s53, 0xfffc4000\n"
    "s_xor_b32 s53, s53, 128\n"
    "v_fma_f32 v222, v222, s56, v241\n"
    "v_mfma_f32_16x16x32_bf16 v[216:219], v[164:167], a[112:115], v[216:219]\n"
    "v_fma_f32 v223, v223, s56, v241\n"
    "v_fma_f32 v224, v224, s56, v240\n"
    "v_mfma_f32_16x16x32_bf16 v[216:219], v[168:171], a[116:119], v[216:219]\n"
    "v_exp_f32 v220, v220\n"
    "v_mfma_f32_16x16x32_bf16 v[216:219], v[172:175], a[120:123], v[216:219]\n"
    "v_exp_f32 v221, v221\n"
    "v_exp_f32 v222, v222\n"
    "v_exp_f32 v223, v223\n"
    "v_mfma_f32_16x16x32_bf16 v[216:219], v[176:179], a[124:127], v[216:219]\n"
    "s_add_u32 s96, s54, s63\n"
    "v_cvt_pk_bf16_f32 v250, v220, v221\n"
    "v_cvt_pk_bf16_f32 v251, v222, v223\n"
    "v_mfma_f32_16x16x32_bf16 v[220:223], v[148:151], a[32:35], 0\n"
    "s_add_u32 s54, s54, 16896\n"
    "ds_read_b128 v[196:199], v233 offset:768\n"
    "v_accvgpr_write_b32 a242, v250\n"
    "v_mfma_f32_16x16x32_bf16 v[220:223], v[152:155], a[36:39], v[220:223]\n"
    "s_sub_u32 s97, s54, 84480\n"
    "ds_read_b128 v[200:203], v233 offset:832\n"
    "v_accvgpr_write_b32 a243, v251\n"
    "v_mfma_f32_16x16x32_bf16 v[220:223], v[156:159], a[40:43], v[220:223]\n"
    "s_min_u32 s54, s54, s97\n"
    "v_fma_f32 v225, v225, s56, v240\n"
    "v_fma_f32 v226, v226, s56, v240\n"
    "v_mfma_f32_16x16x32_bf16 v[220:223], v[160:163], a[44:47], v[220:223]\n"
    "s_sub_u32 s97, s54, s67\n"
    "ds_read_b128 v[204:207], v233 offset:896\n"
    "v_fma_f32 v227, v227, s56, v240\n"
    "v_mfma_f32_16x16x32_bf16 v[220:223], v[164:167], a[48:51], v[220:223]\n"
    "s_mov_b32 s67, s54\n"
    "v_exp_f32 v224, v224\n"
    "v_mfma_f32_16x16x32_bf16 v[220:223], v[168:171], a[52:55], v[220:223]\n"
    "v_exp_f32 v225, v225\n"
    "v_mfma_f32_16x16x32_bf16 v[220:223], v[172:175], a[56:59], v[220:223]\n"
    "v_exp_f32 v226, v226\n"
    "v_mfma_f32_16x16x32_bf16 v[220:223], v[176:179], a[60:63], v[220:223]\n"
    "v_exp_f32 v227, v227\n"
    "v_mfma_f32_16x16x32_bf16 a[132:135], a[144:147], a[232:235], a[132:135]\n"
    "v_cvt_pk_bf16_f32 v134, v224, v225\n"
    "v_cvt_pk_bf16_f32 v135, v226, v227\n"
    "v_mfma_f32_16x16x32_bf16 v[224:227], v[148:151], a[0:3], 0\n"
    "ds_read_b128 v[208:211], v233 offset:960\n"
    "v_fma_f32 v212, v212, s56, v242\n"
    "v_mfma_f32_16x16x32_bf16 v[224:227], v[152:155], a[4:7], v[224:227]\n"
    "v_fma_f32 v213, v213, s56, v242\n"
    "v_fma_f32 v214, v214, s56, v242\n"
    "v_mfma_f32_16x16x32_bf16 v[224:227], v[156:159], a[8:11], v[224:227]\n"
    "v_fma_f32 v215, v215, s56, v242\n"
    "v_add_u32 v234, s97, v234\n"
    "v_mfma_f32_16x16x32_bf16 v[224:227], v[160:163], a[12:15], v[224:227]\n"
    "s_add_u32 s58, s72, 8192\n"
    "v_exp_f32 v212, v212\n"
    "v_mfma_f32_16x16x32_bf16 v[224:227], v[164:167], a[16:19], v[224:227]\n"
    "v_exp_f32 v213, v213\n"
    "v_mfma_f32_16x16x32_bf16 v[224:227], v[168:171], a[20:23], v[224:227]\n"
    "v_exp_f32 v214, v214\n"
    "v_mfma_f32_16x16x32_bf16 v[224:227], v[172:175], a[24:27], v[224:227]\n"
    "v_exp_f32 v215, v215\n"
    "v_mfma_f32_16x16x32_bf16 v[224:227], v[176:179], a[28:31], v[224:227]\n"
    "v_cvt_pk_bf16_f32 v248, v212, v213\n"
    "v_cvt_pk_bf16_f32 v249, v214, v215\n"
    "v_mfma_f32_16x16x32_bf16 a[128:131], a[144:147], v[124:127], a[128:131]\n"
    "s_waitcnt vmcnt(9) lgkmcnt(0)\n"
    "v_xor_b32 v235, 32, v234\n"
    "v_accvgpr_write_b32 a252, v248\n"
    "v_mfma_f32_16x16x32_bf16 v[212:215], v[180:183], a[0:3], 0\n"
    "ds_inc_u32 v228, v236\n"
    "v_mfma_f32_16x16x32_bf16 v[212:215], v[184:187], a[4:7], v[212:215]\n"
    "v_accvgpr_write_b32 a253, v249\n"
    "v_fma_f32 v216, v216, s56, v243\n"
    "v_mfma_f32_16x16x32_bf16 v[212:215], v[188:191], a[8:11], v[212:215]\n"
    "s_add_u32 s61, s61, 1\n"
    "v_fma_f32 v217, v217, s56, v243\n"
    "v_fma_f32 v218, v218, s56, v243\n"
    "v_mfma_f32_16x16x32_bf16 v[212:215], v[192:195], a[12:15], v[212:215]\n"
    "v_fma_f32 v219, v219, s56, v243\n"
    "v_fma_f32 v220, v220, s56, v241\n"
    "v_mfma_f32_16x16x32_bf16 v[212:215], v[196:199], a[16:19], v[212:215]\n"
    "v_exp_f32 v216, v216\n"
    "v_mfma_f32_16x16x32_bf16 v[212:215], v[200:203], a[20:23], v[212:215]\n"
    "v_exp_f32 v217, v217\n"
    "v_mfma_f32_16x16x32_bf16 v[212:215], v[204:207], a[24:27], v[212:215]\n"
    "v_exp_f32 v218, v218\n"
    "v_exp_f32 v219, v219\n"
    "v_mfma_f32_16x16x32_bf16 v[212:215], v[208:211], a[28:31], v[212:215]\n"
    "v_cvt_pk_bf16_f32 v144, v216, v217\n"
    "v_cvt_pk_bf16_f32 v145, v218, v219\n"
    "v_mfma_f32_16x16x32_bf16 v[216:219], v[180:183], a[64:67], 0\n"
    "ds_read_b32 v229, v230\n"
    "v_fma_f32 v221, v221, s56, v241\n"
    "v_mfma_f32_16x16x32_bf16 v[216:219], v[184:187], a[68:71], v[216:219]\n"
    "v_fma_f32 v222, v222, s56, v241\n"
    "v_fma_f32 v223, v223, s56, v241\n"
    "v_mfma_f32_16x16x32_bf16 v[216:219], v[188:191], a[72:75], v[216:219]\n"
    "ds_read_b64_tr_b16 a[0:1], v234 offset:0\n"
    "v_fma_f32 v224, v224, s56, v240\n"
    "v_mfma_f32_16x16x32_bf16 v[216:219], v[192:195], a[76:79], v[216:219]\n"
    "v_exp_f32 v220, v220\n"
    "v_mfma_f32_16x16x32_bf16 v[216:219], v[196:199], a[80:83], v[216:219]\n"
    "v_exp_f32 v221, v221\n"
    "v_mfma_f32_16x16x32_bf16 v[216:219], v[200:203], a[84:87], v[216:219]\n"
    "s_waitcnt lgkmcnt(1)\n"
    "ds_read_b64_tr_b16 a[2:3], v234 offset:2048\n"
    "v_cmp_gt_u32 vcc, s61, v229\n"
    "v_mfma_f32_16x16x32_bf16 v[216:219], v[204:207], a[88:91], v[216:219]\n"
    "v_exp_f32 v222, v222\n"
    "v_exp_f32 v223, v223\n"
    "v_mfma_f32_16x16x32_bf16 v[216:219], v[208:211], a[92:95], v[216:219]\n"
    "ds_read_b64_tr_b16 a[4:5], v234 offset:4096\n"
    "v_cvt_pk_bf16_f32 v250, v220, v221\n"
    "v_cvt_pk_bf16_f32 v251, v222, v223\n"
    "v_mfma_f32_16x16x32_bf16 v[220:223], v[180:183], a[96:99], 0\n"
    "s_cbranch_vccnz L_sp15%=\n"
    "L_rt15%=:\n"
    "ds_read_b64_tr_b16 a[6:7], v234 offset:6144\n"
    "v_mfma_f32_16x16x32_bf16 v[220:223], v[184:187], a[100:103], v[220:223]\n"
    "s_add_u32 m0, s96, 0\n"
    "ds_read_b64_tr_b16 a[8:9], v234 offset:8192\n"
    "v_accvgpr_write_b32 a248, v250\n"
    "v_mfma_f32_16x16x32_bf16 v[220:223], v[188:191], a[104:107], v[220:223]\n"
    "buffer_load_dwordx4 v231, s[40:43], s53 offen lds\n"
    "v_mfma_f32_16x16x32_bf16 v[220:223], v[192:195], a[108:111], v[220:223]\n"
    "s_add_u32 s53, s53, 0x4000\n"
    "ds_read_b64_tr_b16 a[10:11], v234 offset:10240\n"
    "v_accvgpr_write_b32 a249, v251\n"
    "v_mfma_f32_16x16x32_bf16 v[220:223], v[196:199], a[112:115], v[220:223]\n"
    "ds_read_b64_tr_b16 a[12:13], v234 offset:12288\n"
    "v_fma_f32 v225, v225, s56, v240\n"
    "v_mfma_f32_16x16x32_bf16 v[220:223], v[200:203], a[116:119], v[220:223]\n"
    "v_fma_f32 v226, v226, s56, v240\n"
    "v_fma_f32 v227, v227, s56, v240\n"
    "v_mfma_f32_16x16x32_bf16 v[220:223], v[204:207], a[120:123], v[220:223]\n"
    "ds_read_b64_tr_b16 a[14:15], v234 offset:14336\n"
    "v_fma_f32 v212, v212, s56, v240\n"
    "v_mfma_f32_16x16x32_bf16 v[220:223], v[208:211], a[124:127], v[220:223]\n"
    "v_exp_f32 v224, v224\n"
    "v_exp_f32 v225, v225\n"
    "v_exp_f32 v226, v226\n"
    "v_exp_f32 v227, v227\n"
    "v_mfma_f32_16x16x32_bf16 a[136:139], a[144:147], a[244:247], a[136:139]\n"
    "v_cvt_pk_bf16_f32 v140, v224, v225\n"
    "v_cvt_pk_bf16_f32 v141, v226, v227\n"
    "v_mfma_f32_16x16x32_bf16 v[224:227], v[180:183], a[32:35], 0\n"
    "s_add_u32 m0, s96, 4096\n"
    "v_fma_f32 v213, v213, s56, v240\n"
    "v_fma_f32 v214, v214, s56, v240\n"
    "v_mfma_f32_16x16x32_bf16 v[224:227], v[184:187], a[36:39], v[224:227]\n"
    "buffer_load_dwordx4 v231, s[40:43], s53 offen lds\n"
    "v_mfma_f32_16x16x32_bf16 v[224:227], v[188:191], a[40:43], v[224:227]\n"
    "s_add_u32 s53, s53, 0x4000\n"
    "v_fma_f32 v215, v215, s56, v240\n"
    "v_fma_f32 v216, v216, s56, v242\n"
    "v_mfma_f32_16x16x32_bf16 v[224:227], v[192:195], a[44:47], v[224:227]\n"
    "v_exp_f32 v212, v212\n"
    "v_mfma_f32_16x16x32_bf16 v[224:227], v[196:199], a[48:51], v[224:227]\n"
    "v_exp_f32 v213, v213\n"
    "v_mfma_f32_16x16x32_bf16 v[224:227], v[200:203], a[52:55], v[224:227]\n"
    "v_exp_f32 v214, v214\n"
    "v_mfma_f32_16x16x32_bf16 v[224:227], v[204:207], a[56:59], v[224:227]\n"
    "v_exp_f32 v215, v215\n"
    "v_mfma_f32_16x16x32_bf16 v[224:227], v[208:211], a[60:63], v[224:227]\n"
    "v_cvt_pk_bf16_f32 v142, v212, v213\n"
    "v_cvt_pk_bf16_f32 v143, v214, v215\n"
    "v_mfma_f32_16x16x32_bf16 a[140:143], a[144:147], v[136:139], a[140:143]\n"
    "s_waitcnt lgkmcnt(0)\n"
    "v_fma_f32 v217, v217, s56, v242\n"
    "v_fma_f32 v218, v218, s56, v242\n"
    "v_mfma_f32_16x16x32_bf16 v[148:151], a[0:3], v[0:3], 0\n"
    "ds_read_b64_tr_b16 a[16:17], v235 offset:0\n"
    "v_fma_f32 v219, v219, s56, v242\n"
    "v_mfma_f32_16x16x32_bf16 v[148:151], a[4:7], v[16:19], v[148:151]\n"
    "ds_read_b64_tr_b16 a[18:19], v235 offset:2048\n"
    "v_fma_f32 v220, v220, s56, v243\n"
    "v_mfma_f32_16x16x32_bf16 v[148:151], a[8:11], a[148:151], v[148:151]\n"
    "ds_read_b64_tr_b16 a[20:21], v235 offset:4096\n"
    "v_fma_f32 v221, v221, s56, v243\n"
    "v_mfma_f32_16x16x32_bf16 v[148:151], a[12:15], a[156:159], v[148:151]\n"
    "ds_read_b64_tr_b16 a[22:23], v235 offset:6144\n"
    "v_fma_f32 v222, v222, s56, v243\n"
    "v_mfma_f32_16x16x32_bf16 v[152:155], a[0:3], v[4:7], 0\n"
    "ds_read_b64_tr_b16 a[24:25], v235 offset:8192\n"
    "v_fma_f32 v223, v223, s56, v243\n"
    "v_mfma_f32_16x16x32_bf16 v[152:155], a[4:7], v[20:23], v[152:155]\n"
    "ds_read_b64_tr_b16 a[26:27], v235 offset:10240\n"
    "v_fma_f32 v224, v224, s56, v241\n"
    "v_mfma_f32_16x16x32_bf16 v[152:155], a[8:11], v[32:35], v[152:155]\n"
    "ds_read_b64_tr_b16 a[28:29], v235 offset:12288\n"
    "v_fma_f32 v225, v225, s56, v241\n"
    "v_mfma_f32_16x16x32_bf16 v[152:155], a[12:15], v[40:43], v[152:155]\n"
    "s_waitcnt vmcnt(19)\n"
    "ds_read_b64_tr_b16 a[30:31], v235 offset:14336\n"
    "v_fma_f32 v226, v226, s56, v241\n"
    "v_mfma_f32_16x16x32_bf16 v[156:159], a[0:3], v[8:11], 0\n"
    "ds_read_b128 a[64:67], v236 offset:0\n"
    "v_fma_f32 v227, v227, s56, v241\n"
    "v_mfma_f32_16x16x32_bf16 v[156:159], a[4:7], v[24:27], v[156:159]\n"
    "ds_read_b128 a[68:71], v236 offset:64\n"
    "v_mfma_f32_16x16x32_bf16 v[156:159], a[8:11], v[36:39], v[156:159]\n"
    "ds_read_b128 a[72:75], v236 offset:128\n"
    "v_mfma_f32_16x16x32_bf16 v[156:159], a[12:15], v[44:47], v[156:159]\n"
    "ds_read_b128 a[76:79], v236 offset:192\n"
    "v_mfma_f32_16x16x32_bf16 v[160:163], a[0:3], v[12:15], 0\n"
    "ds_read_b128 a[80:83], v236 offset:256\n"
    "v_mfma_f32_16x16x32_bf16 v[160:163], a[4:7], v[28:31], v[160:163]\n"
    "ds_read_b128 a[84:87], v236 offset:320\n"
    "v_mfma_f32_16x16x32_bf16 v[160:163], a[8:11], a[152:155], v[160:163]\n"
    "ds_read_b128 a[88:91], v236 offset:384\n"
    "v_mfma_f32_16x16x32_bf16 v[160:163], a[12:15], a[160:163], v[160:163]\n"
    "s_waitcnt lgkmcnt(7)\n"
    "ds_read_b128 a[92:95], v236 offset:448\n"
    "v_mfma_f32_16x16x32_bf16 v[164:167], a[16:19], v[0:3], 0\n"
    "ds_read_b64_tr_b16 a[0:1], v234 offset:512\n"
    "v_mfma_f32_16x16x32_bf16 v[164:167], a[20:23], v[16:19], v[164:167]\n"
    "ds_read_b64_tr_b16 a[2:3], v234 offset:2560\n"
    "v_mfma_f32_16x16x32_bf16 v[164:167], a[24:27], a[148:151], v[164:167]\n"
    "ds_read_b64_tr_b16 a[4:5], v234 offset:4608\n"
    "v_mfma_f32_16x16x32_bf16 v[164:167], a[28:31], a[156:159], v[164:167]\n"
    "ds_read_b64_tr_b16 a[6:7], v234 offset:6656\n"
    "v_mfma_f32_16x16x32_bf16 v[168:171], a[16:19], v[4:7], 0\n"
    "ds_read_b64_tr_b16 a[8:9], v234 offset:8704\n"
    "v_mfma_f32_16x16x32_bf16 v[168:171], a[20:23], v[20:23], v[168:171]\n"
    "ds_read_b64_tr_b16 a[10:11], v234 offset:10752\n"
    "v_mfma_f32_16x16x32_bf16 v[168:171], a[24:27], v[32:35], v[168:171]\n"
    "ds_read_b64_tr_b16 a[12:13], v234 offset:12800\n"
    "v_mfma_f32_16x16x32_bf16 v[168:171], a[28:31], v[40:43], v[168:171]\n"
    "ds_read_b64_tr_b16 a[14:15], v234 offset:14848\n"
    "v_mfma_f32_16x16x32_bf16 v[172:175], a[16:19], v[8:11], 0\n"
    "ds_read_b128 a[96:99], v236 offset:8448\n"
    "v_mfma_f32_16x16x32_bf16 v[172:175], a[20:23], v[24:27], v[172:175]\n"
    "ds_read_b128 a[100:103], v236 offset:8512\n"
    "v_mfma_f32_16x16x32_bf16 v[172:175], a[24:27], v[36:39], v[172:175]\n"
    "s_waitcnt lgkmcnt(10)\n"
    "ds_read_b128 a[104:107], v236 offset:8576\n"
    "v_mfma_f32_16x16x32_bf16 v[172:175], a[28:31], v[44:47], v[172:175]\n"
    "v_exp_f32 v216, v216\n"
    "v_mfma_f32_16x16x32_bf16 a[132:135], a[144:147], a[240:243], a[132:135]\n"
    "ds_read_b128 a[108:111], v236 offset:8640\n"
    "v_mfma_f32_16x16x32_bf16 v[176:179], a[16:19], v[12:15], 0\n"
    "v_exp_f32 v217, v217\n"
    "v_mfma_f32_16x16x32_bf16 v[176:179], a[20:23], v[28:31], v[176:179]\n"
    "s_add_u32 s55, s54, s63\n"
    "v_exp_f32 v218, v218\n"
    "v_mfma_f32_16x16x32_bf16 v[176:179], a[24:27], a[152:155], v[176:179]\n"
    "s_add_u32 s54, s54, 16896\n"
    "v_exp_f32 v219, v219\n"
    "v_mfma_f32_16x16x32_bf16 v[176:179], a[28:31], a[160:163], v[176:179]\n"
    "s_sub_u32 s97, s54, 84480\n"
    "v_exp_f32 v220, v220\n"
    "v_mfma_f32_16x16x32_bf16 a[128:131], a[144:147], v[132:135], a[128:131]\n"
    "s_waitcnt lgkmcnt(4)\n"
    "v_exp_f32 v221, v221\n"
    "v_mfma_f32_16x16x32_bf16 v[180:183], a[0:3], v[0:3], 0\n"
    "s_min_u32 s54, s54, s97\n"
    "ds_read_b64_tr_b16 a[16:17], v235 offset:512\n"
    "v_cvt_pk_bf16_f32 v248, v216, v217\n"
    "v_mfma_f32_16x16x32_bf16 v[180:183], a[4:7], v[16:19], v[180:183]\n"
    "s_sub_u32 s97, s54, s67\n"
    "ds_read_b64_tr_b16 a[18:19], v235 offset:2560\n"
    "v_cvt_pk_bf16_f32 v249, v218, v219\n"
    "v_mfma_f32_16x16x32_bf16 v[180:183], a[8:11], a[148:151], v[180:183]\n"
    "s_mov_b32 s67, s54\n"
    "ds_read_b64_tr_b16 a[20:21], v235 offset:4608\n"
    "v_cvt_pk_bf16_f32 v146, v220, v221\n"
    "v_mfma_f32_16x16x32_bf16 v[180:183], a[12:15], a[156:159], v[180:183]\n"
    "ds_read_b64_tr_b16 a[22:23], v235 offset:6656\n"
    "v_accvgpr_write_b32 a254, v248\n"
    "v_mfma_f32_16x16x32_bf16 a[128:131], a[144:147], v[140:143], a[128:131]\n"
    "v_exp_f32 v222, v222\n"
    "v_mfma_f32_16x16x32_bf16 v[184:187], a[0:3], v[4:7], 0\n"
    "ds_read_b128 a[112:115], v236 offset:8704\n"
    "ds_read_b64_tr_b16 a[24:25], v235 offset:8704\n"
    "v_exp_f32 v223, v223\n"
    "v_exp_f32 v224, v224\n"
    "v_exp_f32 v225, v225\n"
    "v_exp_f32 v226, v226\n"
    "v_exp_f32 v227, v227\n"
    "v_mfma_f32_16x16x32_bf16 v[184:187], a[4:7], v[20:23], v[184:187]\n"
    "ds_read_b64_tr_b16 a[26:27], v235 offset:10752\n"
    "v_cvt_pk_bf16_f32 v147, v222, v223\n"
    "v_cvt_pk_bf16_f32 v250, v224, v225\n"
    "v_cvt_pk_bf16_f32 v251, v226, v227\n"
    "v_mfma_f32_16x16x32_bf16 v[184:187], a[8:11], v[32:35], v[184:187]\n"
    "ds_read_b128 a[116:119], v236 offset:8768\n"
    "ds_read_b64_tr_b16 a[28:29], v235 offset:12800\n"
    "v_accvgpr_write_b32 a255, v249\n"
    "v_mfma_f32_16x16x32_bf16 v[184:187], a[12:15], v[40:43], v[184:187]\n"
    "s_add_u32 m0, s96, 8192\n"
    "ds_read_b128 a[120:123], v236 offset:8832\n"
    "ds_read_b64_tr_b16 a[30:31], v235 offset:14848\n"
    "v_mfma_f32_16x16x32_bf16 a[136:139], a[144:147], a[252:255], a[136:139]\n"
    "s_add_u32 s52, s74, s60\n"
    "buffer_load_dwordx4 v231, s[40:43], s53 offen lds\n"
    "v_mfma_f32_16x16x32_bf16 v[188:191], a[0:3], v[8:11], 0\n"
    "s_add_u32 s53, s53, 0x4000\n"
    "ds_read_b128 a[124:127], v236 offset:8896\n"
    "v_accvgpr_write_b32 a250, v250\n"
    "v_mfma_f32_16x16x32_bf16 v[188:191], a[4:7], v[24:27], v[188:191]\n"
    "s_add_u32 m0, s96, 12288\n"
    "v_accvgpr_write_b32 a251, v251\n"
    "v_add_u32 v234, s97, v234\n"
    "v_mfma_f32_16x16x32_bf16 v[188:191], a[8:11], v[36:39], v[188:191]\n"
    "s_mov_b32 s38, s76\n"
    "buffer_load_dwordx4 v231, s[40:43], s53 offen lds\n"
    "v_mfma_f32_16x16x32_bf16 v[188:191], a[12:15], v[44:47], v[188:191]\n"
    "s_add_u32 s53, s53, 0x4000\n"
    "v_xor_b32 v235, 32, v234\n"
    "v_accvgpr_read_b32 v244, a128\n"
    "v_mfma_f32_16x16x32_bf16 a[140:143], a[144:147], v[144:147], a[140:143]\n"
    "v_cmp_nge_f32 s[98:99], s57, v244\n"
    "v_mfma_f32_16x16x32_bf16 v[192:195], a[0:3], v[12:15], 0\n"
    "s_add_u32 m0, s59, 0\n"
    "s_or_b64 s[84:85], s[84:85], s[98:99]\n"
    "v_mfma_f32_16x16x32_bf16 v[192:195], a[4:7], v[28:31], v[192:195]\n"
    "buffer_load_dwordx4 v230, s[44:47], s58 offen nt lds\n"
    "v_mfma_f32_16x16x32_bf16 v[192:195], a[8:11], a[152:155], v[192:195]\n"
    "s_add_u32 s58, s58, 1024\n"
    "v_cmp_nle_f32 s[98:99], 0.5, v244\n"
    "v_mfma_f32_16x16x32_bf16 v[192:195], a[12:15], a[160:163], v[192:195]\n"
    "s_or_b64 s[84:85], s[84:85], s[98:99]\n"
    "v_rcp_f32 v244, v244\n"
    "v_mfma_f32_16x16x32_bf16 a[132:135], a[144:147], a[248:251], a[132:135]\n"
    "s_waitcnt vmcnt(9) lgkmcnt(1)\n"
    "ds_inc_u32 v228, v236\n"
    "v_mfma_f32_16x16x32_bf16 v[196:199], a[16:19], v[0:3], 0\n"
    "ds_read_b64_tr_b16 a[0:1], v234 offset:0\n"
    "v_mfma_f32_16x16x32_bf16 v[196:199], a[20:23], v[16:19], v[196:199]\n"
    "ds_read_b64_tr_b16 a[2:3], v234 offset:2048\n"
    "v_mfma_f32_16x16x32_bf16 v[196:199], a[24:27], a[148:151], v[196:199]\n"
    "s_add_u32 s61, s61, 1\n"
    "ds_read_b64_tr_b16 a[4:5], v234 offset:4096\n"
    "v_mfma_f32_16x16x32_bf16 v[196:199], a[28:31], a[156:159], v[196:199]\n"
    "ds_read_b64_tr_b16 a[6:7], v234 offset:6144\n"
    "v_accvgpr_read_b32 v245, a132\n"
    "v_mfma_f32_16x16x32_bf16 v[200:203], a[16:19], v[4:7], 0\n"
    "ds_read_b64_tr_b16 a[8:9], v234 offset:8192\n"
    "v_cmp_nge_f32 s[98:99], s57, v245\n"
    "v_mfma_f32_16x16x32_bf16 v[200:203], a[20:23], v[20:23], v[200:203]\n"
    "s_or_b64 s[84:85], s[84:85], s[98:99]\n"
    "ds_read_b64_tr_b16 a[10:11], v234 offset:10240\n"
    "v_mfma_f32_16x16x32_bf16 v[200:203], a[24:27], v[32:35], v[200:203]\n"
    "ds_read_b64_tr_b16 a[12:13], v234 offset:12288\n"
    "v_cmp_nle_f32 s[98:99], 0.5, v245\n"
    "v_mfma_f32_16x16x32_bf16 v[200:203], a[28:31], v[40:43], v[200:203]\n"
    "s_or_b64 s[84:85], s[84:85], s[98:99]\n"
    "ds_read_b32 v229, v230\n"
    "v_mfma_f32_16x16x32_bf16 v[204:207], a[16:19], v[8:11], 0\n"
    "s_add_u32 m0, s59, 1056\n"
    "ds_read_b64_tr_b16 a[14:15], v234 offset:14336\n"
    "v_accvgpr_read_b32 v246, a136\n"
    "v_mfma_f32_16x16x32_bf16 v[204:207], a[20:23], v[24:27], v[204:207]\n"
    "buffer_load_dwordx4 v230, s[44:47], s58 offen nt lds\n"
    "v_mfma_f32_16x16x32_bf16 v[204:207], a[24:27], v[36:39], v[204:207]\n"
    "s_add_u32 s58, s58, 1024\n"
    "v_cmp_nge_f32 s[98:99], s57, v246\n"
    "v_mfma_f32_16x16x32_bf16 v[204:207], a[28:31], v[44:47], v[204:207]\n"
    "s_or_b64 s[84:85], s[84:85], s[98:99]\n"
    "v_rcp_f32 v245, v245\n"
    "v_mfma_f32_16x16x32_bf16 v[208:211], a[16:19], v[12:15], 0\n"
    "v_subrev_f32 v212, s71, v246\n"
    "v_mfma_f32_16x16x32_bf16 v[208:211], a[20:23], v[28:31], v[208:211]\n"
    "s_waitcnt lgkmcnt(1)\n"
    "v_cmp_gt_u32 vcc, s61, v229\n"
    "v_cmp_nle_f32 s[98:99], 0.5, v212\n"
    "v_mfma_f32_16x16x32_bf16 v[208:211], a[24:27], a[152:155], v[208:211]\n"
    "s_add_u32 m0, s59, 2112\n"
    "v_rcp_f32 v246, v246\n"
    "v_mfma_f32_16x16x32_bf16 v[208:211], a[28:31], a[160:163], v[208:211]\n"
    "s_waitcnt lgkmcnt(0)\n"
    "buffer_load_dwordx4 v230, s[44:47], s58 offen nt lds\n"
    "v_mfma_f32_16x16x32_bf16 v[148:151], a[0:3], a[164:167], v[148:151]\n"
    "s_cbranch_vccnz L_sp16%=\n"
    "L_rt16%=:\n"
    "ds_read_b64_tr_b16 a[16:17], v235 offset:0\n"
    "v_mfma_f32_16x16x32_bf16 v[148:151], a[4:7], a[172:175], v[148:151]\n"
    "s_add_u32 s58, s58, 1024\n"
    "ds_read_b64_tr_b16 a[18:19], v235 offset:2048\n"
    "s_or_b64 s[84:85], s[84:85], s[98:99]\n"
    "v_mfma_f32_16x16x32_bf16 v[148:151], a[8:11], a[180:183], v[148:151]\n"
    "s_waitcnt lgkmcnt(12)\n"
    "ds_read_b64_tr_b16 a[20:21], v235 offset:4096\n"
    "v_accvgpr_read_b32 v247, a140\n"
    "v_mfma_f32_16x16x32_bf16 v[148:151], a[12:15], a[188:191], v[148:151]\n"
    "ds_read_b64_tr_b16 a[22:23], v235 offset:6144\n"
    "v_cmp_nge_f32 s[98:99], s57, v247\n"
    "v_mfma_f32_16x16x32_bf16 v[152:155], a[0:3], v[48:51], v[152:155]\n"
    "s_or_b64 s[84:85], s[84:85], s[98:99]\n"
    "ds_read_b64_tr_b16 a[24:25], v235 offset:8192\n"
    "v_mfma_f32_16x16x32_bf16 v[152:155], a[4:7], v[56:59], v[152:155]\n"
    "ds_read_b64_tr_b16 a[26:27], v235 offset:10240\n"
    "v_subrev_f32 v212, s71, v247\n"
    "v_mfma_f32_16x16x32_bf16 v[152:155], a[8:11], v[64:67], v[152:155]\n"
    "ds_read_b64_tr_b16 a[28:29], v235 offset:12288\n"
    "v_cmp_nle_f32 s[98:99], 0.5, v212\n"
    "v_mfma_f32_16x16x32_bf16 v[152:155], a[12:15], v[72:75], v[152:155]\n"
    "s_or_b64 s[84:85], s[84:85], s[98:99]\n"
    "ds_read_b64_tr_b16 a[30:31], v235 offset:14336\n"
    "v_mfma_f32_16x16x32_bf16 v[156:159], a[0:3], v[52:55], v[156:159]\n"
    "s_add_u32 m0, s55, 0\n"
    "v_rcp_f32 v247, v247\n"
    "v_mfma_f32_16x16x32_bf16 v[156:159], a[4:7], v[60:63], v[156:159]\n"
    "buffer_load_dwordx4 v231, s[40:43], s53 offen lds\n"
    "v_mfma_f32_16x16x32_bf16 v[156:159], a[8:11], v[68:71], v[156:159]\n"
    "s_add_u32 s53, s53, 0x4000\n"
    "v_mfma_f32_16x16x32_bf16 v[156:159], a[12:15], v[76:79], v[156:159]\n"
    "v_mfma_f32_16x16x32_bf16 v[160:163], a[0:3], a[168:171], v[160:163]\n"
    "s_add_u32 m0, s55, 4096\n"
    "v_mfma_f32_16x16x32_bf16 v[160:163], a[4:7], a[176:179], v[160:163]\n"
    "buffer_load_dwordx4 v231, s[40:43], s53 offen lds\n"
    "v_mfma_f32_16x16x32_bf16 v[160:163], a[8:11], a[184:187], v[160:163]\n"
    "s_add_u32 s53, s53, 0x4000\n"
    "v_mfma_f32_16x16x32_bf16 v[160:163], a[12:15], a[192:195], v[160:163]\n"
    "s_waitcnt lgkmcnt(0)\n"
    "v_mfma_f32_16x16x32_bf16 v[164:167], a[16:19], a[164:167], v[164:167]\n"
    "ds_read_b64_tr_b16 a[0:1], v234 offset:512\n"
    "v_mfma_f32_16x16x32_bf16 v[164:167], a[20:23], a[172:175], v[164:167]\n"
    "ds_read_b64_tr_b16 a[2:3], v234 offset:2560\n"
    "v_mfma_f32_16x16x32_bf16 v[164:167], a[24:27], a[180:183], v[164:167]\n"
    "ds_read_b64_tr_b16 a[4:5], v234 offset:4608\n"
    "v_mfma_f32_16x16x32_bf16 v[164:167], a[28:31], a[188:191], v[164:167]\n"
    "ds_read_b64_tr_b16 a[6:7], v234 offset:6656\n"
    "v_mfma_f32_16x16x32_bf16 v[168:171], a[16:19], v[48:51], v[168:171]\n"
    "ds_read_b64_tr_b16 a[8:9], v234 offset:8704\n"
    "v_mfma_f32_16x16x32_bf16 v[168:171], a[20:23], v[56:59], v[168:171]\n"
    "ds_read_b64_tr_b16 a[10:11], v234 offset:10752\n"
    "v_mfma_f32_16x16x32_bf16 v[168:171], a[24:27], v[64:67], v[168:171]\n"
    "ds_read_b64_tr_b16 a[12:13], v234 offset:12800\n"
    "v_mfma_f32_16x16x32_bf16 v[168:171], a[28:31], v[72:75], v[168:171]\n"
    "ds_read_b64_tr_b16 a[14:15], v234 offset:14848\n"
    "v_mfma_f32_16x16x32_bf16 v[172:175], a[16:19], v[52:55], v[172:175]\n"
    "s_add_u32 m0, s55, 8192\n"
    "v_mfma_f32_16x16x32_bf16 v[172:175], a[20:23], v[60:63], v[172:175]\n"
    "buffer_load_dwordx4 v231, s[40:43], s53 offen lds\n"
    "v_mfma_f32_16x16x32_bf16 v[172:175], a[24:27], v[68:71], v[172:175]\n"
    "s_add_u32 s53, s53, 0x4000\n"
    "v_mfma_f32_16x16x32_bf16 v[172:175], a[28:31], v[76:79], v[172:175]\n"
    "v_mfma_f32_16x16x32_bf16 v[176:179], a[16:19], a[168:171], v[176:179]\n"
    "s_add_u32 m0, s55, 12288\n"
    "s_add_u32 s96, s54, s63\n"
    "v_mfma_f32_16x16x32_bf16 v[176:179], a[20:23], a[176:179], v[176:179]\n"
    "s_add_u32 s54, s54, 16896\n"
    "buffer_load_dwordx4 v231, s[40:43], s53 offen lds\n"
    "v_mfma_f32_16x16x32_bf16 v[176:179], a[24:27], a[184:187], v[176:179]\n"
    "s_add_u32 s53, s53, 0x4000\n"
    "s_sub_u32 s97, s54, 84480\n"
    "v_mfma_f32_16x16x32_bf16 v[176:179], a[28:31], a[192:195], v[176:179]\n"
    "s_waitcnt lgkmcnt(0)\n"
    "s_min_u32 s54, s54, s97\n"
    "v_mfma_f32_16x16x32_bf16 v[180:183], a[0:3], a[164:167], v[180:183]\n"
    "s_sub_u32 s97, s54, s67\n"
    "ds_read_b64_tr_b16 a[16:17], v235 offset:512\n"
    "v_mfma_f32_16x16x32_bf16 v[180:183], a[4:7], a[172:175], v[180:183]\n"
    "s_mov_b32 s67, s54\n"
    "ds_read_b64_tr_b16 a[18:19], v235 offset:2560\n"
    "v_mfma_f32_16x16x32_bf16 v[180:183], a[8:11], a[180:183], v[180:183]\n"
    "ds_read_b64_tr_b16 a[20:21], v235 offset:4608\n"
    "v_mfma_f32_16x16x32_bf16 v[180:183], a[12:15], a[188:191], v[180:183]\n"
    "ds_read_b64_tr_b16 a[22:23], v235 offset:6656\n"
    "v_mfma_f32_16x16x32_bf16 v[184:187], a[0:3], v[48:51], v[184:187]\n"
    "ds_read_b64_tr_b16 a[24:25], v235 offset:8704\n"
    "v_mfma_f32_16x16x32_bf16 v[184:187], a[4:7], v[56:59], v[184:187]\n"
    "ds_read_b64_tr_b16 a[26:27], v235 offset:10752\n"
    "v_mfma_f32_16x16x32_bf16 v[184:187], a[8:11], v[64:67], v[184:187]\n"
    "ds_read_b64_tr_b16 a[28:29], v235 offset:12800\n"
    "v_mfma_f32_16x16x32_bf16 v[184:187], a[12:15], v[72:75], v[184:187]\n"
    "ds_read_b64_tr_b16 a[30:31], v235 offset:14848\n"
    "v_add_u32 v234, s97, v234\n"
    "v_mfma_f32_16x16x32_bf16 v[188:191], a[0:3], v[52:55], v[188:191]\n"
    "s_add_u32 m0, s59, 3168\n"
    "v_xor_b32 v235, 32, v234\n"
    "v_mfma_f32_16x16x32_bf16 v[188:191], a[4:7], v[60:63], v[188:191]\n"
    "buffer_load_dwordx4 v230, s[44:47], s58 offen nt lds\n"
    "v_mfma_f32_16x16x32_bf16 v[188:191], a[8:11], v[68:71], v[188:191]\n"
    "s_add_u32 s58, s58, 1024\n"
    "v_mfma_f32_16x16x32_bf16 v[188:191], a[12:15], v[76:79], v[188:191]\n"
    "v_mfma_f32_16x16x32_bf16 v[192:195], a[0:3], a[168:171], v[192:195]\n"
    "s_add_u32 m0, s59, 4224\n"
    "v_mfma_f32_16x16x32_bf16 v[192:195], a[4:7], a[176:179], v[192:195]\n"
    "buffer_load_dwordx4 v230, s[44:47], s58 offen nt lds\n"
    "v_mfma_f32_16x16x32_bf16 v[192:195], a[8:11], a[184:187], v[192:195]\n"
    "s_add_u32 s58, s58, 1024\n"
    "v_mfma_f32_16x16x32_bf16 v[192:195], a[12:15], a[192:195], v[192:195]\n"
    "s_waitcnt vmcnt(13) lgkmcnt(0)\n"
    "ds_inc_u32 v228, v236\n"
    "v_mfma_f32_16x16x32_bf16 v[196:199], a[16:19], a[164:167], v[196:199]\n"
    "ds_read_b64_tr_b16 a[0:1], v234 offset:0\n"
    "v_mfma_f32_16x16x32_bf16 v[196:199], a[20:23], a[172:175], v[196:199]\n"
    "ds_read_b64_tr_b16 a[2:3], v234 offset:2048\n"
    "v_mfma_f32_16x16x32_bf16 v[196:199], a[24:27], a[180:183], v[196:199]\n"
    "s_add_u32 s61, s61, 1\n"
    "ds_read_b64_tr_b16 a[4:5], v234 offset:4096\n"
    "v_mfma_f32_16x16x32_bf16 v[196:199], a[28:31], a[188:191], v[196:199]\n"
    "ds_read_b64_tr_b16 a[6:7], v234 offset:6144\n"
    "v_mfma_f32_16x16x32_bf16 v[200:203], a[16:19], v[48:51], v[200:203]\n"
    "ds_read_b64_tr_b16 a[8:9], v234 offset:8192\n"
    "v_mfma_f32_16x16x32_bf16 v[200:203], a[20:23], v[56:59], v[200:203]\n"
    "ds_read_b64_tr_b16 a[10:11], v234 offset:10240\n"
    "v_mfma_f32_16x16x32_bf16 v[200:203], a[24:27], v[64:67], v[200:203]\n"
    "ds_read_b64_tr_b16 a[12:13], v234 offset:12288\n"
    "v_mfma_f32_16x16x32_bf16 v[200:203], a[28:31], v[72:75], v[200:203]\n"
    "ds_read_b32 v229, v230\n"
    "v_mfma_f32_16x16x32_bf16 v[204:207], a[16:19], v[52:55], v[204:207]\n"
    "s_add_u32 m0, s59, 5280\n"
    "ds_read_b64_tr_b16 a[14:15], v234 offset:14336\n"
    "v_mfma_f32_16x16x32_bf16 v[204:207], a[20:23], v[60:63], v[204:207]\n"
    "buffer_load_dwordx4 v230, s[44:47], s58 offen nt lds\n"
    "v_mfma_f32_16x16x32_bf16 v[204:207], a[24:27], v[68:71], v[204:207]\n"
    "s_add_u32 s58, s58, 1024\n"
    "v_mfma_f32_16x16x32_bf16 v[204:207], a[28:31], v[76:79], v[204:207]\n"
    "v_mfma_f32_16x16x32_bf16 v[208:211], a[16:19], a[168:171], v[208:211]\n"
    "v_mfma_f32_16x16x32_bf16 v[208:211], a[20:23], a[176:179], v[208:211]\n"
    "s_waitcnt lgkmcnt(1)\n"
    "v_cmp_gt_u32 vcc, s61, v229\n"
    "v_mfma_f32_16x16x32_bf16 v[208:211], a[24:27], a[184:187], v[208:211]\n"
    "s_add_u32 m0, s59, 6336\n"
    "v_mfma_f32_16x16x32_bf16 v[208:211], a[28:31], a[192:195], v[208:211]\n"
    "s_waitcnt lgkmcnt(0)\n"
    "buffer_load_dwordx4 v230, s[44:47], s58 offen nt lds\n"
    "v_mfma_f32_16x16x32_bf16 v[148:151], a[0:3], v[80:83], v[148:151]\n"
    "s_cbranch_vccnz L_sp17%=\n"
    "L_rt17%=:\n"
    "ds_read_b64_tr_b16 a[16:17], v235 offset:0\n"
    "v_mfma_f32_16x16x32_bf16 v[148:151], a[4:7], v[92:95], v[148:151]\n"
    "s_add_u32 s58, s58, 1024\n"
    "ds_read_b64_tr_b16 a[18:19], v235 offset:2048\n"
    "v_mfma_f32_16x16x32_bf16 v[148:151], a[8:11], v[100:103], v[148:151]\n"
    "ds_read_b64_tr_b16 a[20:21], v235 offset:4096\n"
    "v_mfma_f32_16x16x32_bf16 v[148:151], a[12:15], v[108:111], v[148:151]\n"
    "ds_read_b64_tr_b16 a[22:23], v235 offset:6144\n"
    "v_mfma_f32_16x16x32_bf16 v[152:155], a[0:3], v[84:87], v[152:155]\n"
    "ds_read_b64_tr_b16 a[24:25], v235 offset:8192\n"
    "v_mfma_f32_16x16x32_bf16 v[152:155], a[4:7], a[200:203], v[152:155]\n"
    "ds_read_b64_tr_b16 a[26:27], v235 offset:10240\n"
    "v_mfma_f32_16x16x32_bf16 v[152:155], a[8:11], a[208:211], v[152:155]\n"
    "ds_read_b64_tr_b16 a[28:29], v235 offset:12288\n"
    "v_mfma_f32_16x16x32_bf16 v[152:155], a[12:15], a[216:219], v[152:155]\n"
    "ds_read_b64_tr_b16 a[30:31], v235 offset:14336\n"
    "v_mfma_f32_16x16x32_bf16 v[156:159], a[0:3], v[88:91], v[156:159]\n"
    "s_add_u32 m0, s96, 0\n"
    "v_mfma_f32_16x16x32_bf16 v[156:159], a[4:7], a[204:207], v[156:159]\n"
    "buffer_load_dwordx4 v231, s[40:43], s53 offen lds\n"
    "v_mfma_f32_16x16x32_bf16 v[156:159], a[8:11], a[212:215], v[156:159]\n"
    "s_add_u32 s53, s53, 0x4000\n"
    "v_mfma_f32_16x16x32_bf16 v[156:159], a[12:15], a[220:223], v[156:159]\n"
    "v_mfma_f32_16x16x32_bf16 v[160:163], a[0:3], a[196:199], v[160:163]\n"
    "s_add_u32 m0, s96, 4096\n"
    "v_mfma_f32_16x16x32_bf16 v[160:163], a[4:7], v[96:99], v[160:163]\n"
    "buffer_load_dwordx4 v231, s[40:43], s53 offen lds\n"
    "v_mfma_f32_16x16x32_bf16 v[160:163], a[8:11], v[104:107], v[160:163]\n"
    "s_add_u32 s53, s53, 0x4000\n"
    "v_mfma_f32_16x16x32_bf16 v[160:163], a[12:15], v[112:115], v[160:163]\n"
    "s_waitcnt lgkmcnt(0)\n"
    "v_mfma_f32_16x16x32_bf16 v[164:167], a[16:19], v[80:83], v[164:167]\n"
    "ds_read_b64_tr_b16 a[0:1], v234 offset:512\n"
    "v_mfma_f32_16x16x32_bf16 v[164:167], a[20:23], v[92:95], v[164:167]\n"
    "ds_read_b64_tr_b16 a[2:3], v234 offset:2560\n"
    "v_mfma_f32_16x16x32_bf16 v[164:167], a[24:27], v[100:103], v[164:167]\n"
    "ds_read_b64_tr_b16 a[4:5], v234 offset:4608\n"
    "v_mfma_f32_16x16x32_bf16 v[164:167], a[28:31], v[108:111], v[164:167]\n"
    "ds_read_b64_tr_b16 a[6:7], v234 offset:6656\n"
    "v_mfma_f32_16x16x32_bf16 v[168:171], a[16:19], v[84:87], v[168:171]\n"
    "ds_read_b64_tr_b16 a[8:9], v234 offset:8704\n"
    "v_mfma_f32_16x16x32_bf16 v[168:171], a[20:23], a[200:203], v[168:171]\n"
    "ds_read_b64_tr_b16 a[10:11], v234 offset:10752\n"
    "v_mfma_f32_16x16x32_bf16 v[168:171], a[24:27], a[208:211], v[168:171]\n"
    "ds_read_b64_tr_b16 a[12:13], v234 offset:12800\n"
    "v_mfma_f32_16x16x32_bf16 v[168:171], a[28:31], a[216:219], v[168:171]\n"
    "ds_read_b64_tr_b16 a[14:15], v234 offset:14848\n"
    "v_mfma_f32_16x16x32_bf16 v[172:175], a[16:19], v[88:91], v[172:175]\n"
    "s_add_u32 m0, s96, 8192\n"
    "v_mfma_f32_16x16x32_bf16 v[172:175], a[20:23], a[204:207], v[172:175]\n"
    "buffer_load_dwordx4 v231, s[40:43], s53 offen lds\n"
    "v_mfma_f32_16x16x32_bf16 v[172:175], a[24:27], a[212:215], v[172:175]\n"
    "s_add_u32 s53, s53, 0x4000\n"
    "v_mfma_f32_16x16x32_bf16 v[172:175], a[28:31], a[220:223], v[172:175]\n"
    "v_mfma_f32_16x16x32_bf16 v[176:179], a[16:19], a[196:199], v[176:179]\n"
    "s_add_u32 m0, s96, 12288\n"
    "s_add_u32 s55, s54, s63\n"
    "v_mfma_f32_16x16x32_bf16 v[176:179], a[20:23], v[96:99], v[176:179]\n"
    "s_add_u32 s54, s54, 16896\n"
    "buffer_load_dwordx4 v231, s[40:43], s53 offen lds\n"
    "v_mfma_f32_16x16x32_bf16 v[176:179], a[24:27], v[104:107], v[176:179]\n"
    "s_add_u32 s53, s53, 0x4000\n"
    "s_sub_u32 s97, s54, 84480\n"
    "v_mfma_f32_16x16x32_bf16 v[176:179], a[28:31], v[112:115], v[176:179]\n"
    "s_waitcnt lgkmcnt(0)\n"
    "s_min_u32 s54, s54, s97\n"
    "v_mfma_f32_16x16x32_bf16 v[180:183], a[0:3], v[80:83], v[180:183]\n"
    "s_sub_u32 s97, s54, s67\n"
    "ds_read_b64_tr_b16 a[16:17], v235 offset:512\n"
    "v_mfma_f32_16x16x32_bf16 v[180:183], a[4:7], v[92:95], v[180:183]\n"
    "s_mov_b32 s67, s54\n"
    "ds_read_b64_tr_b16 a[18:19], v235 offset:2560\n"
    "v_mfma_f32_16x16x32_bf16 v[180:183], a[8:11], v[100:103], v[180:183]\n"
    "ds_read_b64_tr_b16 a[20:21], v235 offset:4608\n"
    "v_mfma_f32_16x16x32_bf16 v[180:183], a[12:15], v[108:111], v[180:183]\n"
    "ds_read_b64_tr_b16 a[22:23], v235 offset:6656\n"
    "v_mfma_f32_16x16x32_bf16 v[184:187], a[0:3], v[84:87], v[184:187]\n"
    "ds_read_b64_tr_b16 a[24:25], v235 offset:8704\n"
    "v_mfma_f32_16x16x32_bf16 v[184:187], a[4:7], a[200:203], v[184:187]\n"
    "ds_read_b64_tr_b16 a[26:27], v235 offset:10752\n"
    "v_mfma_f32_16x16x32_bf16 v[184:187], a[8:11], a[208:211], v[184:187]\n"
    "ds_read_b64_tr_b16 a[28:29], v235 offset:12800\n"
    "v_mfma_f32_16x16x32_bf16 v[184:187], a[12:15], a[216:219], v[184:187]\n"
    "ds_read_b64_tr_b16 a[30:31], v235 offset:14848\n"
    "v_add_u32 v234, s97, v234\n"
    "v_mfma_f32_16x16x32_bf16 v[188:191], a[0:3], v[88:91], v[188:191]\n"
    "s_add_u32 m0, s59, 7392\n"
    "v_xor_b32 v235, 32, v234\n"
    "v_mfma_f32_16x16x32_bf16 v[188:191], a[4:7], a[204:207], v[188:191]\n"
    "buffer_load_dwordx4 v230, s[44:47], s58 offen nt lds\n"
    "v_mfma_f32_16x16x32_bf16 v[188:191], a[8:11], a[212:215], v[188:191]\n"
    "s_add_u32 s58, s58, 1024\n"
    "v_mfma_f32_16x16x32_bf16 v[188:191], a[12:15], a[220:223], v[188:191]\n"
    "v_mfma_f32_16x16x32_bf16 v[192:195], a[0:3], a[196:199], v[192:195]\n"
    "v_mfma_f32_16x16x32_bf16 v[192:195], a[4:7], v[96:99], v[192:195]\n"
    "v_mfma_f32_16x16x32_bf16 v[192:195], a[8:11], v[104:107], v[192:195]\n"
    "v_mfma_f32_16x16x32_bf16 v[192:195], a[12:15], v[112:115], v[192:195]\n"
    "s_waitcnt vmcnt(16) lgkmcnt(0)\n"
    "ds_inc_u32 v228, v236\n"
    "v_mfma_f32_16x16x32_bf16 v[196:199], a[16:19], v[80:83], v[196:199]\n"
    "ds_read_b64_tr_b16 a[0:1], v234 offset:0\n"
    "v_mfma_f32_16x16x32_bf16 v[196:199], a[20:23], v[92:95], v[196:199]\n"
    "ds_read_b64_tr_b16 a[2:3], v234 offset:2048\n"
    "v_mfma_f32_16x16x32_bf16 v[196:199], a[24:27], v[100:103], v[196:199]\n"
    "s_add_u32 s61, s61, 1\n"
    "ds_read_b64_tr_b16 a[4:5], v234 offset:4096\n"
    "v_mfma_f32_16x16x32_bf16 v[196:199], a[28:31], v[108:111], v[196:199]\n"
    "ds_read_b64_tr_b16 a[6:7], v234 offset:6144\n"
    "v_mfma_f32_16x16x32_bf16 v[200:203], a[16:19], v[84:87], v[200:203]\n"
    "ds_read_b64_tr_b16 a[8:9], v234 offset:8192\n"
    "v_mfma_f32_16x16x32_bf16 v[200:203], a[20:23], a[200:203], v[200:203]\n"
    "ds_read_b64_tr_b16 a[10:11], v234 offset:10240\n"
    "v_mfma_f32_16x16x32_bf16 v[200:203], a[24:27], a[208:211], v[200:203]\n"
    "ds_read_b64_tr_b16 a[12:13], v234 offset:12288\n"
    "v_mfma_f32_16x16x32_bf16 v[200:203], a[28:31], a[216:219], v[200:203]\n"
    "ds_read_b64_tr_b16 a[14:15], v234 offset:14336\n"
    "v_mfma_f32_16x16x32_bf16 v[204:207], a[16:19], v[88:91], v[204:207]\n"
    "ds_read_b32 v229, v230\n"
    "v_mfma_f32_16x16x32_bf16 v[204:207], a[20:23], a[204:207], v[204:207]\n"
    "v_mfma_f32_16x16x32_bf16 v[204:207], a[24:27], a[212:215], v[204:207]\n"
    "v_mfma_f32_16x16x32_bf16 v[204:207], a[28:31], a[220:223], v[204:207]\n"
    "v_mfma_f32_16x16x32_bf16 v[208:211], a[16:19], a[196:199], v[208:211]\n"
    "v_mfma_f32_16x16x32_bf16 v[208:211], a[20:23], v[96:99], v[208:211]\n"
    "s_waitcnt lgkmcnt(0)\n"
    "v_cmp_gt_u32 vcc, s61, v229\n"
    "v_mfma_f32_16x16x32_bf16 v[208:211], a[24:27], v[104:107], v[208:211]\n"
    "v_mfma_f32_16x16x32_bf16 v[208:211], a[28:31], v[112:115], v[208:211]\n"
    "s_waitcnt lgkmcnt(1)\n"
    "v_mfma_f32_16x16x32_bf16 v[148:151], a[0:3], v[116:119], v[148:151]\n"
    "s_cbranch_vccnz L_sp18%=\n"
    "L_rt18%=:\n"
    "ds_read_b64_tr_b16 a[16:17], v235 offset:0\n"
    "v_mfma_f32_16x16x32_bf16 v[148:151], a[4:7], v[124:127], v[148:151]\n"
    "ds_read_b64_tr_b16 a[18:19], v235 offset:2048\n"
    "v_mfma_f32_16x16x32_bf16 v[148:151], a[8:11], v[132:135], v[148:151]\n"
    "ds_read_b64_tr_b16 a[20:21], v235 offset:4096\n"
    "v_mfma_f32_16x16x32_bf16 v[148:151], a[12:15], v[140:143], v[148:151]\n"
    "ds_read_b64_tr_b16 a[22:23], v235 offset:6144\n"
    "v_mfma_f32_16x16x32_bf16 v[152:155], a[0:3], a[224:227], v[152:155]\n"
    "ds_read_b64_tr_b16 a[24:25], v235 offset:8192\n"
    "v_mfma_f32_16x16x32_bf16 v[152:155], a[4:7], a[232:235], v[152:155]\n"
    "ds_read_b64_tr_b16 a[26:27], v235 offset:10240\n"
    "v_mfma_f32_16x16x32_bf16 v[152:155], a[8:11], a[240:243], v[152:155]\n"
    "ds_read_b64_tr_b16 a[28:29], v235 offset:12288\n"
    "v_mfma_f32_16x16x32_bf16 v[152:155], a[12:15], a[248:251], v[152:155]\n"
    "ds_read_b64_tr_b16 a[30:31], v235 offset:14336\n"
    "v_mul_f32 v148, v148, v244\n"
    "v_mfma_f32_16x16x32_bf16 v[156:159], a[0:3], a[228:231], v[156:159]\n"
    "s_add_u32 m0, s55, 0\n"
    "v_mul_f32 v149, v149, v244\n"
    "v_mul_f32 v150, v150, v244\n"
    "v_mfma_f32_16x16x32_bf16 v[156:159], a[4:7], a[236:239], v[156:159]\n"
    "buffer_load_dwordx4 v231, s[40:43], s53 offen lds\n"
    "v_mfma_f32_16x16x32_bf16 v[156:159], a[8:11], a[244:247], v[156:159]\n"
    "s_add_u32 s53, s53, 0x4000\n"
    "v_mul_f32 v151, v151, v244\n"
    "v_cvt_pk_bf16_f32 v148, v148, v149\n"
    "v_mfma_f32_16x16x32_bf16 v[156:159], a[12:15], a[252:255], v[156:159]\n"
    "v_cvt_pk_bf16_f32 v149, v150, v151\n"
    "v_mul_f32 v152, v152, v245\n"
    "v_mfma_f32_16x16x32_bf16 v[160:163], a[0:3], v[120:123], v[160:163]\n"
    "s_add_u32 m0, s55, 4096\n"
    "ds_write_b64 v237, v[148:149] offset:0\n"
    "v_mfma_f32_16x16x32_bf16 v[160:163], a[4:7], v[128:131], v[160:163]\n"
    "buffer_load_dwordx4 v231, s[40:43], s53 offen lds\n"
    "v_mfma_f32_16x16x32_bf16 v[160:163], a[8:11], v[136:139], v[160:163]\n"
    "s_add_u32 s53, s53, 0x4000\n"
    "v_mul_f32 v153, v153, v245\n"
    "v_mul_f32 v154, v154, v245\n"
    "v_mfma_f32_16x16x32_bf16 v[160:163], a[12:15], v[144:147], v[160:163]\n"
    "s_waitcnt lgkmcnt(1)\n"
    "v_mul_f32 v155, v155, v245\n"
    "v_cvt_pk_bf16_f32 v152, v152, v153\n"
    "v_mfma_f32_16x16x32_bf16 v[164:167], a[16:19], v[116:119], v[164:167]\n"
    "ds_read_b64_tr_b16 a[0:1], v234 offset:512\n"
    "v_cvt_pk_bf16_f32 v153, v154, v155\n"
    "v_mfma_f32_16x16x32_bf16 v[164:167], a[20:23], v[124:127], v[164:167]\n"
    "ds_read_b64_tr_b16 a[2:3], v234 offset:2560\n"
    "v_mul_f32 v156, v156, v246\n"
    "v_mfma_f32_16x16x32_bf16 v[164:167], a[24:27], v[132:135], v[164:167]\n"
    "ds_read_b64_tr_b16 a[4:5], v234 offset:4608\n"
    "v_mul_f32 v157, v157, v246\n"
    "v_mfma_f32_16x16x32_bf16 v[164:167], a[28:31], v[140:143], v[164:167]\n"
    "ds_read_b64_tr_b16 a[6:7], v234 offset:6656\n"
    "v_mul_f32 v158, v158, v246\n"
    "v_mfma_f32_16x16x32_bf16 v[168:171], a[16:19], a[224:227], v[168:171]\n"
    "ds_read_b64_tr_b16 a[8:9], v234 offset:8704\n"
    "v_mul_f32 v159, v159, v246\n"
    "v_mfma_f32_16x16x32_bf16 v[168:171], a[20:23], a[232:235], v[168:171]\n"
    "ds_read_b64_tr_b16 a[10:11], v234 offset:10752\n"
    "v_cvt_pk_bf16_f32 v156, v156, v157\n"
    "v_mfma_f32_16x16x32_bf16 v[168:171], a[24:27], a[240:243], v[168:171]\n"
    "ds_read_b64_tr_b16 a[12:13], v234 offset:12800\n"
    "v_cvt_pk_bf16_f32 v157, v158, v159\n"
    "v_mfma_f32_16x16x32_bf16 v[168:171], a[28:31], a[248:251], v[168:171]\n"
    "ds_read_b64_tr_b16 a[14:15], v234 offset:14848\n"
    "v_mul_f32 v160, v160, v247\n"
    "v_mfma_f32_16x16x32_bf16 v[172:175], a[16:19], a[228:231], v[172:175]\n"
    "ds_write_b64 v237, v[152:153] offset:2304\n"
    "v_mfma_f32_16x16x32_bf16 v[172:175], a[20:23], a[236:239], v[172:175]\n"
    "ds_write_b64 v237, v[156:157] offset:4608\n"
    "v_mfma_f32_16x16x32_bf16 v[172:175], a[24:27], a[244:247], v[172:175]\n"
    "v_mul_f32 v161, v161, v247\n"
    "v_mul_f32 v162, v162, v247\n"
    "v_mfma_f32_16x16x32_bf16 v[172:175], a[28:31], a[252:255], v[172:175]\n"
    "s_add_u32 m0, s55, 8192\n"
    "v_mul_f32 v163, v163, v247\n"
    "v_cvt_pk_bf16_f32 v160, v160, v161\n"
    "v_mfma_f32_16x16x32_bf16 v[176:179], a[16:19], v[120:123], v[176:179]\n"
    "s_add_u32 s96, s54, s63\n"
    "buffer_load_dwordx4 v231, s[40:43], s53 offen lds\n"
    "v_mfma_f32_16x16x32_bf16 v[176:179], a[20:23], v[128:131], v[176:179]\n"
    "s_add_u32 s53, s53, 0x4000\n"
    "v_cvt_pk_bf16_f32 v161, v162, v163\n"
    "v_mul_f32 v164, v164, v244\n"
    "v_mfma_f32_16x16x32_bf16 v[176:179], a[24:27], v[136:139], v[176:179]\n"
    "s_add_u32 s54, s54, 16896\n"
    "ds_write_b64 v237, v[160:161] offset:6912\n"
    "v_mfma_f32_16x16x32_bf16 v[176:179], a[28:31], v[144:147], v[176:179]\n"
    "s_waitcnt lgkmcnt(3)\n"
    "v_mul_f32 v165, v165, v244\n"
    "v_mul_f32 v166, v166, v244\n"
    "v_mfma_f32_16x16x32_bf16 v[180:183], a[0:3], v[116:119], v[180:183]\n"
    "s_sub_u32 s97, s54, 84480\n"
    "ds_read_b64_tr_b16 a[16:17], v235 offset:512\n"
    "v_mul_f32 v167, v167, v244\n"
    "v_mfma_f32_16x16x32_bf16 v[180:183], a[4:7], v[124:127], v[180:183]\n"
    "s_min_u32 s54, s54, s97\n"
    "ds_read_b64_tr_b16 a[18:19], v235 offset:2560\n"
    "v_cvt_pk_bf16_f32 v164, v164, v165\n"
    "v_mfma_f32_16x16x32_bf16 v[180:183], a[8:11], v[132:135], v[180:183]\n"
    "s_sub_u32 s97, s54, s67\n"
    "ds_read_b64_tr_b16 a[20:21], v235 offset:4608\n"
    "v_cvt_pk_bf16_f32 v165, v166, v167\n"
    "v_mfma_f32_16x16x32_bf16 v[180:183], a[12:15], v[140:143], v[180:183]\n"
    "s_mov_b32 s67, s54\n"
    "ds_read_b64_tr_b16 a[22:23], v235 offset:6656\n"
    "v_mul_f32 v168, v168, v245\n"
    "v_mfma_f32_16x16x32_bf16 v[184:187], a[0:3], a[224:227], v[184:187]\n"
    "ds_read_b64_tr_b16 a[24:25], v235 offset:8704\n"
    "v_mul_f32 v169, v169, v245\n"
    "v_mfma_f32_16x16x32_bf16 v[184:187], a[4:7], a[232:235], v[184:187]\n"
    "ds_read_b64_tr_b16 a[26:27], v235 offset:10752\n"
    "v_mul_f32 v170, v170, v245\n"
    "v_mfma_f32_16x16x32_bf16 v[184:187], a[8:11], a[240:243], v[184:187]\n"
    "ds_read_b64_tr_b16 a[28:29], v235 offset:12800\n"
    "v_mul_f32 v171, v171, v245\n"
    "v_mfma_f32_16x16x32_bf16 v[184:187], a[12:15], a[248:251], v[184:187]\n"
    "ds_read_b64_tr_b16 a[30:31], v235 offset:14848\n"
    "v_add_u32 v234, s97, v234\n"
    "v_mfma_f32_16x16x32_bf16 v[188:191], a[0:3], a[228:231], v[188:191]\n"
    "v_xor_b32 v235, 32, v234\n"
    "v_cvt_pk_bf16_f32 v168, v168, v169\n"
    "v_mfma_f32_16x16x32_bf16 v[188:191], a[4:7], a[236:239], v[188:191]\n"
    "ds_write_b64 v237, v[164:165] offset:576\n"
    "v_mfma_f32_16x16x32_bf16 v[188:191], a[8:11], a[244:247], v[188:191]\n"
    "v_cvt_pk_bf16_f32 v169, v170, v171\n"
    "v_mul_f32 v172, v172, v246\n"
    "v_mfma_f32_16x16x32_bf16 v[188:191], a[12:15], a[252:255], v[188:191]\n"
    "s_add_u32 m0, s55, 12288\n"
    "ds_write_b64 v237, v[168:169] offset:2880\n"
    "v_mfma_f32_16x16x32_bf16 v[192:195], a[0:3], v[120:123], v[192:195]\n"
    "buffer_load_dwordx4 v231, s[40:43], s53 offen lds\n"
    "v_mfma_f32_16x16x32_bf16 v[192:195], a[4:7], v[128:131], v[192:195]\n"
    "v_mul_f32 v173, v173, v246\n"
    "v_mul_f32 v174, v174, v246\n"
    "v_mfma_f32_16x16x32_bf16 v[192:195], a[8:11], v[136:139], v[192:195]\n"
    "v_mul_f32 v175, v175, v246\n"
    "v_cvt_pk_bf16_f32 v172, v172, v173\n"
    "v_mfma_f32_16x16x32_bf16 v[192:195], a[12:15], v[144:147], v[192:195]\n"
    "s_waitcnt vmcnt(13) lgkmcnt(2)\n"
    "s_add_u32 s53, s53, 0xfffc4000\n"
    "s_xor_b32 s53, s53, 384\n"
    "v_mfma_f32_16x16x32_bf16 v[196:199], a[16:19], v[116:119], v[196:199]\n"
    "ds_inc_u32 v228, v236\n"
    "v_mfma_f32_16x16x32_bf16 v[196:199], a[20:23], v[124:127], v[196:199]\n"
    "ds_read_b64_tr_b16 a[0:1], v234 offset:0\n"
    "v_cvt_pk_bf16_f32 v173, v174, v175\n"
    "v_mfma_f32_16x16x32_bf16 v[196:199], a[24:27], v[132:135], v[196:199]\n"
    "s_add_u32 s61, s61, 1\n"
    "ds_read_b64_tr_b16 a[2:3], v234 offset:2048\n"
    "v_mul_f32 v176, v176, v247\n"
    "v_mfma_f32_16x16x32_bf16 v[196:199], a[28:31], v[140:143], v[196:199]\n"
    "ds_read_b64_tr_b16 a[4:5], v234 offset:4096\n"
    "v_mul_f32 v177, v177, v247\n"
    "v_mfma_f32_16x16x32_bf16 v[200:203], a[16:19], a[224:227], v[200:203]\n"
    "ds_read_b64_tr_b16 a[6:7], v234 offset:6144\n"
    "v_mul_f32 v178, v178, v247\n"
    "v_mfma_f32_16x16x32_bf16 v[200:203], a[20:23], a[232:235], v[200:203]\n"
    "ds_read_b64_tr_b16 a[8:9], v234 offset:8192\n"
    "v_mul_f32 v179, v179, v247\n"
    "v_mfma_f32_16x16x32_bf16 v[200:203], a[24:27], a[240:243], v[200:203]\n"
    "ds_read_b64_tr_b16 a[10:11], v234 offset:10240\n"
    "v_cvt_pk_bf16_f32 v176, v176, v177\n"
    "v_mfma_f32_16x16x32_bf16 v[200:203], a[28:31], a[248:251], v[200:203]\n"
    "ds_read_b64_tr_b16 a[12:13], v234 offset:12288\n"
    "v_cvt_pk_bf16_f32 v177, v178, v179\n"
    "v_mfma_f32_16x16x32_bf16 v[204:207], a[16:19], a[228:231], v[204:207]\n"
    "ds_read_b64_tr_b16 a[14:15], v234 offset:14336\n"
    "v_mul_f32 v180, v180, v244\n"
    "v_mfma_f32_16x16x32_bf16 v[204:207], a[20:23], a[236:239], v[204:207]\n"
    "ds_read_b32 v229, v230\n"
    "v_mul_f32 v181, v181, v244\n"
    "v_mfma_f32_16x16x32_bf16 v[204:207], a[24:27], a[244:247], v[204:207]\n"
    "ds_write_b64 v237, v[172:173] offset:5184\n"
    "v_mfma_f32_16x16x32_bf16 v[204:207], a[28:31], a[252:255], v[204:207]\n"
    "ds_write_b64 v237, v[176:177] offset:7488\n"
    "v_mfma_f32_16x16x32_bf16 v[208:211], a[16:19], v[120:123], v[208:211]\n"
    "v_mul_f32 v182, v182, v244\n"
    "v_mul_f32 v183, v183, v244\n"
    "v_mfma_f32_16x16x32_bf16 v[208:211], a[20:23], v[128:131], v[208:211]\n"
    "s_waitcnt lgkmcnt(2)\n"
    "v_cmp_gt_u32 vcc, s61, v229\n"
    "v_cvt_pk_bf16_f32 v180, v180, v181\n"
    "v_mfma_f32_16x16x32_bf16 v[208:211], a[24:27], v[136:139], v[208:211]\n"
    "v_cvt_pk_bf16_f32 v181, v182, v183\n"
    "v_mul_f32 v184, v184, v245\n"
    "v_mfma_f32_16x16x32_bf16 v[208:211], a[28:31], v[144:147], v[208:211]\n"
    "s_waitcnt lgkmcnt(3)\n"
    "ds_write_b64 v237, v[180:181] offset:1152\n"
    "v_mfma_f32_16x16x32_bf16 v[148:151], a[0:3], v[0:3], 0\n"
    "s_cbranch_vccnz L_sp19%=\n"
    "L_rt19%=:\n"
    "ds_read_b64_tr_b16 a[16:17], v235 offset:0\n"
    "v_mfma_f32_16x16x32_bf16 v[148:151], a[4:7], v[16:19], v[148:151]\n"
    "ds_read_b64_tr_b16 a[18:19], v235 offset:2048\n"
    "v_mul_f32 v185, v185, v245\n"
    "v_mfma_f32_16x16x32_bf16 v[148:151], a[8:11], a[148:151], v[148:151]\n"
    "ds_read_b64_tr_b16 a[20:21], v235 offset:4096\n"
    "v_mul_f32 v186, v186, v245\n"
    "v_mfma_f32_16x16x32_bf16 v[148:151], a[12:15], a[156:159], v[148:151]\n"
    "ds_read_b64_tr_b16 a[22:23], v235 offset:6144\n"
    "v_mul_f32 v187, v187, v245\n"
    "v_mfma_f32_16x16x32_bf16 v[152:155], a[0:3], v[4:7], 0\n"
    "ds_read_b64_tr_b16 a[24:25], v235 offset:8192\n"
    "v_cvt_pk_bf16_f32 v184, v184, v185\n"
    "v_mfma_f32_16x16x32_bf16 v[152:155], a[4:7], v[20:23], v[152:155]\n"
    "ds_read_b64_tr_b16 a[26:27], v235 offset:10240\n"
    "v_cvt_pk_bf16_f32 v185, v186, v187\n"
    "v_mfma_f32_16x16x32_bf16 v[152:155], a[8:11], v[32:35], v[152:155]\n"
    "ds_read_b64_tr_b16 a[28:29], v235 offset:12288\n"
    "v_mul_f32 v196, v196, v244\n"
    "v_mfma_f32_16x16x32_bf16 v[152:155], a[12:15], v[40:43], v[152:155]\n"
    "ds_read_b64_tr_b16 a[30:31], v235 offset:14336\n"
    "v_mul_f32 v197, v197, v244\n"
    "v_mfma_f32_16x16x32_bf16 v[156:159], a[0:3], v[8:11], 0\n"
    "ds_write_b64 v237, v[184:185] offset:3456\n"
    "v_mfma_f32_16x16x32_bf16 v[156:159], a[4:7], v[24:27], v[156:159]\n"
    "v_mul_f32 v198, v198, v244\n"
    "v_mul_f32 v199, v199, v244\n"
    "v_mfma_f32_16x16x32_bf16 v[156:159], a[8:11], v[36:39], v[156:159]\n"
    "v_cvt_pk_bf16_f32 v196, v196, v197\n"
    "v_cvt_pk_bf16_f32 v197, v198, v199\n"
    "v_mfma_f32_16x16x32_bf16 v[156:159], a[12:15], v[44:47], v[156:159]\n"
    "s_add_u32 m0, s96, 0\n"
    "ds_write_b64 v237, v[196:197] offset:1728\n"
    "v_mfma_f32_16x16x32_bf16 v[160:163], a[0:3], v[12:15], 0\n"
    "buffer_load_dwordx4 v231, s[40:43], s53 offen lds\n"
    "v_mfma_f32_16x16x32_bf16 v[160:163], a[4:7], v[28:31], v[160:163]\n"
    "s_add_u32 s53, s53, 0x4000\n"
    "v_mul_f32 v188, v188, v246\n"
    "v_mul_f32 v189, v189, v246\n"
    "v_mfma_f32_16x16x32_bf16 v[160:163], a[8:11], a[152:155], v[160:163]\n"
    "v_mul_f32 v190, v190, v246\n"
    "v_mul_f32 v191, v191, v246\n"
    "v_mfma_f32_16x16x32_bf16 v[160:163], a[12:15], a[160:163], v[160:163]\n"
    "s_waitcnt lgkmcnt(2)\n"
    "v_cvt_pk_bf16_f32 v188, v188, v189\n"
    "v_cvt_pk_bf16_f32 v189, v190, v191\n"
    "v_mfma_f32_16x16x32_bf16 v[164:167], a[16:19], v[0:3], 0\n"
    "ds_read_b64_tr_b16 a[0:1], v234 offset:512\n"
    "v_mul_f32 v192, v192, v247\n"
    "v_mfma_f32_16x16x32_bf16 v[164:167], a[20:23], v[16:19], v[164:167]\n"
    "ds_read_b64_tr_b16 a[2:3], v234 offset:2560\n"
    "v_mul_f32 v193, v193, v247\n"
    "v_mfma_f32_16x16x32_bf16 v[164:167], a[24:27], a[148:151], v[164:167]\n"
    "ds_read_b64_tr_b16 a[4:5], v234 offset:4608\n"
    "v_mul_f32 v194, v194, v247\n"
    "v_mfma_f32_16x16x32_bf16 v[164:167], a[28:31], a[156:159], v[164:167]\n"
    "ds_read_b64_tr_b16 a[6:7], v234 offset:6656\n"
    "v_mul_f32 v195, v195, v247\n"
    "v_mfma_f32_16x16x32_bf16 v[168:171], a[16:19], v[4:7], 0\n"
    "ds_read_b64_tr_b16 a[8:9], v234 offset:8704\n"
    "v_cvt_pk_bf16_f32 v192, v192, v193\n"
    "v_mfma_f32_16x16x32_bf16 v[168:171], a[20:23], v[20:23], v[168:171]\n"
    "ds_read_b64_tr_b16 a[10:11], v234 offset:10752\n"
    "v_cvt_pk_bf16_f32 v193, v194, v195\n"
    "v_mfma_f32_16x16x32_bf16 v[168:171], a[24:27], v[32:35], v[168:171]\n"
    "ds_read_b64_tr_b16 a[12:13], v234 offset:12800\n"
    "v_mul_f32 v200, v200, v245\n"
    "v_mfma_f32_16x16x32_bf16 v[168:171], a[28:31], v[40:43], v[168:171]\n"
    "ds_read_b64_tr_b16 a[14:15], v234 offset:14848\n"
    "v_mul_f32 v201, v201, v245\n"
    "v_mfma_f32_16x16x32_bf16 v[172:175], a[16:19], v[8:11], 0\n"
    "s_add_u32 m0, s96, 4096\n"
    "ds_read_b64 v[252:253], v238 offset:0\n"
    "v_mul_f32 v202, v202, v245\n"
    "v_mfma_f32_16x16x32_bf16 v[172:175], a[20:23], v[24:27], v[172:175]\n"
    "buffer_load_dwordx4 v231, s[40:43], s53 offen lds\n"
    "v_mfma_f32_16x16x32_bf16 v[172:175], a[24:27], v[36:39], v[172:175]\n"
    "s_add_u32 s53, s53, 0x4000\n"
    "ds_write_b64 v237, v[188:189] offset:5760\n"
    "v_mfma_f32_16x16x32_bf16 v[172:175], a[28:31], v[44:47], v[172:175]\n"
    "ds_read_b64 v[254:255], v238 offset:128\n"
    "v_mul_f32 v203, v203, v245\n"
    "v_mfma_f32_16x16x32_bf16 v[176:179], a[16:19], v[12:15], 0\n"
    "s_add_u32 m0, s96, 8192\n"
    "ds_write_b64 v237, v[192:193] offset:8064\n"
    "v_mfma_f32_16x16x32_bf16 v[176:179], a[20:23], v[28:31], v[176:179]\n"
    "s_add_u32 s55, s54, s63\n"
    "buffer_load_dwordx4 v231, s[40:43], s53 offen lds\n"
    "v_mfma_f32_16x16x32_bf16 v[176:179], a[24:27], a[152:155], v[176:179]\n"
    "s_add_u32 s53, s53, 0x4000\n"
    "v_cvt_pk_bf16_f32 v200, v200, v201\n"
    "v_cvt_pk_bf16_f32 v201, v202, v203\n"
    "v_mfma_f32_16x16x32_bf16 v[176:179], a[28:31], a[160:163], v[176:179]\n"
    "s_waitcnt lgkmcnt(4)\n"
    "ds_write_b64 v237, v[200:201] offset:4032\n"
    "v_mfma_f32_16x16x32_bf16 v[180:183], a[0:3], v[0:3], 0\n"
    "s_add_u32 s54, s54, 16896\n"
    "ds_read_b64_tr_b16 a[16:17], v235 offset:512\n"
    "v_mul_f32 v204, v204, v246\n"
    "v_mfma_f32_16x16x32_bf16 v[180:183], a[4:7], v[16:19], v[180:183]\n"
    "s_sub_u32 s97, s54, 84480\n"
    "ds_read_b64_tr_b16 a[18:19], v235 offset:2560\n"
    "v_mul_f32 v205, v205, v246\n"
    "v_mfma_f32_16x16x32_bf16 v[180:183], a[8:11], a[148:151], v[180:183]\n"
    "s_min_u32 s54, s54, s97\n"
    "ds_read_b64_tr_b16 a[20:21], v235 offset:4608\n"
    "v_mul_f32 v206, v206, v246\n"
    "v_mfma_f32_16x16x32_bf16 v[180:183], a[12:15], a[156:159], v[180:183]\n"
    "s_sub_u32 s97, s54, s67\n"
    "ds_read_b64_tr_b16 a[22:23], v235 offset:6656\n"
    "v_mul_f32 v207, v207, v246\n"
    "v_mfma_f32_16x16x32_bf16 v[184:187], a[0:3], v[4:7], 0\n"
    "s_mov_b32 s67, s54\n"
    "ds_read_b64_tr_b16 a[24:25], v235 offset:8704\n"
    "v_cvt_pk_bf16_f32 v204, v204, v205\n"
    "v_mfma_f32_16x16x32_bf16 v[184:187], a[4:7], v[20:23], v[184:187]\n"
    "ds_read_b64_tr_b16 a[26:27], v235 offset:10752\n"
    "v_cvt_pk_bf16_f32 v205, v206, v207\n"
    "v_mfma_f32_16x16x32_bf16 v[184:187], a[8:11], v[32:35], v[184:187]\n"
    "ds_read_b64_tr_b16 a[28:29], v235 offset:12800\n"
    "v_mul_f32 v208, v208, v247\n"
    "v_mfma_f32_16x16x32_bf16 v[184:187], a[12:15], v[40:43], v[184:187]\n"
    "ds_read_b64_tr_b16 a[30:31], v235 offset:14848\n"
    "v_add_u32 v234, s97, v234\n"
    "v_mfma_f32_16x16x32_bf16 v[188:191], a[0:3], v[8:11], 0\n"
    "s_add_u32 m0, s96, 12288\n"
    "v_xor_b32 v235, 32, v234\n"
    "v_mul_f32 v209, v209, v247\n"
    "v_mfma_f32_16x16x32_bf16 v[188:191], a[4:7], v[24:27], v[188:191]\n"
    "buffer_load_dwordx4 v231, s[40:43], s53 offen lds\n"
    "v_mfma_f32_16x16x32_bf16 v[188:191], a[8:11], v[36:39], v[188:191]\n"
    "s_add_u32 s53, s53, 0x4000\n"
    "ds_write_b64 v237, v[204:205] offset:6336\n"
    "v_mfma_f32_16x16x32_bf16 v[188:191], a[12:15], v[44:47], v[188:191]\n"
    "s_waitcnt lgkmcnt(11)\n"
    "v_mul_f32 v210, v210, v247\n"
    "v_mul_f32 v211, v211, v247\n"
    "v_mfma_f32_16x16x32_bf16 v[192:195], a[0:3], v[12:15], 0\n"
    "buffer_store_dwordx4 v[252:255], v232, s[48:51], s24 offen offset:0 sc1\n"
    "v_mfma_f32_16x16x32_bf16 v[192:195], a[4:7], v[28:31], v[192:195]\n"
    "s_nop 0\n"
    "ds_read_b64 v[252:253], v238 offset:64\n"
    "v_cvt_pk_bf16_f32 v208, v208, v209\n"
    "v_mfma_f32_16x16x32_bf16 v[192:195], a[8:11], a[152:155], v[192:195]\n"
    "ds_read_b64 v[254:255], v238 offset:192\n"
    "v_cvt_pk_bf16_f32 v209, v210, v211\n"
    "v_mfma_f32_16x16x32_bf16 v[192:195], a[12:15], a[160:163], v[192:195]\n"
    "s_waitcnt vmcnt(10) lgkmcnt(3)\n"
    "ds_inc_u32 v228, v236\n"
    "v_mfma_f32_16x16x32_bf16 v[196:199], a[16:19], v[0:3], 0\n"
    "s_mov_b32 s58, s72\n"
    "ds_read_b64_tr_b16 a[0:1], v234 offset:0\n"
    "v_mfma_f32_16x16x32_bf16 v[196:199], a[20:23], v[16:19], v[196:199]\n"
    "ds_read_b64_tr_b16 a[2:3], v234 offset:2048\n"
    "v_mfma_f32_16x16x32_bf16 v[196:199], a[24:27], a[148:151], v[196:199]\n"
    "s_add_u32 s61, s61, 1\n"
    "ds_read_b64_tr_b16 a[4:5], v234 offset:4096\n"
    "v_mfma_f32_16x16x32_bf16 v[196:199], a[28:31], a[156:159], v[196:199]\n"
    "ds_read_b64_tr_b16 a[6:7], v234 offset:6144\n"
    "v_mfma_f32_16x16x32_bf16 v[200:203], a[16:19], v[4:7], 0\n"
    "ds_read_b64_tr_b16 a[8:9], v234 offset:8192\n"
    "v_mfma_f32_16x16x32_bf16 v[200:203], a[20:23], v[20:23], v[200:203]\n"
    "ds_read_b64_tr_b16 a[10:11], v234 offset:10240\n"
    "v_mfma_f32_16x16x32_bf16 v[200:203], a[24:27], v[32:35], v[200:203]\n"
    "ds_read_b64_tr_b16 a[12:13], v234 offset:12288\n"
    "v_mfma_f32_16x16x32_bf16 v[200:203], a[28:31], v[40:43], v[200:203]\n"
    "ds_read_b32 v229, v230\n"
    "v_mfma_f32_16x16x32_bf16 v[204:207], a[16:19], v[8:11], 0\n"
    "s_waitcnt lgkmcnt(9)\n"
    "ds_read_b64_tr_b16 a[14:15], v234 offset:14336\n"
    "v_mfma_f32_16x16x32_bf16 v[204:207], a[20:23], v[24:27], v[204:207]\n"
    "buffer_store_dwordx4 v[252:255], v232, s[48:51], s25 offen offset:0 sc1\n"
    "v_mfma_f32_16x16x32_bf16 v[204:207], a[24:27], v[36:39], v[204:207]\n"
    "ds_write_b64 v237, v[208:209] offset:8640\n"
    "v_mfma_f32_16x16x32_bf16 v[204:207], a[28:31], v[44:47], v[204:207]\n"
    "ds_read_b64 v[252:253], v238 offset:2304\n"
    "v_mfma_f32_16x16x32_bf16 v[208:211], a[16:19], v[12:15], 0\n"
    "ds_read_b64 v[254:255], v238 offset:2432\n"
    "v_mfma_f32_16x16x32_bf16 v[208:211], a[20:23], v[28:31], v[208:211]\n"
    "s_waitcnt lgkmcnt(4)\n"
    "v_cmp_gt_u32 vcc, s61, v229\n"
    "v_mfma_f32_16x16x32_bf16 v[208:211], a[24:27], a[152:155], v[208:211]\n"
    "v_mfma_f32_16x16x32_bf16 v[208:211], a[28:31], a[160:163], v[208:211]\n"
    "s_waitcnt lgkmcnt(3)\n"
    "v_mfma_f32_16x16x32_bf16 v[148:151], a[0:3], a[164:167], v[148:151]\n"
    "s_cbranch_vccnz L_sp20%=\n"
    "L_rt20%=:\n"
    "ds_read_b64_tr_b16 a[16:17], v235 offset:0\n"
    "v_mfma_f32_16x16x32_bf16 v[148:151], a[4:7], a[172:175], v[148:151]\n"
    "ds_read_b64_tr_b16 a[18:19], v235 offset:2048\n"
    "v_mfma_f32_16x16x32_bf16 v[148:151], a[8:11], a[180:183], v[148:151]\n"
    "ds_read_b64_tr_b16 a[20:21], v235 offset:4096\n"
    "v_mfma_f32_16x16x32_bf16 v[148:151], a[12:15], a[188:191], v[148:151]\n"
    "ds_read_b64_tr_b16 a[22:23], v235 offset:6144\n"
    "v_mfma_f32_16x16x32_bf16 v[152:155], a[0:3], v[48:51], v[152:155]\n"
    "ds_read_b64_tr_b16 a[24:25], v235 offset:8192\n"
    "v_mfma_f32_16x16x32_bf16 v[152:155], a[4:7], v[56:59], v[152:155]\n"
    "ds_read_b64_tr_b16 a[26:27], v235 offset:10240\n"
    "v_mfma_f32_16x16x32_bf16 v[152:155], a[8:11], v[64:67], v[152:155]\n"
    "ds_read_b64_tr_b16 a[28:29], v235 offset:12288\n"
    "v_mfma_f32_16x16x32_bf16 v[152:155], a[12:15], v[72:75], v[152:155]\n"
    "ds_read_b64_tr_b16 a[30:31], v235 offset:14336\n"
    "v_mfma_f32_16x16x32_bf16 v[156:159], a[0:3], v[52:55], v[156:159]\n"
    "s_add_u32 m0, s55, 0\n"
    "v_mfma_f32_16x16x32_bf16 v[156:159], a[4:7], v[60:63], v[156:159]\n"
    "buffer_load_dwordx4 v231, s[40:43], s53 offen lds\n"
    "v_mfma_f32_16x16x32_bf16 v[156:159], a[8:11], v[68:71], v[156:159]\n"
    "s_add_u32 s53, s53, 0x4000\n"
    "v_mfma_f32_16x16x32_bf16 v[156:159], a[12:15], v[76:79], v[156:159]\n"
    "v_mfma_f32_16x16x32_bf16 v[160:163], a[0:3], a[168:171], v[160:163]\n"
    "s_waitcnt lgkmcnt(8)\n"
    "v_mfma_f32_16x16x32_bf16 v[160:163], a[4:7], a[176:179], v[160:163]\n"
    "buffer_store_dwordx4 v[252:255], v232, s[48:51], s26 offen offset:0 sc1\n"
    "v_mfma_f32_16x16x32_bf16 v[160:163], a[8:11], a[184:187], v[160:163]\n"
    "s_nop 0\n"
    "ds_read_b64 v[252:253], v238 offset:2368\n"
    "v_mfma_f32_16x16x32_bf16 v[160:163], a[12:15], a[192:195], v[160:163]\n"
    "s_waitcnt lgkmcnt(1)\n"
    "ds_read_b64 v[254:255], v238 offset:2496\n"
    "v_mfma_f32_16x16x32_bf16 v[164:167], a[16:19], a[164:167], v[164:167]\n"
    "ds_read_b64_tr_b16 a[0:1], v234 offset:512\n"
    "v_mfma_f32_16x16x32_bf16 v[164:167], a[20:23], a[172:175], v[164:167]\n"
    "ds_read_b64_tr_b16 a[2:3], v234 offset:2560\n"
    "v_mfma_f32_16x16x32_bf16 v[164:167], a[24:27], a[180:183], v[164:167]\n"
    "ds_read_b64_tr_b16 a[4:5], v234 offset:4608\n"
    "v_mfma_f32_16x16x32_bf16 v[164:167], a[28:31], a[188:191], v[164:167]\n"
    "ds_read_b64_tr_b16 a[6:7], v234 offset:6656\n"
    "v_mfma_f32_16x16x32_bf16 v[168:171], a[16:19], v[48:51], v[168:171]\n"
    "ds_read_b64_tr_b16 a[8:9], v234 offset:8704\n"
    "v_mfma_f32_16x16x32_bf16 v[168:171], a[20:23], v[56:59], v[168:171]\n"
    "ds_read_b64_tr_b16 a[10:11], v234 offset:10752\n"
    "v_mfma_f32_16x16x32_bf16 v[168:171], a[24:27], v[64:67], v[168:171]\n"
    "ds_read_b64_tr_b16 a[12:13], v234 offset:12800\n"
    "v_mfma_f32_16x16x32_bf16 v[168:171], a[28:31], v[72:75], v[168:171]\n"
    "ds_read_b64_tr_b16 a[14:15], v234 offset:14848\n"
    "v_mfma_f32_16x16x32_bf16 v[172:175], a[16:19], v[52:55], v[172:175]\n"
    "s_add_u32 m0, s55, 4096\n"
    "v_mfma_f32_16x16x32_bf16 v[172:175], a[20:23], v[60:63], v[172:175]\n"
    "buffer_load_dwordx4 v231, s[40:43], s53 offen lds\n"
    "v_mfma_f32_16x16x32_bf16 v[172:175], a[24:27], v[68:71], v[172:175]\n"
    "s_add_u32 s53, s53, 0x4000\n"
    "v_mfma_f32_16x16x32_bf16 v[172:175], a[28:31], v[76:79], v[172:175]\n"
    "v_mfma_f32_16x16x32_bf16 v[176:179], a[16:19], a[168:171], v[176:179]\n"
    "s_add_u32 m0, s55, 8192\n"
    "s_add_u32 s96, s54, s63\n"
    "v_mfma_f32_16x16x32_bf16 v[176:179], a[20:23], a[176:179], v[176:179]\n"
    "s_add_u32 s54, s54, 16896\n"
    "buffer_load_dwordx4 v231, s[40:43], s53 offen lds\n"
    "v_mfma_f32_16x16x32_bf16 v[176:179], a[24:27], a[184:187], v[176:179]\n"
    "s_add_u32 s53, s53, 0x4000\n"
    "s_sub_u32 s97, s54, 84480\n"
    "v_mfma_f32_16x16x32_bf16 v[176:179], a[28:31], a[192:195], v[176:179]\n"
    "s_waitcnt lgkmcnt(0)\n"
    "s_min_u32 s54, s54, s97\n"
    "v_mfma_f32_16x16x32_bf16 v[180:183], a[0:3], a[164:167], v[180:183]\n"
    "s_sub_u32 s97, s54, s67\n"
    "ds_read_b64_tr_b16 a[16:17], v235 offset:512\n"
    "v_mfma_f32_16x16x32_bf16 v[180:183], a[4:7], a[172:175], v[180:183]\n"
    "s_mov_b32 s67, s54\n"
    "ds_read_b64_tr_b16 a[18:19], v235 offset:2560\n"
    "v_mfma_f32_16x16x32_bf16 v[180:183], a[8:11], a[180:183], v[180:183]\n"
    "ds_read_b64_tr_b16 a[20:21], v235 offset:4608\n"
    "v_mfma_f32_16x16x32_bf16 v[180:183], a[12:15], a[188:191], v[180:183]\n"
    "ds_read_b64_tr_b16 a[22:23], v235 offset:6656\n"
    "v_mfma_f32_16x16x32_bf16 v[184:187], a[0:3], v[48:51], v[184:187]\n"
    "ds_read_b64_tr_b16 a[24:25], v235 offset:8704\n"
    "v_mfma_f32_16x16x32_bf16 v[184:187], a[4:7], v[56:59], v[184:187]\n"
    "ds_read_b64_tr_b16 a[26:27], v235 offset:10752\n"
    "v_mfma_f32_16x16x32_bf16 v[184:187], a[8:11], v[64:67], v[184:187]\n"
    "ds_read_b64_tr_b16 a[28:29], v235 offset:12800\n"
    "v_mfma_f32_16x16x32_bf16 v[184:187], a[12:15], v[72:75], v[184:187]\n"
    "ds_read_b64_tr_b16 a[30:31], v235 offset:14848\n"
    "v_add_u32 v234, s97, v234\n"
    "v_mfma_f32_16x16x32_bf16 v[188:191], a[0:3], v[52:55], v[188:191]\n"
    "s_add_u32 m0, s55, 12288\n"
    "v_xor_b32 v235, 32, v234\n"
    "v_mfma_f32_16x16x32_bf16 v[188:191], a[4:7], v[60:63], v[188:191]\n"
    "buffer_load_dwordx4 v231, s[40:43], s53 offen lds\n"
    "v_mfma_f32_16x16x32_bf16 v[188:191], a[8:11], v[68:71], v[188:191]\n"
    "s_add_u32 s53, s53, 0x4000\n"
    "v_mfma_f32_16x16x32_bf16 v[188:191], a[12:15], v[76:79], v[188:191]\n"
    "s_waitcnt lgkmcnt(15)\n"
    "v_mfma_f32_16x16x32_bf16 v[192:195], a[0:3], a[168:171], v[192:195]\n"
    "buffer_store_dwordx4 v[252:255], v232, s[48:51], s27 offen offset:0 sc1\n"
    "v_mfma_f32_16x16x32_bf16 v[192:195], a[4:7], a[176:179], v[192:195]\n"
    "s_nop 0\n"
    "ds_read_b64 v[252:253], v238 offset:4608\n"
    "v_mfma_f32_16x16x32_bf16 v[192:195], a[8:11], a[184:187], v[192:195]\n"
    "ds_read_b64 v[254:255], v238 offset:4736\n"
    "v_mfma_f32_16x16x32_bf16 v[192:195], a[12:15], a[192:195], v[192:195]\n"
    "s_waitcnt vmcnt(12) lgkmcnt(2)\n"
    "ds_inc_u32 v228, v236\n"
    "v_mfma_f32_16x16x32_bf16 v[196:199], a[16:19], a[164:167], v[196:199]\n"
    "ds_read_b64_tr_b16 a[0:1], v234 offset:0\n"
    "v_mfma_f32_16x16x32_bf16 v[196:199], a[20:23], a[172:175], v[196:199]\n"
    "ds_read_b64_tr_b16 a[2:3], v234 offset:2048\n"
    "v_mfma_f32_16x16x32_bf16 v[196:199], a[24:27], a[180:183], v[196:199]\n"
    "s_add_u32 s61, s61, 1\n"
    "ds_read_b64_tr_b16 a[4:5], v234 offset:4096\n"
    "v_mfma_f32_16x16x32_bf16 v[196:199], a[28:31], a[188:191], v[196:199]\n"
    "ds_read_b64_tr_b16 a[6:7], v234 offset:6144\n"
    "v_mfma_f32_16x16x32_bf16 v[200:203], a[16:19], v[48:51], v[200:203]\n"
    "ds_read_b64_tr_b16 a[8:9], v234 offset:8192\n"
    "v_mfma_f32_16x16x32_bf16 v[200:203], a[20:23], v[56:59], v[200:203]\n"
    "ds_read_b64_tr_b16 a[10:11], v234 offset:10240\n"
    "v_mfma_f32_16x16x32_bf16 v[200:203], a[24:27], v[64:67], v[200:203]\n"
    "ds_read_b64_tr_b16 a[12:13], v234 offset:12288\n"
    "v_mfma_f32_16x16x32_bf16 v[200:203], a[28:31], v[72:75], v[200:203]\n"
    "ds_read_b32 v229, v230\n"
    "v_mfma_f32_16x16x32_bf16 v[204:207], a[16:19], v[52:55], v[204:207]\n"
    "s_waitcnt lgkmcnt(9)\n"
    "ds_read_b64_tr_b16 a[14:15], v234 offset:14336\n"
    "v_mfma_f32_16x16x32_bf16 v[204:207], a[20:23], v[60:63], v[204:207]\n"
    "buffer_store_dwordx4 v[252:255], v232, s[48:51], s28 offen offset:0 sc1\n"
    "v_mfma_f32_16x16x32_bf16 v[204:207], a[24:27], v[68:71], v[204:207]\n"
    "v_mfma_f32_16x16x32_bf16 v[204:207], a[28:31], v[76:79], v[204:207]\n"
    "v_mfma_f32_16x16x32_bf16 v[208:211], a[16:19], a[168:171], v[208:211]\n"
    "v_mfma_f32_16x16x32_bf16 v[208:211], a[20:23], a[176:179], v[208:211]\n"
    "s_waitcnt lgkmcnt(1)\n"
    "v_cmp_gt_u32 vcc, s61, v229\n"
    "v_mfma_f32_16x16x32_bf16 v[208:211], a[24:27], a[184:187], v[208:211]\n"
    "v_mfma_f32_16x16x32_bf16 v[208:211], a[28:31], a[192:195], v[208:211]\n"
    "s_waitcnt lgkmcnt(0)\n"
    "v_mfma_f32_16x16x32_bf16 v[148:151], a[0:3], v[80:83], v[148:151]\n"
    "s_cbranch_vccnz L_sp21%=\n"
    "L_rt21%=:\n"
    "ds_read_b64_tr_b16 a[16:17], v235 offset:0\n"
    "v_mfma_f32_16x16x32_bf16 v[148:151], a[4:7], v[92:95], v[148:151]\n"
    "ds_read_b64_tr_b16 a[18:19], v235 offset:2048\n"
    "v_mfma_f32_16x16x32_bf16 v[148:151], a[8:11], v[100:103], v[148:151]\n"
    "ds_read_b64_tr_b16 a[20:21], v235 offset:4096\n"
    "v_mfma_f32_16x16x32_bf16 v[148:151], a[12:15], v[108:111], v[148:151]\n"
    "ds_read_b64_tr_b16 a[22:23], v235 offset:6144\n"
    "v_mfma_f32_16x16x32_bf16 v[152:155], a[0:3], v[84:87], v[152:155]\n"
    "ds_read_b64_tr_b16 a[24:25], v235 offset:8192\n"
    "v_mfma_f32_16x16x32_bf16 v[152:155], a[4:7], a[200:203], v[152:155]\n"
    "ds_read_b64_tr_b16 a[26:27], v235 offset:10240\n"
    "v_mfma_f32_16x16x32_bf16 v[152:155], a[8:11], a[208:211], v[152:155]\n"
    "ds_read_b64_tr_b16 a[28:29], v235 offset:12288\n"
    "v_mfma_f32_16x16x32_bf16 v[152:155], a[12:15], a[216:219], v[152:155]\n"
    "ds_read_b64_tr_b16 a[30:31], v235 offset:14336\n"
    "v_mfma_f32_16x16x32_bf16 v[156:159], a[0:3], v[88:91], v[156:159]\n"
    "s_add_u32 m0, s96, 0\n"
    "v_mfma_f32_16x16x32_bf16 v[156:159], a[4:7], a[204:207], v[156:159]\n"
    "buffer_load_dwordx4 v231, s[40:43], s53 offen lds\n"
    "v_mfma_f32_16x16x32_bf16 v[156:159], a[8:11], a[212:215], v[156:159]\n"
    "s_add_u32 s53, s53, 0x4000\n"
    "v_mfma_f32_16x16x32_bf16 v[156:159], a[12:15], a[220:223], v[156:159]\n"
    "ds_read_b64 v[252:253], v238 offset:4672\n"
    "v_mfma_f32_16x16x32_bf16 v[160:163], a[0:3], a[196:199], v[160:163]\n"
    "s_add_u32 m0, s96, 4096\n"
    "ds_read_b64 v[254:255], v238 offset:4800\n"
    "v_mfma_f32_16x16x32_bf16 v[160:163], a[4:7], v[96:99], v[160:163]\n"
    "buffer_load_dwordx4 v231, s[40:43], s53 offen lds\n"
    "v_mfma_f32_16x16x32_bf16 v[160:163], a[8:11], v[104:107], v[160:163]\n"
    "s_add_u32 s53, s53, 0x4000\n"
    "v_mfma_f32_16x16x32_bf16 v[160:163], a[12:15], v[112:115], v[160:163]\n"
    "s_waitcnt lgkmcnt(2)\n"
    "v_mfma_f32_16x16x32_bf16 v[164:167], a[16:19], v[80:83], v[164:167]\n"
    "ds_read_b64_tr_b16 a[0:1], v234 offset:512\n"
    "v_mfma_f32_16x16x32_bf16 v[164:167], a[20:23], v[92:95], v[164:167]\n"
    "ds_read_b64_tr_b16 a[2:3], v234 offset:2560\n"
    "v_mfma_f32_16x16x32_bf16 v[164:167], a[24:27], v[100:103], v[164:167]\n"
    "ds_read_b64_tr_b16 a[4:5], v234 offset:4608\n"
    "v_mfma_f32_16x16x32_bf16 v[164:167], a[28:31], v[108:111], v[164:167]\n"
    "ds_read_b64_tr_b16 a[6:7], v234 offset:6656\n"
    "v_mfma_f32_16x16x32_bf16 v[168:171], a[16:19], v[84:87], v[168:171]\n"
    "ds_read_b64_tr_b16 a[8:9], v234 offset:8704\n"
    "v_mfma_f32_16x16x32_bf16 v[168:171], a[20:23], a[200:203], v[168:171]\n"
    "ds_read_b64_tr_b16 a[10:11], v234 offset:10752\n"
    "v_mfma_f32_16x16x32_bf16 v[168:171], a[24:27], a[208:211], v[168:171]\n"
    "ds_read_b64_tr_b16 a[12:13], v234 offset:12800\n"
    "v_mfma_f32_16x16x32_bf16 v[168:171], a[28:31], a[216:219], v[168:171]\n"
    "s_waitcnt vmcnt(19)\n"
    "ds_read_b64_tr_b16 a[14:15], v234 offset:14848\n"
    "v_mfma_f32_16x16x32_bf16 v[172:175], a[16:19], v[88:91], v[172:175]\n"
    "ds_read_b128 a[32:35], v236 offset:0\n"
    "v_mfma_f32_16x16x32_bf16 v[172:175], a[20:23], a[204:207], v[172:175]\n"
    "ds_read_b128 a[36:39], v236 offset:64\n"
    "v_mfma_f32_16x16x32_bf16 v[172:175], a[24:27], a[212:215], v[172:175]\n"
    "ds_read_b128 a[40:43], v236 offset:128\n"
    "v_mfma_f32_16x16x32_bf16 v[172:175], a[28:31], a[220:223], v[172:175]\n"
    "ds_read_b128 a[44:47], v236 offset:192\n"
    "v_mfma_f32_16x16x32_bf16 v[176:179], a[16:19], a[196:199], v[176:179]\n"
    "s_add_u32 s55, s54, s63\n"
    "ds_read_b128 a[48:51], v236 offset:256\n"
    "v_mfma_f32_16x16x32_bf16 v[176:179], a[20:23], v[96:99], v[176:179]\n"
    "s_add_u32 s54, s54, 16896\n"
    "ds_read_b128 a[52:55], v236 offset:320\n"
    "v_mfma_f32_16x16x32_bf16 v[176:179], a[24:27], v[104:107], v[176:179]\n"
    "s_sub_u32 s97, s54, 84480\n"
    "ds_read_b128 a[56:59], v236 offset:384\n"
    "v_mfma_f32_16x16x32_bf16 v[176:179], a[28:31], v[112:115], v[176:179]\n"
    "s_waitcnt lgkmcnt(7)\n"
    "ds_read_b128 a[60:63], v236 offset:448\n"
    "s_min_u32 s54, s54, s97\n"
    "v_mfma_f32_16x16x32_bf16 v[180:183], a[0:3], v[80:83], v[180:183]\n"
    "s_sub_u32 s97, s54, s67\n"
    "ds_read_b64_tr_b16 a[16:17], v235 offset:512\n"
    "v_mfma_f32_16x16x32_bf16 v[180:183], a[4:7], v[92:95], v[180:183]\n"
    "s_mov_b32 s67, s54\n"
    "ds_read_b64_tr_b16 a[18:19], v235 offset:2560\n"
    "v_mfma_f32_16x16x32_bf16 v[180:183], a[8:11], v[100:103], v[180:183]\n"
    "ds_read_b64_tr_b16 a[20:21], v235 offset:4608\n"
    "v_mfma_f32_16x16x32_bf16 v[180:183], a[12:15], v[108:111], v[180:183]\n"
    "ds_read_b64_tr_b16 a[22:23], v235 offset:6656\n"
    "v_mfma_f32_16x16x32_bf16 v[184:187], a[0:3], v[84:87], v[184:187]\n"
    "ds_read_b64_tr_b16 a[24:25], v235 offset:8704\n"
    "v_mfma_f32_16x16x32_bf16 v[184:187], a[4:7], a[200:203], v[184:187]\n"
    "ds_read_b64_tr_b16 a[26:27], v235 offset:10752\n"
    "v_mfma_f32_16x16x32_bf16 v[184:187], a[8:11], a[208:211], v[184:187]\n"
    "s_waitcnt lgkmcnt(6)\n"
    "ds_read_b64_tr_b16 a[28:29], v235 offset:12800\n"
    "v_mfma_f32_16x16x32_bf16 v[184:187], a[12:15], a[216:219], v[184:187]\n"
    "ds_read_b64_tr_b16 a[30:31], v235 offset:14848\n"
    "v_add_u32 v234, s97, v234\n"
    "v_mfma_f32_16x16x32_bf16 v[188:191], a[0:3], v[88:91], v[188:191]\n"
    "s_waitcnt lgkmcnt(15)\n"
    "v_xor_b32 v235, 32, v234\n"
    "v_mfma_f32_16x16x32_bf16 v[188:191], a[4:7], a[204:207], v[188:191]\n"
    "buffer_store_dwordx4 v[252:255], v232, s[48:51], s29 offen offset:0 sc1\n"
    "v_mfma_f32_16x16x32_bf16 v[188:191], a[8:11], a[212:215], v[188:191]\n"
    "s_nop 0\n"
    "ds_read_b64 v[252:253], v238 offset:6912\n"
    "v_mfma_f32_16x16x32_bf16 v[188:191], a[12:15], a[220:223], v[188:191]\n"
    "s_add_u32 m0, s96, 8192\n"
    "ds_read_b64 v[254:255], v238 offset:7040\n"
    "v_mfma_f32_16x16x32_bf16 v[192:195], a[0:3], a[196:199], v[192:195]\n"
    "buffer_load_dwordx4 v231, s[40:43], s53 offen lds\n"
    "v_mfma_f32_16x16x32_bf16 v[192:195], a[4:7], v[96:99], v[192:195]\n"
    "s_add_u32 s53, s53, 0x4000\n"
    "v_mfma_f32_16x16x32_bf16 v[192:195], a[8:11], v[104:107], v[192:195]\n"
    "s_add_u32 m0, s96, 12288\n"
    "v_mfma_f32_16x16x32_bf16 v[192:195], a[12:15], v[112:115], v[192:195]\n"
    "s_waitcnt vmcnt(13) lgkmcnt(2)\n"
    "buffer_load_dwordx4 v231, s[40:43], s53 offen lds\n"
    "v_mfma_f32_16x16x32_bf16 v[196:199], a[16:19], v[80:83], v[196:199]\n"
    "s_add_u32 s53, s53, 0x4000\n"
    "ds_inc_u32 v228, v236\n"
    "v_mfma_f32_16x16x32_bf16 v[196:199], a[20:23], v[92:95], v[196:199]\n"
    "ds_read_b64_tr_b16 a[0:1], v234 offset:0\n"
    "v_mfma_f32_16x16x32_bf16 v[196:199], a[24:27], v[100:103], v[196:199]\n"
    "s_add_u32 s61, s61, 1\n"
    "ds_read_b64_tr_b16 a[2:3], v234 offset:2048\n"
    "v_mfma_f32_16x16x32_bf16 v[196:199], a[28:31], v[108:111], v[196:199]\n"
    "ds_read_b64_tr_b16 a[4:5], v234 offset:4096\n"
    "v_mfma_f32_16x16x32_bf16 v[200:203], a[16:19], v[84:87], v[200:203]\n"
    "ds_read_b64_tr_b16 a[6:7], v234 offset:6144\n"
    "v_mfma_f32_16x16x32_bf16 v[200:203], a[20:23], a[200:203], v[200:203]\n"
    "ds_read_b64_tr_b16 a[8:9], v234 offset:8192\n"
    "v_mfma_f32_16x16x32_bf16 v[200:203], a[24:27], a[208:211], v[200:203]\n"
    "ds_read_b64_tr_b16 a[10:11], v234 offset:10240\n"
    "v_mfma_f32_16x16x32_bf16 v[200:203], a[28:31], a[216:219], v[200:203]\n"
    "ds_read_b64_tr_b16 a[12:13], v234 offset:12288\n"
    "v_mfma_f32_16x16x32_bf16 v[204:207], a[16:19], v[88:91], v[204:207]\n"
    "s_waitcnt lgkmcnt(8)\n"
    "ds_read_b32 v229, v230\n"
    "ds_read_b64_tr_b16 a[14:15], v234 offset:14336\n"
    "v_mfma_f32_16x16x32_bf16 v[204:207], a[20:23], a[204:207], v[204:207]\n"
    "buffer_store_dwordx4 v[252:255], v232, s[48:51], s30 offen offset:0 sc1\n"
    "v_mfma_f32_16x16x32_bf16 v[204:207], a[24:27], a[212:215], v[204:207]\n"
    "s_nop 0\n"
    "ds_read_b64 v[252:253], v238 offset:6976\n"
    "v_mfma_f32_16x16x32_bf16 v[204:207], a[28:31], a[220:223], v[204:207]\n"
    "ds_read_b64 v[254:255], v238 offset:7104\n"
    "v_mfma_f32_16x16x32_bf16 v[208:211], a[16:19], a[196:199], v[208:211]\n"
    "v_mfma_f32_16x16x32_bf16 v[208:211], a[20:23], v[96:99], v[208:211]\n"
    "s_waitcnt lgkmcnt(3)\n"
    "v_cmp_gt_u32 vcc, s61, v229\n"
    "v_mfma_f32_16x16x32_bf16 v[208:211], a[24:27], v[104:107], v[208:211]\n"
    "s_add_u32 m0, s59, 0\n"
    "v_mfma_f32_16x16x32_bf16 v[208:211], a[28:31], v[112:115], v[208:211]\n"
    "s_waitcnt lgkmcnt(2)\n"
    "buffer_load_dwordx4 v230, s[44:47], s58 offen nt lds\n"
    "v_mfma_f32_16x16x32_bf16 v[148:151], a[0:3], v[116:119], v[148:151]\n"
    "s_cbranch_vccnz L_sp22%=\n"
    "L_rt22%=:\n"
    "ds_read_b64_tr_b16 a[16:17], v235 offset:0\n"
    "v_mfma_f32_16x16x32_bf16 v[148:151], a[4:7], v[124:127], v[148:151]\n"
    "s_add_u32 s58, s58, 1024\n"
    "ds_read_b64_tr_b16 a[18:19], v235 offset:2048\n"
    "v_mfma_f32_16x16x32_bf16 v[148:151], a[8:11], v[132:135], v[148:151]\n"
    "ds_read_b64_tr_b16 a[20:21], v235 offset:4096\n"
    "v_mfma_f32_16x16x32_bf16 v[148:151], a[12:15], v[140:143], v[148:151]\n"
    "ds_read_b64_tr_b16 a[22:23], v235 offset:6144\n"
    "v_mfma_f32_16x16x32_bf16 v[152:155], a[0:3], a[224:227], v[152:155]\n"
    "ds_read_b64_tr_b16 a[24:25], v235 offset:8192\n"
    "v_mfma_f32_16x16x32_bf16 v[152:155], a[4:7], a[232:235], v[152:155]\n"
    "ds_read_b64_tr_b16 a[26:27], v235 offset:10240\n"
    "v_mfma_f32_16x16x32_bf16 v[152:155], a[8:11], a[240:243], v[152:155]\n"
    "ds_read_b64_tr_b16 a[28:29], v235 offset:12288\n"
    "v_mfma_f32_16x16x32_bf16 v[152:155], a[12:15], a[248:251], v[152:155]\n"
    "ds_read_b64_tr_b16 a[30:31], v235 offset:14336\n"
    "v_mul_f32 v148, v148, v244\n"
    "v_mfma_f32_16x16x32_bf16 v[156:159], a[0:3], a[228:231], v[156:159]\n"
    "s_waitcnt lgkmcnt(8)\n"
    "v_mul_f32 v149, v149, v244\n"
    "v_mul_f32 v150, v150, v244\n"
    "v_mfma_f32_16x16x32_bf16 v[156:159], a[4:7], a[236:239], v[156:159]\n"
    "buffer_store_dwordx4 v[252:255], v232, s[48:51], s31 offen offset:0 sc1\n"
    "v_mfma_f32_16x16x32_bf16 v[156:159], a[8:11], a[244:247], v[156:159]\n"
    "v_mul_f32 v151, v151, v244\n"
    "v_cvt_pk_bf16_f32 v148, v148, v149\n"
    "v_mfma_f32_16x16x32_bf16 v[156:159], a[12:15], a[252:255], v[156:159]\n"
    "v_cvt_pk_bf16_f32 v149, v150, v151\n"
    "v_mul_f32 v152, v152, v245\n"
    "v_mfma_f32_16x16x32_bf16 v[160:163], a[0:3], v[120:123], v[160:163]\n"
    "s_add_u32 m0, s55, 0\n"
    "ds_write_b64 v237, v[148:149] offset:0\n"
    "v_mfma_f32_16x16x32_bf16 v[160:163], a[4:7], v[128:131], v[160:163]\n"
    "buffer_load_dwordx4 v231, s[40:43], s53 offen lds\n"
    "v_mfma_f32_16x16x32_bf16 v[160:163], a[8:11], v[136:139], v[160:163]\n"
    "s_add_u32 s53, s53, 0x4000\n"
    "v_mul_f32 v153, v153, v245\n"
    "v_mul_f32 v154, v154, v245\n"
    "v_mfma_f32_16x16x32_bf16 v[160:163], a[12:15], v[144:147], v[160:163]\n"
    "s_waitcnt lgkmcnt(1)\n"
    "v_mul_f32 v155, v155, v245\n"
    "v_cvt_pk_bf16_f32 v152, v152, v153\n"
    "v_mfma_f32_16x16x32_bf16 v[164:167], a[16:19], v[116:119], v[164:167]\n"
    "ds_read_b64_tr_b16 a[0:1], v234 offset:512\n"
    "v_cvt_pk_bf16_f32 v153, v154, v155\n"
    "v_mfma_f32_16x16x32_bf16 v[164:167], a[20:23], v[124:127], v[164:167]\n"
    "ds_read_b64_tr_b16 a[2:3], v234 offset:2560\n"
    "v_mul_f32 v156, v156, v246\n"
    "v_mfma_f32_16x16x32_bf16 v[164:167], a[24:27], v[132:135], v[164:167]\n"
    "ds_read_b64_tr_b16 a[4:5], v234 offset:4608\n"
    "v_mul_f32 v157, v157, v246\n"
    "v_mfma_f32_16x16x32_bf16 v[164:167], a[28:31], v[140:143], v[164:167]\n"
    "ds_read_b64_tr_b16 a[6:7], v234 offset:6656\n"
    "v_mul_f32 v158, v158, v246\n"
    "v_mfma_f32_16x16x32_bf16 v[168:171], a[16:19], a[224:227], v[168:171]\n"
    "ds_read_b64_tr_b16 a[8:9], v234 offset:8704\n"
    "v_mul_f32 v159, v159, v246\n"
    "v_mfma_f32_16x16x32_bf16 v[168:171], a[20:23], a[232:235], v[168:171]\n"
    "ds_read_b64_tr_b16 a[10:11], v234 offset:10752\n"
    "v_cvt_pk_bf16_f32 v156, v156, v157\n"
    "v_mfma_f32_16x16x32_bf16 v[168:171], a[24:27], a[240:243], v[168:171]\n"
    "ds_read_b64_tr_b16 a[12:13], v234 offset:12800\n"
    "v_cvt_pk_bf16_f32 v157, v158, v159\n"
    "v_mfma_f32_16x16x32_bf16 v[168:171], a[28:31], a[248:251], v[168:171]\n"
    "ds_read_b64_tr_b16 a[14:15], v234 offset:14848\n"
    "v_mul_f32 v160, v160, v247\n"
    "v_mfma_f32_16x16x32_bf16 v[172:175], a[16:19], a[228:231], v[172:175]\n"
    "ds_write_b64 v237, v[152:153] offset:2304\n"
    "v_mfma_f32_16x16x32_bf16 v[172:175], a[20:23], a[236:239], v[172:175]\n"
    "ds_write_b64 v237, v[156:157] offset:4608\n"
    "v_mfma_f32_16x16x32_bf16 v[172:175], a[24:27], a[244:247], v[172:175]\n"
    "v_mul_f32 v161, v161, v247\n"
    "v_mul_f32 v162, v162, v247\n"
    "v_mfma_f32_16x16x32_bf16 v[172:175], a[28:31], a[252:255], v[172:175]\n"
    "s_add_u32 m0, s55, 4096\n"
    "v_mul_f32 v163, v163, v247\n"
    "v_cvt_pk_bf16_f32 v160, v160, v161\n"
    "v_mfma_f32_16x16x32_bf16 v[176:179], a[16:19], v[120:123], v[176:179]\n"
    "s_add_u32 s96, s54, s63\n"
    "buffer_load_dwordx4 v231, s[40:43], s53 offen lds\n"
    "v_mfma_f32_16x16x32_bf16 v[176:179], a[20:23], v[128:131], v[176:179]\n"
    "s_add_u32 s53, s53, 0x4000\n"
    "v_cvt_pk_bf16_f32 v161, v162, v163\n"
    "v_mul_f32 v164, v164, v244\n"
    "v_mfma_f32_16x16x32_bf16 v[176:179], a[24:27], v[136:139], v[176:179]\n"
    "s_add_u32 m0, s55, 8192\n"
    "ds_write_b64 v237, v[160:161] offset:6912\n"
    "v_mfma_f32_16x16x32_bf16 v[176:179], a[28:31], v[144:147], v[176:179]\n"
    "s_waitcnt lgkmcnt(3)\n"
    "buffer_load_dwordx4 v231, s[40:43], s53 offen lds\n"
    "v_mfma_f32_16x16x32_bf16 v[180:183], a[0:3], v[116:119], v[180:183]\n"
    "s_add_u32 s53, s53, 0x4000\n"
    "ds_read_b64_tr_b16 a[16:17], v235 offset:512\n"
    "v_mul_f32 v165, v165, v244\n"
    "v_mfma_f32_16x16x32_bf16 v[180:183], a[4:7], v[124:127], v[180:183]\n"
    "s_add_u32 s54, s54, 16896\n"
    "ds_read_b64_tr_b16 a[18:19], v235 offset:2560\n"
    "v_mul_f32 v166, v166, v244\n"
    "v_mfma_f32_16x16x32_bf16 v[180:183], a[8:11], v[132:135], v[180:183]\n"
    "s_sub_u32 s97, s54, 84480\n"
    "ds_read_b64_tr_b16 a[20:21], v235 offset:4608\n"
    "v_mul_f32 v167, v167, v244\n"
    "v_mfma_f32_16x16x32_bf16 v[180:183], a[12:15], v[140:143], v[180:183]\n"
    "s_min_u32 s54, s54, s97\n"
    "ds_read_b64_tr_b16 a[22:23], v235 offset:6656\n"
    "v_cvt_pk_bf16_f32 v164, v164, v165\n"
    "v_mfma_f32_16x16x32_bf16 v[184:187], a[0:3], a[224:227], v[184:187]\n"
    "s_sub_u32 s97, s54, s67\n"
    "ds_read_b64_tr_b16 a[24:25], v235 offset:8704\n"
    "v_cvt_pk_bf16_f32 v165, v166, v167\n"
    "v_mfma_f32_16x16x32_bf16 v[184:187], a[4:7], a[232:235], v[184:187]\n"
    "s_mov_b32 s67, s54\n"
    "ds_read_b64_tr_b16 a[26:27], v235 offset:10752\n"
    "v_mul_f32 v168, v168, v245\n"
    "v_mfma_f32_16x16x32_bf16 v[184:187], a[8:11], a[240:243], v[184:187]\n"
    "ds_read_b64_tr_b16 a[28:29], v235 offset:12800\n"
    "v_mul_f32 v169, v169, v245\n"
    "v_mfma_f32_16x16x32_bf16 v[184:187], a[12:15], a[248:251], v[184:187]\n"
    "ds_read_b64_tr_b16 a[30:31], v235 offset:14848\n"
    "v_add_u32 v234, s97, v234\n"
    "v_mfma_f32_16x16x32_bf16 v[188:191], a[0:3], a[228:231], v[188:191]\n"
    "v_xor_b32 v235, 32, v234\n"
    "v_mul_f32 v170, v170, v245\n"
    "v_mfma_f32_16x16x32_bf16 v[188:191], a[4:7], a[236:239], v[188:191]\n"
    "ds_write_b64 v237, v[164:165] offset:576\n"
    "v_mfma_f32_16x16x32_bf16 v[188:191], a[8:11], a[244:247], v[188:191]\n"
    "v_mul_f32 v171, v171, v245\n"
    "v_cvt_pk_bf16_f32 v168, v168, v169\n"
    "v_mfma_f32_16x16x32_bf16 v[188:191], a[12:15], a[252:255], v[188:191]\n"
    "s_add_u32 m0, s55, 12288\n"
    "v_cvt_pk_bf16_f32 v169, v170, v171\n"
    "v_mul_f32 v172, v172, v246\n"
    "v_mfma_f32_16x16x32_bf16 v[192:195], a[0:3], v[120:123], v[192:195]\n"
    "buffer_load_dwordx4 v231, s[40:43], s53 offen lds\n"
    "v_mfma_f32_16x16x32_bf16 v[192:195], a[4:7], v[128:131], v[192:195]\n"
    "ds_write_b64 v237, v[168:169] offset:2880\n"
    "v_mfma_f32_16x16x32_bf16 v[192:195], a[8:11], v[136:139], v[192:195]\n"
    "v_mul_f32 v173, v173, v246\n"
    "v_mul_f32 v174, v174, v246\n"
    "v_mfma_f32_16x16x32_bf16 v[192:195], a[12:15], v[144:147], v[192:195]\n"
    "s_waitcnt vmcnt(14) lgkmcnt(2)\n"
    "s_add_u32 s53, s53, 0xfffc4000\n"
    "s_xor_b32 s53, s53, 128\n"
    "v_mfma_f32_16x16x32_bf16 v[196:199], a[16:19], v[116:119], v[196:199]\n"
    "ds_inc_u32 v228, v236\n"
    "v_mfma_f32_16x16x32_bf16 v[196:199], a[20:23], v[124:127], v[196:199]\n"
    "ds_read_b64_tr_b16 a[0:1], v234 offset:0\n"
    "v_mul_f32 v175, v175, v246\n"
    "v_mfma_f32_16x16x32_bf16 v[196:199], a[24:27], v[132:135], v[196:199]\n"
    "s_add_u32 s61, s61, 1\n"
    "ds_read_b64_tr_b16 a[2:3], v234 offset:2048\n"
    "v_cvt_pk_bf16_f32 v172, v172, v173\n"
    "v_mfma_f32_16x16x32_bf16 v[196:199], a[28:31], v[140:143], v[196:199]\n"
    "ds_read_b64_tr_b16 a[4:5], v234 offset:4096\n"
    "v_cvt_pk_bf16_f32 v173, v174, v175\n"
    "v_mfma_f32_16x16x32_bf16 v[200:203], a[16:19], a[224:227], v[200:203]\n"
    "ds_read_b64_tr_b16 a[6:7], v234 offset:6144\n"
    "v_mul_f32 v176, v176, v247\n"
    "v_mfma_f32_16x16x32_bf16 v[200:203], a[20:23], a[232:235], v[200:203]\n"
    "ds_read_b64_tr_b16 a[8:9], v234 offset:8192\n"
    "v_mul_f32 v177, v177, v247\n"
    "v_mfma_f32_16x16x32_bf16 v[200:203], a[24:27], a[240:243], v[200:203]\n"
    "ds_read_b64_tr_b16 a[10:11], v234 offset:10240\n"
    "v_mul_f32 v178, v178, v247\n"
    "v_mfma_f32_16x16x32_bf16 v[200:203], a[28:31], a[248:251], v[200:203]\n"
    "ds_read_b64_tr_b16 a[12:13], v234 offset:12288\n"
    "v_mul_f32 v179, v179, v247\n"
    "v_mfma_f32_16x16x32_bf16 v[204:207], a[16:19], a[228:231], v[204:207]\n"
    "ds_read_b64_tr_b16 a[14:15], v234 offset:14336\n"
    "v_cvt_pk_bf16_f32 v176, v176, v177\n"
    "v_mfma_f32_16x16x32_bf16 v[204:207], a[20:23], a[236:239], v[204:207]\n"
    "ds_read_b32 v229, v230\n"
    "v_cvt_pk_bf16_f32 v177, v178, v179\n"
    "v_mfma_f32_16x16x32_bf16 v[204:207], a[24:27], a[244:247], v[204:207]\n"
    "ds_write_b64 v237, v[172:173] offset:5184\n"
    "v_mfma_f32_16x16x32_bf16 v[204:207], a[28:31], a[252:255], v[204:207]\n"
    "s_add_u32 m0, s59, 1056\n"
    "ds_write_b64 v237, v[176:177] offset:7488\n"
    "v_mfma_f32_16x16x32_bf16 v[208:211], a[16:19], v[120:123], v[208:211]\n"
    "buffer_load_dwordx4 v230, s[44:47], s58 offen nt lds\n"
    "v_mfma_f32_16x16x32_bf16 v[208:211], a[20:23], v[128:131], v[208:211]\n"
    "s_waitcnt lgkmcnt(2)\n"
    "v_cmp_gt_u32 vcc, s61, v229\n"
    "v_mul_f32 v180, v180, v244\n"
    "v_mfma_f32_16x16x32_bf16 v[208:211], a[24:27], v[136:139], v[208:211]\n"
    "s_add_u32 s58, s58, 1024\n"
    "v_mul_f32 v181, v181, v244\n"
    "v_mul_f32 v182, v182, v244\n"
    "v_mfma_f32_16x16x32_bf16 v[208:211], a[28:31], v[144:147], v[208:211]\n"
    "s_waitcnt lgkmcnt(3)\n"
    "v_mul_f32 v183, v183, v244\n"
    "v_cvt_pk_bf16_f32 v180, v180, v181\n"
    "v_mfma_f32_16x16x32_bf16 v[148:151], a[0:3], v[0:3], 0\n"
    "s_cbranch_vccnz L_sp23%=\n"
    "L_rt23%=:\n"
    "ds_read_b64_tr_b16 a[16:17], v235 offset:0\n"
    "v_mfma_f32_16x16x32_bf16 v[148:151], a[4:7], v[16:19], v[148:151]\n"
    "ds_read_b64_tr_b16 a[18:19], v235 offset:2048\n"
    "v_cvt_pk_bf16_f32 v181, v182, v183\n"
    "v_mfma_f32_16x16x32_bf16 v[148:151], a[8:11], a[148:151], v[148:151]\n"
    "ds_read_b64_tr_b16 a[20:21], v235 offset:4096\n"
    "v_mul_f32 v184, v184, v245\n"
    "v_mfma_f32_16x16x32_bf16 v[148:151], a[12:15], a[156:159], v[148:151]\n"
    "ds_read_b64_tr_b16 a[22:23], v235 offset:6144\n"
    "v_mul_f32 v185, v185, v245\n"
    "v_mfma_f32_16x16x32_bf16 v[152:155], a[0:3], v[4:7], 0\n"
    "ds_read_b64_tr_b16 a[24:25], v235 offset:8192\n"
    "v_mul_f32 v186, v186, v245\n"
    "v_mfma_f32_16x16x32_bf16 v[152:155], a[4:7], v[20:23], v[152:155]\n"
    "ds_read_b64_tr_b16 a[26:27], v235 offset:10240\n"
    "v_mul_f32 v187, v187, v245\n"
    "v_mfma_f32_16x16x32_bf16 v[152:155], a[8:11], v[32:35], v[152:155]\n"
    "ds_read_b64_tr_b16 a[28:29], v235 offset:12288\n"
    "v_cvt_pk_bf16_f32 v184, v184, v185\n"
    "v_mfma_f32_16x16x32_bf16 v[152:155], a[12:15], v[40:43], v[152:155]\n"
    "ds_read_b64_tr_b16 a[30:31], v235 offset:14336\n"
    "v_cvt_pk_bf16_f32 v185, v186, v187\n"
    "v_mfma_f32_16x16x32_bf16 v[156:159], a[0:3], v[8:11], 0\n"
    "ds_write_b64 v237, v[180:181] offset:1152\n"
    "v_mfma_f32_16x16x32_bf16 v[156:159], a[4:7], v[24:27], v[156:159]\n"
    "ds_write_b64 v237, v[184:185] offset:3456\n"
    "v_mfma_f32_16x16x32_bf16 v[156:159], a[8:11], v[36:39], v[156:159]\n"
    "v_mul_f32 v196, v196, v244\n"
    "v_mul_f32 v197, v197, v244\n"
    "v_mfma_f32_16x16x32_bf16 v[156:159], a[12:15], v[44:47], v[156:159]\n"
    "s_add_u32 m0, s96, 0\n"
    "v_mul_f32 v198, v198, v244\n"
    "v_mul_f32 v199, v199, v244\n"
    "v_mfma_f32_16x16x32_bf16 v[160:163], a[0:3], v[12:15], 0\n"
    "buffer_load_dwordx4 v231, s[40:43], s53 offen lds\n"
    "v_mfma_f32_16x16x32_bf16 v[160:163], a[4:7], v[28:31], v[160:163]\n"
    "s_add_u32 s53, s53, 0x4000\n"
    "v_cvt_pk_bf16_f32 v196, v196, v197\n"
    "v_cvt_pk_bf16_f32 v197, v198, v199\n"
    "v_mfma_f32_16x16x32_bf16 v[160:163], a[8:11], a[152:155], v[160:163]\n"
    "ds_write_b64 v237, v[196:197] offset:1728\n"
    "v_mfma_f32_16x16x32_bf16 v[160:163], a[12:15], a[160:163], v[160:163]\n"
    "s_waitcnt lgkmcnt(3)\n"
    "v_mul_f32 v188, v188, v246\n"
    "v_mul_f32 v189, v189, v246\n"
    "v_mfma_f32_16x16x32_bf16 v[164:167], a[16:19], v[0:3], 0\n"
    "ds_read_b64_tr_b16 a[0:1], v234 offset:512\n"
    "v_mul_f32 v190, v190, v246\n"
    "v_mfma_f32_16x16x32_bf16 v[164:167], a[20:23], v[16:19], v[164:167]\n"
    "ds_read_b64_tr_b16 a[2:3], v234 offset:2560\n"
    "v_mul_f32 v191, v191, v246\n"
    "v_mfma_f32_16x16x32_bf16 v[164:167], a[24:27], a[148:151], v[164:167]\n"
    "ds_read_b64_tr_b16 a[4:5], v234 offset:4608\n"
    "v_cvt_pk_bf16_f32 v188, v188, v189\n"
    "v_mfma_f32_16x16x32_bf16 v[164:167], a[28:31], a[156:159], v[164:167]\n"
    "ds_read_b64_tr_b16 a[6:7], v234 offset:6656\n"
    "v_cvt_pk_bf16_f32 v189, v190, v191\n"
    "v_mfma_f32_16x16x32_bf16 v[168:171], a[16:19], v[4:7], 0\n"
    "ds_read_b64_tr_b16 a[8:9], v234 offset:8704\n"
    "v_mul_f32 v192, v192, v247\n"
    "v_mfma_f32_16x16x32_bf16 v[168:171], a[20:23], v[20:23], v[168:171]\n"
    "ds_read_b64_tr_b16 a[10:11], v234 offset:10752\n"
    "v_mul_f32 v193, v193, v247\n"
    "v_mfma_f32_16x16x32_bf16 v[168:171], a[24:27], v[32:35], v[168:171]\n"
    "ds_read_b64_tr_b16 a[12:13], v234 offset:12800\n"
    "v_mul_f32 v194, v194, v247\n"
    "v_mfma_f32_16x16x32_bf16 v[168:171], a[28:31], v[40:43], v[168:171]\n"
    "ds_read_b64_tr_b16 a[14:15], v234 offset:14848\n"
    "v_mul_f32 v195, v195, v247\n"
    "v_mfma_f32_16x16x32_bf16 v[172:175], a[16:19], v[8:11], 0\n"
    "s_add_u32 m0, s96, 4096\n"
    "ds_read_b64 v[252:253], v238 offset:0\n"
    "v_cvt_pk_bf16_f32 v192, v192, v193\n"
    "v_mfma_f32_16x16x32_bf16 v[172:175], a[20:23], v[24:27], v[172:175]\n"
    "buffer_load_dwordx4 v231, s[40:43], s53 offen lds\n"
    "v_mfma_f32_16x16x32_bf16 v[172:175], a[24:27], v[36:39], v[172:175]\n"
    "s_add_u32 s53, s53, 0x4000\n"
    "ds_write_b64 v237, v[188:189] offset:5760\n"
    "v_mfma_f32_16x16x32_bf16 v[172:175], a[28:31], v[44:47], v[172:175]\n"
    "ds_read_b64 v[254:255], v238 offset:128\n"
    "v_cvt_pk_bf16_f32 v193, v194, v195\n"
    "v_mfma_f32_16x16x32_bf16 v[176:179], a[16:19], v[12:15], 0\n"
    "s_add_u32 m0, s96, 8192\n"
    "ds_write_b64 v237, v[192:193] offset:8064\n"
    "v_mfma_f32_16x16x32_bf16 v[176:179], a[20:23], v[28:31], v[176:179]\n"
    "s_add_u32 s55, s54, s63\n"
    "buffer_load_dwordx4 v231, s[40:43], s53 offen lds\n"
    "v_mfma_f32_16x16x32_bf16 v[176:179], a[24:27], a[152:155], v[176:179]\n"
    "s_add_u32 s53, s53, 0x4000\n"
    "v_xor_b32 v232, 128, v232\n"
    "v_mul_f32 v200, v200, v245\n"
    "v_mfma_f32_16x16x32_bf16 v[176:179], a[28:31], a[160:163], v[176:179]\n"
    "s_waitcnt lgkmcnt(4)\n"
    "v_mul_f32 v201, v201, v245\n"
    "v_mul_f32 v202, v202, v245\n"
    "v_mfma_f32_16x16x32_bf16 v[180:183], a[0:3], v[0:3], 0\n"
    "s_add_u32 s54, s54, 16896\n"
    "ds_read_b64_tr_b16 a[16:17], v235 offset:512\n"
    "v_mul_f32 v203, v203, v245\n"
    "v_mfma_f32_16x16x32_bf16 v[180:183], a[4:7], v[16:19], v[180:183]\n"
    "s_sub_u32 s97, s54, 84480\n"
    "ds_read_b64_tr_b16 a[18:19], v235 offset:2560\n"
    "v_cvt_pk_bf16_f32 v200, v200, v201\n"
    "v_mfma_f32_16x16x32_bf16 v[180:183], a[8:11], a[148:151], v[180:183]\n"
    "s_min_u32 s54, s54, s97\n"
    "ds_read_b64_tr_b16 a[20:21], v235 offset:4608\n"
    "v_cvt_pk_bf16_f32 v201, v202, v203\n"
    "v_mfma_f32_16x16x32_bf16 v[180:183], a[12:15], a[156:159], v[180:183]\n"
    "s_sub_u32 s97, s54, s67\n"
    "ds_read_b64_tr_b16 a[22:23], v235 offset:6656\n"
    "v_mul_f32 v204, v204, v246\n"
    "v_mfma_f32_16x16x32_bf16 v[184:187], a[0:3], v[4:7], 0\n"
    "s_mov_b32 s67, s54\n"
    "ds_read_b64_tr_b16 a[24:25], v235 offset:8704\n"
    "v_mul_f32 v205, v205, v246\n"
    "v_mfma_f32_16x16x32_bf16 v[184:187], a[4:7], v[20:23], v[184:187]\n"
    "ds_read_b64_tr_b16 a[26:27], v235 offset:10752\n"
    "v_mul_f32 v206, v206, v246\n"
    "v_mfma_f32_16x16x32_bf16 v[184:187], a[8:11], v[32:35], v[184:187]\n"
    "ds_read_b64_tr_b16 a[28:29], v235 offset:12800\n"
    "v_mul_f32 v207, v207, v246\n"
    "v_mfma_f32_16x16x32_bf16 v[184:187], a[12:15], v[40:43], v[184:187]\n"
    "ds_read_b64_tr_b16 a[30:31], v235 offset:14848\n"
    "v_add_u32 v234, s97, v234\n"
    "v_mfma_f32_16x16x32_bf16 v[188:191], a[0:3], v[8:11], 0\n"
    "s_add_u32 m0, s96, 12288\n"
    "v_xor_b32 v235, 32, v234\n"
    "v_cvt_pk_bf16_f32 v204, v204, v205\n"
    "v_mfma_f32_16x16x32_bf16 v[188:191], a[4:7], v[24:27], v[188:191]\n"
    "buffer_load_dwordx4 v231, s[40:43], s53 offen lds\n"
    "v_mfma_f32_16x16x32_bf16 v[188:191], a[8:11], v[36:39], v[188:191]\n"
    "s_add_u32 s53, s53, 0x4000\n"
    "ds_write_b64 v237, v[200:201] offset:4032\n"
    "v_mfma_f32_16x16x32_bf16 v[188:191], a[12:15], v[44:47], v[188:191]\n"
    "s_waitcnt lgkmcnt(10)\n"
    "v_cvt_pk_bf16_f32 v205, v206, v207\n"
    "v_mul_f32 v208, v208, v247\n"
    "v_mfma_f32_16x16x32_bf16 v[192:195], a[0:3], v[12:15], 0\n"
    "buffer_store_dwordx4 v[252:255], v232, s[48:51], s24 offen offset:0 sc1\n"
    "v_mfma_f32_16x16x32_bf16 v[192:195], a[4:7], v[28:31], v[192:195]\n"
    "ds_write_b64 v237, v[204:205] offset:6336\n"
    "v_mfma_f32_16x16x32_bf16 v[192:195], a[8:11], a[152:155], v[192:195]\n"
    "ds_read_b64 v[252:253], v238 offset:64\n"
    "v_mul_f32 v209, v209, v247\n"
    "v_mfma_f32_16x16x32_bf16 v[192:195], a[12:15], a[160:163], v[192:195]\n"
    "s_waitcnt vmcnt(13) lgkmcnt(3)\n"
    "ds_inc_u32 v228, v236\n"
    "v_mfma_f32_16x16x32_bf16 v[196:199], a[16:19], v[0:3], 0\n"
    "ds_read_b64_tr_b16 a[0:1], v234 offset:0\n"
    "v_mul_f32 v210, v210, v247\n"
    "v_mfma_f32_16x16x32_bf16 v[196:199], a[20:23], v[16:19], v[196:199]\n"
    "ds_read_b64_tr_b16 a[2:3], v234 offset:2048\n"
    "v_mul_f32 v211, v211, v247\n"
    "v_mfma_f32_16x16x32_bf16 v[196:199], a[24:27], a[148:151], v[196:199]\n"
    "s_add_u32 s61, s61, 1\n"
    "ds_read_b64_tr_b16 a[4:5], v234 offset:4096\n"
    "v_cvt_pk_bf16_f32 v208, v208, v209\n"
    "v_mfma_f32_16x16x32_bf16 v[196:199], a[28:31], a[156:159], v[196:199]\n"
    "ds_read_b64_tr_b16 a[6:7], v234 offset:6144\n"
    "v_cvt_pk_bf16_f32 v209, v210, v211\n"
    "v_mfma_f32_16x16x32_bf16 v[200:203], a[16:19], v[4:7], 0\n"
    "ds_read_b64_tr_b16 a[8:9], v234 offset:8192\n"
    "v_mfma_f32_16x16x32_bf16 v[200:203], a[20:23], v[20:23], v[200:203]\n"
    "ds_read_b64_tr_b16 a[10:11], v234 offset:10240\n"
    "v_mfma_f32_16x16x32_bf16 v[200:203], a[24:27], v[32:35], v[200:203]\n"
    "ds_read_b64_tr_b16 a[12:13], v234 offset:12288\n"
    "v_mfma_f32_16x16x32_bf16 v[200:203], a[28:31], v[40:43], v[200:203]\n"
    "ds_read_b64 v[254:255], v238 offset:192\n"
    "v_mfma_f32_16x16x32_bf16 v[204:207], a[16:19], v[8:11], 0\n"
    "s_add_u32 m0, s59, 2112\n"
    "ds_read_b32 v229, v230\n"
    "ds_read_b64_tr_b16 a[14:15], v234 offset:14336\n"
    "v_mfma_f32_16x16x32_bf16 v[204:207], a[20:23], v[24:27], v[204:207]\n"
    "buffer_load_dwordx4 v230, s[44:47], s58 offen nt lds\n"
    "v_mfma_f32_16x16x32_bf16 v[204:207], a[24:27], v[36:39], v[204:207]\n"
    "s_add_u32 s58, s58, 1024\n"
    "ds_write_b64 v237, v[208:209] offset:8640\n"
    "v_mfma_f32_16x16x32_bf16 v[204:207], a[28:31], v[44:47], v[204:207]\n"
    "v_mfma_f32_16x16x32_bf16 v[208:211], a[16:19], v[12:15], 0\n"
    "v_mfma_f32_16x16x32_bf16 v[208:211], a[20:23], v[28:31], v[208:211]\n"
    "s_waitcnt lgkmcnt(2)\n"
    "v_cmp_gt_u32 vcc, s61, v229\n"
    "v_mfma_f32_16x16x32_bf16 v[208:211], a[24:27], a[152:155], v[208:211]\n"
    "s_waitcnt lgkmcnt(3)\n"
    "v_mfma_f32_16x16x32_bf16 v[208:211], a[28:31], a[160:163], v[208:211]\n"
    "s_waitcnt lgkmcnt(1)\n"
    "buffer_store_dwordx4 v[252:255], v232, s[48:51], s25 offen offset:0 sc1\n"
    "v_mfma_f32_16x16x32_bf16 v[148:151], a[0:3], a[164:167], v[148:151]\n"
    "s_cbranch_vccnz L_sp24%=\n"
    "L_rt24%=:\n"
    "ds_read_b64_tr_b16 a[16:17], v235 offset:0\n"
    "v_mfma_f32_16x16x32_bf16 v[148:151], a[4:7], a[172:175], v[148:151]\n"
    "ds_read_b64_tr_b16 a[18:19], v235 offset:2048\n"
    "v_mfma_f32_16x16x32_bf16 v[148:151], a[8:11], a[180:183], v[148:151]\n"
    "ds_read_b64_tr_b16 a[20:21], v235 offset:4096\n"
    "v_mfma_f32_16x16x32_bf16 v[148:151], a[12:15], a[188:191], v[148:151]\n"
    "ds_read_b64_tr_b16 a[22:23], v235 offset:6144\n"
    "v_mfma_f32_16x16x32_bf16 v[152:155], a[0:3], v[48:51], v[152:155]\n"
    "ds_read_b64_tr_b16 a[24:25], v235 offset:8192\n"
    "v_mfma_f32_16x16x32_bf16 v[152:155], a[4:7], v[56:59], v[152:155]\n"
    "ds_read_b64 v[252:253], v238 offset:2304\n"
    "v_mfma_f32_16x16x32_bf16 v[152:155], a[8:11], v[64:67], v[152:155]\n"
    "ds_read_b64_tr_b16 a[26:27], v235 offset:10240\n"
    "v_mfma_f32_16x16x32_bf16 v[152:155], a[12:15], v[72:75], v[152:155]\n"
    "ds_read_b64 v[254:255], v238 offset:2432\n"
    "ds_read_b64_tr_b16 a[28:29], v235 offset:12288\n"
    "v_mfma_f32_16x16x32_bf16 v[156:159], a[0:3], v[52:55], v[156:159]\n"
    "s_add_u32 m0, s55, 0\n"
    "ds_read_b64_tr_b16 a[30:31], v235 offset:14336\n"
    "v_mfma_f32_16x16x32_bf16 v[156:159], a[4:7], v[60:63], v[156:159]\n"
    "buffer_load_dwordx4 v231, s[40:43], s53 offen lds\n"
    "v_mfma_f32_16x16x32_bf16 v[156:159], a[8:11], v[68:71], v[156:159]\n"
    "s_add_u32 s53, s53, 0x4000\n"
    "v_mfma_f32_16x16x32_bf16 v[156:159], a[12:15], v[76:79], v[156:159]\n"
    "v_mfma_f32_16x16x32_bf16 v[160:163], a[0:3], a[168:171], v[160:163]\n"
    "s_waitcnt lgkmcnt(2)\n"
    "v_mfma_f32_16x16x32_bf16 v[160:163], a[4:7], a[176:179], v[160:163]\n"
    "buffer_store_dwordx4 v[252:255], v232, s[48:51], s26 offen offset:0 sc1\n"
    "v_mfma_f32_16x16x32_bf16 v[160:163], a[8:11], a[184:187], v[160:163]\n"
    "v_mfma_f32_16x16x32_bf16 v[160:163], a[12:15], a[192:195], v[160:163]\n"
    "s_waitcnt lgkmcnt(0)\n"
    "v_mfma_f32_16x16x32_bf16 v[164:167], a[16:19], a[164:167], v[164:167]\n"
    "ds_read_b64_tr_b16 a[0:1], v234 offset:512\n"
    "v_mfma_f32_16x16x32_bf16 v[164:167], a[20:23], a[172:175], v[164:167]\n"
    "ds_read_b64_tr_b16 a[2:3], v234 offset:2560\n"
    "v_mfma_f32_16x16x32_bf16 v[164:167], a[24:27], a[180:183], v[164:167]\n"
    "ds_read_b64_tr_b16 a[4:5], v234 offset:4608\n"
    "v_mfma_f32_16x16x32_bf16 v[164:167], a[28:31], a[188:191], v[164:167]\n"
    "ds_read_b64_tr_b16 a[6:7], v234 offset:6656\n"
    "v_mfma_f32_16x16x32_bf16 v[168:171], a[16:19], v[48:51], v[168:171]\n"
    "ds_read_b64_tr_b16 a[8:9], v234 offset:8704\n"
    "v_mfma_f32_16x16x32_bf16 v[168:171], a[20:23], v[56:59], v[168:171]\n"
    "ds_read_b64_tr_b16 a[10:11], v234 offset:10752\n"
    "v_mfma_f32_16x16x32_bf16 v[168:171], a[24:27], v[64:67], v[168:171]\n"
    "ds_read_b64_tr_b16 a[12:13], v234 offset:12800\n"
    "v_mfma_f32_16x16x32_bf16 v[168:171], a[28:31], v[72:75], v[168:171]\n"
    "ds_read_b64_tr_b16 a[14:15], v234 offset:14848\n"
    "v_mfma_f32_16x16x32_bf16 v[172:175], a[16:19], v[52:55], v[172:175]\n"
    "s_add_u32 m0, s55, 4096\n"
    "ds_read_b64 v[252:253], v238 offset:2368\n"
    "v_mfma_f32_16x16x32_bf16 v[172:175], a[20:23], v[60:63], v[172:175]\n"
    "buffer_load_dwordx4 v231, s[40:43], s53 offen lds\n"
    "v_mfma_f32_16x16x32_bf16 v[172:175], a[24:27], v[68:71], v[172:175]\n"
    "s_add_u32 s53, s53, 0x4000\n"
    "ds_read_b64 v[254:255], v238 offset:2496\n"
    "v_mfma_f32_16x16x32_bf16 v[172:175], a[28:31], v[76:79], v[172:175]\n"
    "v_mfma_f32_16x16x32_bf16 v[176:179], a[16:19], a[168:171], v[176:179]\n"
    "s_add_u32 m0, s55, 8192\n"
    "s_add_u32 s96, s54, s63\n"
    "v_mfma_f32_16x16x32_bf16 v[176:179], a[20:23], a[176:179], v[176:179]\n"
    "s_add_u32 s54, s54, 16896\n"
    "buffer_load_dwordx4 v231, s[40:43], s53 offen lds\n"
    "v_mfma_f32_16x16x32_bf16 v[176:179], a[24:27], a[184:187], v[176:179]\n"
    "s_add_u32 s53, s53, 0x4000\n"
    "s_sub_u32 s97, s54, 84480\n"
    "v_mfma_f32_16x16x32_bf16 v[176:179], a[28:31], a[192:195], v[176:179]\n"
    "s_waitcnt lgkmcnt(2)\n"
    "s_min_u32 s54, s54, s97\n"
    "v_mfma_f32_16x16x32_bf16 v[180:183], a[0:3], a[164:167], v[180:183]\n"
    "s_sub_u32 s97, s54, s67\n"
    "ds_read_b64_tr_b16 a[16:17], v235 offset:512\n"
    "v_mfma_f32_16x16x32_bf16 v[180:183], a[4:7], a[172:175], v[180:183]\n"
    "s_mov_b32 s67, s54\n"
    "ds_read_b64_tr_b16 a[18:19], v235 offset:2560\n"
    "v_mfma_f32_16x16x32_bf16 v[180:183], a[8:11], a[180:183], v[180:183]\n"
    "ds_read_b64_tr_b16 a[20:21], v235 offset:4608\n"
    "v_mfma_f32_16x16x32_bf16 v[180:183], a[12:15], a[188:191], v[180:183]\n"
    "ds_read_b64_tr_b16 a[22:23], v235 offset:6656\n"
    "v_mfma_f32_16x16x32_bf16 v[184:187], a[0:3], v[48:51], v[184:187]\n"
    "ds_read_b64_tr_b16 a[24:25], v235 offset:8704\n"
    "v_mfma_f32_16x16x32_bf16 v[184:187], a[4:7], v[56:59], v[184:187]\n"
    "ds_read_b64_tr_b16 a[26:27], v235 offset:10752\n"
    "v_mfma_f32_16x16x32_bf16 v[184:187], a[8:11], v[64:67], v[184:187]\n"
    "ds_read_b64_tr_b16 a[28:29], v235 offset:12800\n"
    "v_mfma_f32_16x16x32_bf16 v[184:187], a[12:15], v[72:75], v[184:187]\n"
    "ds_read_b64_tr_b16 a[30:31], v235 offset:14848\n"
    "v_add_u32 v234, s97, v234\n"
    "v_mfma_f32_16x16x32_bf16 v[188:191], a[0:3], v[52:55], v[188:191]\n"
    "s_add_u32 m0, s59, 3168\n"
    "v_xor_b32 v235, 32, v234\n"
    "v_mfma_f32_16x16x32_bf16 v[188:191], a[4:7], v[60:63], v[188:191]\n"
    "buffer_load_dwordx4 v230, s[44:47], s58 offen nt lds\n"
    "v_mfma_f32_16x16x32_bf16 v[188:191], a[8:11], v[68:71], v[188:191]\n"
    "s_add_u32 s58, s58, 1024\n"
    "v_mfma_f32_16x16x32_bf16 v[188:191], a[12:15], v[76:79], v[188:191]\n"
    "s_add_u32 m0, s55, 12288\n"
    "v_mfma_f32_16x16x32_bf16 v[192:195], a[0:3], a[168:171], v[192:195]\n"
    "buffer_load_dwordx4 v231, s[40:43], s53 offen lds\n"
    "v_mfma_f32_16x16x32_bf16 v[192:195], a[4:7], a[176:179], v[192:195]\n"
    "s_add_u32 s53, s53, 0x4000\n"
    "v_mfma_f32_16x16x32_bf16 v[192:195], a[8:11], a[184:187], v[192:195]\n"
    "s_waitcnt lgkmcnt(8)\n"
    "v_mfma_f32_16x16x32_bf16 v[192:195], a[12:15], a[192:195], v[192:195]\n"
    "s_waitcnt vmcnt(14) lgkmcnt(0)\n"
    "buffer_store_dwordx4 v[252:255], v232, s[48:51], s27 offen offset:0 sc1\n"
    "v_mfma_f32_16x16x32_bf16 v[196:199], a[16:19], a[164:167], v[196:199]\n"
    "ds_inc_u32 v228, v236\n"
    "v_mfma_f32_16x16x32_bf16 v[196:199], a[20:23], a[172:175], v[196:199]\n"
    "ds_read_b64_tr_b16 a[0:1], v234 offset:0\n"
    "v_mfma_f32_16x16x32_bf16 v[196:199], a[24:27], a[180:183], v[196:199]\n"
    "s_add_u32 s61, s61, 1\n"
    "ds_read_b64_tr_b16 a[2:3], v234 offset:2048\n"
    "v_mfma_f32_16x16x32_bf16 v[196:199], a[28:31], a[188:191], v[196:199]\n"
    "ds_read_b64_tr_b16 a[4:5], v234 offset:4096\n"
    "v_mfma_f32_16x16x32_bf16 v[200:203], a[16:19], v[48:51], v[200:203]\n"
    "ds_read_b64_tr_b16 a[6:7], v234 offset:6144\n"
    "v_mfma_f32_16x16x32_bf16 v[200:203], a[20:23], v[56:59], v[200:203]\n"
    "ds_read_b64_tr_b16 a[8:9], v234 offset:8192\n"
    "v_mfma_f32_16x16x32_bf16 v[200:203], a[24:27], v[64:67], v[200:203]\n"
    "ds_read_b64_tr_b16 a[10:11], v234 offset:10240\n"
    "v_mfma_f32_16x16x32_bf16 v[200:203], a[28:31], v[72:75], v[200:203]\n"
    "ds_read_b64 v[252:253], v238 offset:4608\n"
    "ds_read_b64_tr_b16 a[12:13], v234 offset:12288\n"
    "v_mfma_f32_16x16x32_bf16 v[204:207], a[16:19], v[52:55], v[204:207]\n"
    "s_add_u32 m0, s59, 4224\n"
    "ds_read_b64 v[254:255], v238 offset:4736\n"
    "ds_read_b32 v229, v230\n"
    "ds_read_b64_tr_b16 a[14:15], v234 offset:14336\n"
    "v_mfma_f32_16x16x32_bf16 v[204:207], a[20:23], v[60:63], v[204:207]\n"
    "buffer_load_dwordx4 v230, s[44:47], s58 offen nt lds\n"
    "v_mfma_f32_16x16x32_bf16 v[204:207], a[24:27], v[68:71], v[204:207]\n"
    "s_add_u32 s58, s58, 1024\n"
    "v_mfma_f32_16x16x32_bf16 v[204:207], a[28:31], v[76:79], v[204:207]\n"
    "v_mfma_f32_16x16x32_bf16 v[208:211], a[16:19], a[168:171], v[208:211]\n"
    "v_mfma_f32_16x16x32_bf16 v[208:211], a[20:23], a[176:179], v[208:211]\n"
    "s_waitcnt lgkmcnt(1)\n"
    "v_cmp_gt_u32 vcc, s61, v229\n"
    "v_mfma_f32_16x16x32_bf16 v[208:211], a[24:27], a[184:187], v[208:211]\n"
    "s_waitcnt lgkmcnt(2)\n"
    "v_mfma_f32_16x16x32_bf16 v[208:211], a[28:31], a[192:195], v[208:211]\n"
    "s_waitcnt lgkmcnt(0)\n"
    "buffer_store_dwordx4 v[252:255], v232, s[48:51], s28 offen offset:0 sc1\n"
    "v_mfma_f32_16x16x32_bf16 v[148:151], a[0:3], v[80:83], v[148:151]\n"
    "s_cbranch_vccnz L_sp25%=\n"
    "L_rt25%=:\n"
    "ds_read_b64_tr_b16 a[16:17], v235 offset:0\n"
    "v_mfma_f32_16x16x32_bf16 v[148:151], a[4:7], v[92:95], v[148:151]\n"
    "ds_read_b64_tr_b16 a[18:19], v235 offset:2048\n"
    "v_mfma_f32_16x16x32_bf16 v[148:151], a[8:11], v[100:103], v[148:151]\n"
    "ds_read_b64_tr_b16 a[20:21], v235 offset:4096\n"
    "v_mfma_f32_16x16x32_bf16 v[148:151], a[12:15], v[108:111], v[148:151]\n"
    "ds_read_b64_tr_b16 a[22:23], v235 offset:6144\n"
    "v_mfma_f32_16x16x32_bf16 v[152:155], a[0:3], v[84:87], v[152:155]\n"
    "ds_read_b64_tr_b16 a[24:25], v235 offset:8192\n"
    "v_mfma_f32_16x16x32_bf16 v[152:155], a[4:7], a[200:203], v[152:155]\n"
    "ds_read_b64_tr_b16 a[26:27], v235 offset:10240\n"
    "v_mfma_f32_16x16x32_bf16 v[152:155], a[8:11], a[208:211], v[152:155]\n"
    "ds_read_b64_tr_b16 a[28:29], v235 offset:12288\n"
    "v_mfma_f32_16x16x32_bf16 v[152:155], a[12:15], a[216:219], v[152:155]\n"
    "ds_read_b64_tr_b16 a[30:31], v235 offset:14336\n"
    "v_mfma_f32_16x16x32_bf16 v[156:159], a[0:3], v[88:91], v[156:159]\n"
    "s_add_u32 m0, s96, 0\n"
    "ds_read_b64 v[252:253], v238 offset:4672\n"
    "v_mfma_f32_16x16x32_bf16 v[156:159], a[4:7], a[204:207], v[156:159]\n"
    "buffer_load_dwordx4 v231, s[40:43], s53 offen lds\n"
    "v_mfma_f32_16x16x32_bf16 v[156:159], a[8:11], a[212:215], v[156:159]\n"
    "s_add_u32 s53, s53, 0x4000\n"
    "ds_read_b64 v[254:255], v238 offset:4800\n"
    "v_mfma_f32_16x16x32_bf16 v[156:159], a[12:15], a[220:223], v[156:159]\n"
    "v_mfma_f32_16x16x32_bf16 v[160:163], a[0:3], a[196:199], v[160:163]\n"
    "s_add_u32 m0, s96, 4096\n"
    "v_mfma_f32_16x16x32_bf16 v[160:163], a[4:7], v[96:99], v[160:163]\n"
    "buffer_load_dwordx4 v231, s[40:43], s53 offen lds\n"
    "v_mfma_f32_16x16x32_bf16 v[160:163], a[8:11], v[104:107], v[160:163]\n"
    "s_add_u32 s53, s53, 0x4000\n"
    "v_mfma_f32_16x16x32_bf16 v[160:163], a[12:15], v[112:115], v[160:163]\n"
    "s_waitcnt lgkmcnt(2)\n"
    "v_mfma_f32_16x16x32_bf16 v[164:167], a[16:19], v[80:83], v[164:167]\n"
    "ds_read_b64_tr_b16 a[0:1], v234 offset:512\n"
    "v_mfma_f32_16x16x32_bf16 v[164:167], a[20:23], v[92:95], v[164:167]\n"
    "ds_read_b64_tr_b16 a[2:3], v234 offset:2560\n"
    "v_mfma_f32_16x16x32_bf16 v[164:167], a[24:27], v[100:103], v[164:167]\n"
    "ds_read_b64_tr_b16 a[4:5], v234 offset:4608\n"
    "v_mfma_f32_16x16x32_bf16 v[164:167], a[28:31], v[108:111], v[164:167]\n"
    "ds_read_b64_tr_b16 a[6:7], v234 offset:6656\n"
    "v_mfma_f32_16x16x32_bf16 v[168:171], a[16:19], v[84:87], v[168:171]\n"
    "ds_read_b64_tr_b16 a[8:9], v234 offset:8704\n"
    "v_mfma_f32_16x16x32_bf16 v[168:171], a[20:23], a[200:203], v[168:171]\n"
    "ds_read_b64_tr_b16 a[10:11], v234 offset:10752\n"
    "v_mfma_f32_16x16x32_bf16 v[168:171], a[24:27], a[208:211], v[168:171]\n"
    "ds_read_b64_tr_b16 a[12:13], v234 offset:12800\n"
    "v_mfma_f32_16x16x32_bf16 v[168:171], a[28:31], a[216:219], v[168:171]\n"
    "ds_read_b64_tr_b16 a[14:15], v234 offset:14848\n"
    "v_mfma_f32_16x16x32_bf16 v[172:175], a[16:19], v[88:91], v[172:175]\n"
    "s_waitcnt lgkmcnt(8)\n"
    "v_mfma_f32_16x16x32_bf16 v[172:175], a[20:23], a[204:207], v[172:175]\n"
    "buffer_store_dwordx4 v[252:255], v232, s[48:51], s29 offen offset:0 sc1\n"
    "v_mfma_f32_16x16x32_bf16 v[172:175], a[24:27], a[212:215], v[172:175]\n"
    "s_nop 0\n"
    "ds_read_b64 v[252:253], v238 offset:6912\n"
    "v_mfma_f32_16x16x32_bf16 v[172:175], a[28:31], a[220:223], v[172:175]\n"
    "ds_read_b64 v[254:255], v238 offset:7040\n"
    "v_mfma_f32_16x16x32_bf16 v[176:179], a[16:19], a[196:199], v[176:179]\n"
    "s_add_u32 m0, s96, 8192\n"
    "s_add_u32 s55, s54, s63\n"
    "v_mfma_f32_16x16x32_bf16 v[176:179], a[20:23], v[96:99], v[176:179]\n"
    "s_add_u32 s54, s54, 16896\n"
    "buffer_load_dwordx4 v231, s[40:43], s53 offen lds\n"
    "v_mfma_f32_16x16x32_bf16 v[176:179], a[24:27], v[104:107], v[176:179]\n"
    "s_add_u32 s53, s53, 0x4000\n"
    "s_sub_u32 s97, s54, 84480\n"
    "v_mfma_f32_16x16x32_bf16 v[176:179], a[28:31], v[112:115], v[176:179]\n"
    "s_waitcnt lgkmcnt(2)\n"
    "s_min_u32 s54, s54, s97\n"
    "v_mfma_f32_16x16x32_bf16 v[180:183], a[0:3], v[80:83], v[180:183]\n"
    "s_sub_u32 s97, s54, s67\n"
    "ds_read_b64_tr_b16 a[16:17], v235 offset:512\n"
    "v_mfma_f32_16x16x32_bf16 v[180:183], a[4:7], v[92:95], v[180:183]\n"
    "s_mov_b32 s67, s54\n"
    "ds_read_b64_tr_b16 a[18:19], v235 offset:2560\n"
    "v_mfma_f32_16x16x32_bf16 v[180:183], a[8:11], v[100:103], v[180:183]\n"
    "ds_read_b64_tr_b16 a[20:21], v235 offset:4608\n"
    "v_mfma_f32_16x16x32_bf16 v[180:183], a[12:15], v[108:111], v[180:183]\n"
    "ds_read_b64_tr_b16 a[22:23], v235 offset:6656\n"
    "v_mfma_f32_16x16x32_bf16 v[184:187], a[0:3], v[84:87], v[184:187]\n"
    "ds_read_b64_tr_b16 a[24:25], v235 offset:8704\n"
    "v_mfma_f32_16x16x32_bf16 v[184:187], a[4:7], a[200:203], v[184:187]\n"
    "ds_read_b64_tr_b16 a[26:27], v235 offset:10752\n"
    "v_mfma_f32_16x16x32_bf16 v[184:187], a[8:11], a[208:211], v[184:187]\n"
    "ds_read_b64_tr_b16 a[28:29], v235 offset:12800\n"
    "v_mfma_f32_16x16x32_bf16 v[184:187], a[12:15], a[216:219], v[184:187]\n"
    "ds_read_b64_tr_b16 a[30:31], v235 offset:14848\n"
    "v_add_u32 v234, s97, v234\n"
    "v_mfma_f32_16x16x32_bf16 v[188:191], a[0:3], v[88:91], v[188:191]\n"
    "s_add_u32 m0, s96, 12288\n"
    "v_xor_b32 v235, 32, v234\n"
    "v_mfma_f32_16x16x32_bf16 v[188:191], a[4:7], a[204:207], v[188:191]\n"
    "buffer_load_dwordx4 v231, s[40:43], s53 offen lds\n"
    "v_mfma_f32_16x16x32_bf16 v[188:191], a[8:11], a[212:215], v[188:191]\n"
    "s_add_u32 s53, s53, 0x4000\n"
    "v_mfma_f32_16x16x32_bf16 v[188:191], a[12:15], a[220:223], v[188:191]\n"
    "v_mfma_f32_16x16x32_bf16 v[192:195], a[0:3], a[196:199], v[192:195]\n"
    "s_waitcnt lgkmcnt(8)\n"
    "v_mfma_f32_16x16x32_bf16 v[192:195], a[4:7], v[96:99], v[192:195]\n"
    "buffer_store_dwordx4 v[252:255], v232, s[48:51], s30 offen offset:0 sc1\n"
    "v_mfma_f32_16x16x32_bf16 v[192:195], a[8:11], v[104:107], v[192:195]\n"
    "s_nop 0\n"
    "ds_read_b64 v[252:253], v238 offset:6976\n"
    "v_mfma_f32_16x16x32_bf16 v[192:195], a[12:15], v[112:115], v[192:195]\n"
    "s_waitcnt vmcnt(18) lgkmcnt(1)\n"
    "ds_inc_u32 v228, v236\n"
    "v_mfma_f32_16x16x32_bf16 v[196:199], a[16:19], v[80:83], v[196:199]\n"
    "ds_read_b64_tr_b16 a[0:1], v234 offset:0\n"
    "v_mfma_f32_16x16x32_bf16 v[196:199], a[20:23], v[92:95], v[196:199]\n"
    "ds_read_b64_tr_b16 a[2:3], v234 offset:2048\n"
    "v_mfma_f32_16x16x32_bf16 v[196:199], a[24:27], v[100:103], v[196:199]\n"
    "s_add_u32 s61, s61, 1\n"
    "ds_read_b64_tr_b16 a[4:5], v234 offset:4096\n"
    "v_mfma_f32_16x16x32_bf16 v[196:199], a[28:31], v[108:111], v[196:199]\n"
    "ds_read_b64_tr_b16 a[6:7], v234 offset:6144\n"
    "v_mfma_f32_16x16x32_bf16 v[200:203], a[16:19], v[84:87], v[200:203]\n"
    "ds_read_b64_tr_b16 a[8:9], v234 offset:8192\n"
    "v_mfma_f32_16x16x32_bf16 v[200:203], a[20:23], a[200:203], v[200:203]\n"
    "ds_read_b64_tr_b16 a[10:11], v234 offset:10240\n"
    "v_mfma_f32_16x16x32_bf16 v[200:203], a[24:27], a[208:211], v[200:203]\n"
    "ds_read_b64_tr_b16 a[12:13], v234 offset:12288\n"
    "v_mfma_f32_16x16x32_bf16 v[200:203], a[28:31], a[216:219], v[200:203]\n"
    "ds_read_b64 v[254:255], v238 offset:7104\n"
    "v_mfma_f32_16x16x32_bf16 v[204:207], a[16:19], v[88:91], v[204:207]\n"
    "s_add_u32 m0, s59, 5280\n"
    "ds_read_b32 v229, v230\n"
    "ds_read_b64_tr_b16 a[14:15], v234 offset:14336\n"
    "v_mfma_f32_16x16x32_bf16 v[204:207], a[20:23], a[204:207], v[204:207]\n"
    "buffer_load_dwordx4 v230, s[44:47], s58 offen nt lds\n"
    "v_mfma_f32_16x16x32_bf16 v[204:207], a[24:27], a[212:215], v[204:207]\n"
    "s_add_u32 s58, s58, 1024\n"
    "v_mfma_f32_16x16x32_bf16 v[204:207], a[28:31], a[220:223], v[204:207]\n"
    "v_mfma_f32_16x16x32_bf16 v[208:211], a[16:19], a[196:199], v[208:211]\n"
    "v_mfma_f32_16x16x32_bf16 v[208:211], a[20:23], v[96:99], v[208:211]\n"
    "s_waitcnt lgkmcnt(1)\n"
    "v_cmp_gt_u32 vcc, s61, v229\n"
    "v_mfma_f32_16x16x32_bf16 v[208:211], a[24:27], v[104:107], v[208:211]\n"
    "s_waitcnt lgkmcnt(2)\n"
    "v_mfma_f32_16x16x32_bf16 v[208:211], a[28:31], v[112:115], v[208:211]\n"
    "s_waitcnt lgkmcnt(0)\n"
    "buffer_store_dwordx4 v[252:255], v232, s[48:51], s31 offen offset:0 sc1\n"
    "v_mfma_f32_16x16x32_bf16 v[148:151], a[0:3], v[116:119], v[148:151]\n"
    "s_cbranch_vccnz L_sp26%=\n"
    "L_rt26%=:\n"
    "ds_read_b64_tr_b16 a[16:17], v235 offset:0\n"
    "v_mfma_f32_16x16x32_bf16 v[148:151], a[4:7], v[124:127], v[148:151]\n"
    "ds_read_b64_tr_b16 a[18:19], v235 offset:2048\n"
    "v_xor_b32 v232, 384, v232\n"
    "v_mfma_f32_16x16x32_bf16 v[148:151], a[8:11], v[132:135], v[148:151]\n"
    "ds_read_b64_tr_b16 a[20:21], v235 offset:4096\n"
    "v_mfma_f32_16x16x32_bf16 v[148:151], a[12:15], v[140:143], v[148:151]\n"
    "ds_read_b64_tr_b16 a[22:23], v235 offset:6144\n"
    "v_mfma_f32_16x16x32_bf16 v[152:155], a[0:3], a[224:227], v[152:155]\n"
    "ds_read_b64_tr_b16 a[24:25], v235 offset:8192\n"
    "v_mfma_f32_16x16x32_bf16 v[152:155], a[4:7], a[232:235], v[152:155]\n"
    "ds_read_b64_tr_b16 a[26:27], v235 offset:10240\n"
    "v_mfma_f32_16x16x32_bf16 v[152:155], a[8:11], a[240:243], v[152:155]\n"
    "ds_read_b64_tr_b16 a[28:29], v235 offset:12288\n"
    "v_mfma_f32_16x16x32_bf16 v[152:155], a[12:15], a[248:251], v[152:155]\n"
    "ds_read_b64_tr_b16 a[30:31], v235 offset:14336\n"
    "v_mul_f32 v148, v148, v244\n"
    "v_mfma_f32_16x16x32_bf16 v[156:159], a[0:3], a[228:231], v[156:159]\n"
    "s_add_u32 m0, s55, 0\n"
    "v_mul_f32 v149, v149, v244\n"
    "v_mul_f32 v150, v150, v244\n"
    "v_mfma_f32_16x16x32_bf16 v[156:159], a[4:7], a[236:239], v[156:159]\n"
    "buffer_load_dwordx4 v231, s[40:43], s53 offen lds\n"
    "v_mfma_f32_16x16x32_bf16 v[156:159], a[8:11], a[244:247], v[156:159]\n"
    "s_add_u32 s53, s53, 0x4000\n"
    "v_mul_f32 v151, v151, v244\n"
    "v_cvt_pk_bf16_f32 v148, v148, v149\n"
    "v_mfma_f32_16x16x32_bf16 v[156:159], a[12:15], a[252:255], v[156:159]\n"
    "v_cvt_pk_bf16_f32 v149, v150, v151\n"
    "v_mul_f32 v152, v152, v245\n"
    "v_mfma_f32_16x16x32_bf16 v[160:163], a[0:3], v[120:123], v[160:163]\n"
    "s_add_u32 m0, s55, 4096\n"
    "ds_write_b64 v237, v[148:149] offset:0\n"
    "v_mfma_f32_16x16x32_bf16 v[160:163], a[4:7], v[128:131], v[160:163]\n"
    "buffer_load_dwordx4 v231, s[40:43], s53 offen lds\n"
    "v_mfma_f32_16x16x32_bf16 v[160:163], a[8:11], v[136:139], v[160:163]\n"
    "s_add_u32 s53, s53, 0x4000\n"
    "v_mul_f32 v153, v153, v245\n"
    "v_mul_f32 v154, v154, v245\n"
    "v_mfma_f32_16x16x32_bf16 v[160:163], a[12:15], v[144:147], v[160:163]\n"
    "s_waitcnt lgkmcnt(1)\n"
    "v_mul_f32 v155, v155, v245\n"
    "v_cvt_pk_bf16_f32 v152, v152, v153\n"
    "v_mfma_f32_16x16x32_bf16 v[164:167], a[16:19], v[116:119], v[164:167]\n"
    "ds_read_b64_tr_b16 a[0:1], v234 offset:512\n"
    "v_cvt_pk_bf16_f32 v153, v154, v155\n"
    "v_mfma_f32_16x16x32_bf16 v[164:167], a[20:23], v[124:127], v[164:167]\n"
    "ds_read_b64_tr_b16 a[2:3], v234 offset:2560\n"
    "v_mul_f32 v156, v156, v246\n"
    "v_mfma_f32_16x16x32_bf16 v[164:167], a[24:27], v[132:135], v[164:167]\n"
    "ds_read_b64_tr_b16 a[4:5], v234 offset:4608\n"
    "v_mul_f32 v157, v157, v246\n"
    "v_mfma_f32_16x16x32_bf16 v[164:167], a[28:31], v[140:143], v[164:167]\n"
    "ds_read_b64_tr_b16 a[6:7], v234 offset:6656\n"
    "v_mul_f32 v158, v158, v246\n"
    "v_mfma_f32_16x16x32_bf16 v[168:171], a[16:19], a[224:227], v[168:171]\n"
    "ds_read_b64_tr_b16 a[8:9], v234 offset:8704\n"
    "v_mul_f32 v159, v159, v246\n"
    "v_mfma_f32_16x16x32_bf16 v[168:171], a[20:23], a[232:235], v[168:171]\n"
    "ds_read_b64_tr_b16 a[10:11], v234 offset:10752\n"
    "v_cvt_pk_bf16_f32 v156, v156, v157\n"
    "v_mfma_f32_16x16x32_bf16 v[168:171], a[24:27], a[240:243], v[168:171]\n"
    "ds_read_b64_tr_b16 a[12:13], v234 offset:12800\n"
    "v_cvt_pk_bf16_f32 v157, v158, v159\n"
    "v_mfma_f32_16x16x32_bf16 v[168:171], a[28:31], a[248:251], v[168:171]\n"
    "ds_read_b64_tr_b16 a[14:15], v234 offset:14848\n"
    "v_mul_f32 v160, v160, v247\n"
    "v_mfma_f32_16x16x32_bf16 v[172:175], a[16:19], a[228:231], v[172:175]\n"
    "ds_write_b64 v237, v[152:153] offset:2304\n"
    "v_mfma_f32_16x16x32_bf16 v[172:175], a[20:23], a[236:239], v[172:175]\n"
    "ds_write_b64 v237, v[156:157] offset:4608\n"
    "v_mfma_f32_16x16x32_bf16 v[172:175], a[24:27], a[244:247], v[172:175]\n"
    "v_mul_f32 v161, v161, v247\n"
    "v_mul_f32 v162, v162, v247\n"
    "v_mfma_f32_16x16x32_bf16 v[172:175], a[28:31], a[252:255], v[172:175]\n"
    "s_add_u32 m0, s59, 6336\n"
    "v_mul_f32 v163, v163, v247\n"
    "v_cvt_pk_bf16_f32 v160, v160, v161\n"
    "v_mfma_f32_16x16x32_bf16 v[176:179], a[16:19], v[120:123], v[176:179]\n"
    "s_add_u32 s96, s54, s62\n"
    "buffer_load_dwordx4 v230, s[44:47], s58 offen nt lds\n"
    "v_mfma_f32_16x16x32_bf16 v[176:179], a[20:23], v[128:131], v[176:179]\n"
    "s_add_u32 s58, s58, 1024\n"
    "v_cvt_pk_bf16_f32 v161, v162, v163\n"
    "v_mul_f32 v164, v164, v244\n"
    "v_mfma_f32_16x16x32_bf16 v[176:179], a[24:27], v[136:139], v[176:179]\n"
    "s_add_u32 m0, s55, 8192\n"
    "ds_write_b64 v237, v[160:161] offset:6912\n"
    "v_mfma_f32_16x16x32_bf16 v[176:179], a[28:31], v[144:147], v[176:179]\n"
    "s_waitcnt lgkmcnt(3)\n"
    "buffer_load_dwordx4 v231, s[40:43], s53 offen lds\n"
    "v_mfma_f32_16x16x32_bf16 v[180:183], a[0:3], v[116:119], v[180:183]\n"
    "s_add_u32 s53, s53, 0x4000\n"
    "ds_read_b64_tr_b16 a[16:17], v235 offset:512\n"
    "v_mul_f32 v165, v165, v244\n"
    "v_mfma_f32_16x16x32_bf16 v[180:183], a[4:7], v[124:127], v[180:183]\n"
    "s_add_u32 s54, s54, 16896\n"
    "ds_read_b64_tr_b16 a[18:19], v235 offset:2560\n"
    "v_mul_f32 v166, v166, v244\n"
    "v_mfma_f32_16x16x32_bf16 v[180:183], a[8:11], v[132:135], v[180:183]\n"
    "s_sub_u32 s97, s54, 84480\n"
    "ds_read_b64_tr_b16 a[20:21], v235 offset:4608\n"
    "v_mul_f32 v167, v167, v244\n"
    "v_mfma_f32_16x16x32_bf16 v[180:183], a[12:15], v[140:143], v[180:183]\n"
    "s_min_u32 s54, s54, s97\n"
    "ds_read_b64_tr_b16 a[22:23], v235 offset:6656\n"
    "v_cvt_pk_bf16_f32 v164, v164, v165\n"
    "v_mfma_f32_16x16x32_bf16 v[184:187], a[0:3], a[224:227], v[184:187]\n"
    "s_sub_u32 s97, s54, s67\n"
    "ds_read_b64_tr_b16 a[24:25], v235 offset:8704\n"
    "v_cvt_pk_bf16_f32 v165, v166, v167\n"
    "v_mfma_f32_16x16x32_bf16 v[184:187], a[4:7], a[232:235], v[184:187]\n"
    "s_mov_b32 s67, s54\n"
    "ds_read_b64_tr_b16 a[26:27], v235 offset:10752\n"
    "v_mul_f32 v168, v168, v245\n"
    "v_mfma_f32_16x16x32_bf16 v[184:187], a[8:11], a[240:243], v[184:187]\n"
    "ds_read_b64_tr_b16 a[28:29], v235 offset:12800\n"
    "v_mul_f32 v169, v169, v245\n"
    "v_mfma_f32_16x16x32_bf16 v[184:187], a[12:15], a[248:251], v[184:187]\n"
    "ds_read_b64_tr_b16 a[30:31], v235 offset:14848\n"
    "v_add_u32 v234, s97, v234\n"
    "v_mfma_f32_16x16x32_bf16 v[188:191], a[0:3], a[228:231], v[188:191]\n"
    "v_xor_b32 v235, 32, v234\n"
    "v_mul_f32 v170, v170, v245\n"
    "v_mfma_f32_16x16x32_bf16 v[188:191], a[4:7], a[236:239], v[188:191]\n"
    "ds_write_b64 v237, v[164:165] offset:576\n"
    "v_mfma_f32_16x16x32_bf16 v[188:191], a[8:11], a[244:247], v[188:191]\n"
    "v_mul_f32 v171, v171, v245\n"
    "v_cvt_pk_bf16_f32 v168, v168, v169\n"
    "v_mfma_f32_16x16x32_bf16 v[188:191], a[12:15], a[252:255], v[188:191]\n"
    "s_add_u32 m0, s55, 12288\n"
    "v_cvt_pk_bf16_f32 v169, v170, v171\n"
    "v_mul_f32 v172, v172, v246\n"
    "v_mfma_f32_16x16x32_bf16 v[192:195], a[0:3], v[120:123], v[192:195]\n"
    "buffer_load_dwordx4 v231, s[40:43], s53 offen lds\n"
    "v_mfma_f32_16x16x32_bf16 v[192:195], a[4:7], v[128:131], v[192:195]\n"
    "ds_write_b64 v237, v[168:169] offset:2880\n"
    "v_mfma_f32_16x16x32_bf16 v[192:195], a[8:11], v[136:139], v[192:195]\n"
    "v_mul_f32 v173, v173, v246\n"
    "v_mul_f32 v174, v174, v246\n"
    "v_mfma_f32_16x16x32_bf16 v[192:195], a[12:15], v[144:147], v[192:195]\n"
    "s_waitcnt vmcnt(16) lgkmcnt(2)\n"
    "s_add_u32 s53, s53, 0xfffc4000\n"
    "s_xor_b32 s53, s53, 384\n"
    "v_mfma_f32_16x16x32_bf16 v[196:199], a[16:19], v[116:119], v[196:199]\n"
    "ds_inc_u32 v228, v236\n"
    "v_mfma_f32_16x16x32_bf16 v[196:199], a[20:23], v[124:127], v[196:199]\n"
    "ds_read_b64_tr_b16 a[0:1], v234 offset:0\n"
    "v_mul_f32 v175, v175, v246\n"
    "v_mfma_f32_16x16x32_bf16 v[196:199], a[24:27], v[132:135], v[196:199]\n"
    "s_add_u32 s61, s61, 1\n"
    "ds_read_b64_tr_b16 a[2:3], v234 offset:2048\n"
    "v_cvt_pk_bf16_f32 v172, v172, v173\n"
    "v_mfma_f32_16x16x32_bf16 v[196:199], a[28:31], v[140:143], v[196:199]\n"
    "ds_read_b64_tr_b16 a[4:5], v234 offset:4096\n"
    "v_cvt_pk_bf16_f32 v173, v174, v175\n"
    "v_mfma_f32_16x16x32_bf16 v[200:203], a[16:19], a[224:227], v[200:203]\n"
    "ds_read_b64_tr_b16 a[6:7], v234 offset:6144\n"
    "v_mul_f32 v176, v176, v247\n"
    "v_mfma_f32_16x16x32_bf16 v[200:203], a[20:23], a[232:235], v[200:203]\n"
    "ds_read_b64_tr_b16 a[8:9], v234 offset:8192\n"
    "v_mul_f32 v177, v177, v247\n"
    "v_mfma_f32_16x16x32_bf16 v[200:203], a[24:27], a[240:243], v[200:203]\n"
    "ds_read_b64_tr_b16 a[10:11], v234 offset:10240\n"
    "v_mul_f32 v178, v178, v247\n"
    "v_mfma_f32_16x16x32_bf16 v[200:203], a[28:31], a[248:251], v[200:203]\n"
    "ds_read_b64_tr_b16 a[12:13], v234 offset:12288\n"
    "v_mul_f32 v179, v179, v247\n"
    "v_mfma_f32_16x16x32_bf16 v[204:207], a[16:19], a[228:231], v[204:207]\n"
    "ds_read_b64_tr_b16 a[14:15], v234 offset:14336\n"
    "v_cvt_pk_bf16_f32 v176, v176, v177\n"
    "v_mfma_f32_16x16x32_bf16 v[204:207], a[20:23], a[236:239], v[204:207]\n"
    "ds_read_b32 v229, v230\n"
    "v_cvt_pk_bf16_f32 v177, v178, v179\n"
    "v_mfma_f32_16x16x32_bf16 v[204:207], a[24:27], a[244:247], v[204:207]\n"
    "ds_write_b64 v237, v[172:173] offset:5184\n"
    "v_mfma_f32_16x16x32_bf16 v[204:207], a[28:31], a[252:255], v[204:207]\n"
    "s_add_u32 m0, s59, 7392\n"
    "ds_write_b64 v237, v[176:177] offset:7488\n"
    "v_mfma_f32_16x16x32_bf16 v[208:211], a[16:19], v[120:123], v[208:211]\n"
    "buffer_load_dwordx4 v230, s[44:47], s58 offen nt lds\n"
    "v_mfma_f32_16x16x32_bf16 v[208:211], a[20:23], v[128:131], v[208:211]\n"
    "s_waitcnt lgkmcnt(2)\n"
    "v_cmp_gt_u32 vcc, s61, v229\n"
    "v_mul_f32 v180, v180, v244\n"
    "v_mfma_f32_16x16x32_bf16 v[208:211], a[24:27], v[136:139], v[208:211]\n"
    "s_add_u32 s58, s58, 1024\n"
    "v_mul_f32 v181, v181, v244\n"
    "v_mul_f32 v182, v182, v244\n"
    "v_mfma_f32_16x16x32_bf16 v[208:211], a[28:31], v[144:147], v[208:211]\n"
    "s_waitcnt lgkmcnt(3)\n"
    "v_mul_f32 v183, v183, v244\n"
    "v_cvt_pk_bf16_f32 v180, v180, v181\n"
    "v_mfma_f32_16x16x32_bf16 v[148:151], a[0:3], v[0:3], 0\n"
    "s_cbranch_vccnz L_sp27%=\n"
    "L_rt27%=:\n"
    "ds_read_b64_tr_b16 a[16:17], v235 offset:0\n"
    "v_mfma_f32_16x16x32_bf16 v[148:151], a[4:7], v[16:19], v[148:151]\n"
    "ds_read_b64_tr_b16 a[18:19], v235 offset:2048\n"
    "v_cvt_pk_bf16_f32 v181, v182, v183\n"
    "v_mfma_f32_16x16x32_bf16 v[148:151], a[8:11], a[148:151], v[148:151]\n"
    "ds_read_b64_tr_b16 a[20:21], v235 offset:4096\n"
    "v_mul_f32 v184, v184, v245\n"
    "v_mfma_f32_16x16x32_bf16 v[148:151], a[12:15], a[156:159], v[148:151]\n"
    "ds_read_b64_tr_b16 a[22:23], v235 offset:6144\n"
    "v_mul_f32 v185, v185, v245\n"
    "v_mfma_f32_16x16x32_bf16 v[152:155], a[0:3], v[4:7], 0\n"
    "ds_read_b64_tr_b16 a[24:25], v235 offset:8192\n"
    "v_mul_f32 v186, v186, v245\n"
    "v_mfma_f32_16x16x32_bf16 v[152:155], a[4:7], v[20:23], v[152:155]\n"
    "ds_read_b64_tr_b16 a[26:27], v235 offset:10240\n"
    "v_mul_f32 v187, v187, v245\n"
    "v_mfma_f32_16x16x32_bf16 v[152:155], a[8:11], v[32:35], v[152:155]\n"
    "ds_read_b64_tr_b16 a[28:29], v235 offset:12288\n"
    "v_cvt_pk_bf16_f32 v184, v184, v185\n"
    "v_mfma_f32_16x16x32_bf16 v[152:155], a[12:15], v[40:43], v[152:155]\n"
    "ds_read_b64_tr_b16 a[30:31], v235 offset:14336\n"
    "v_cvt_pk_bf16_f32 v185, v186, v187\n"
    "v_mfma_f32_16x16x32_bf16 v[156:159], a[0:3], v[8:11], 0\n"
    "ds_write_b64 v237, v[180:181] offset:1152\n"
    "v_mfma_f32_16x16x32_bf16 v[156:159], a[4:7], v[24:27], v[156:159]\n"
    "ds_write_b64 v237, v[184:185] offset:3456\n"
    "v_mfma_f32_16x16x32_bf16 v[156:159], a[8:11], v[36:39], v[156:159]\n"
    "v_mul_f32 v196, v196, v244\n"
    "v_mul_f32 v197, v197, v244\n"
    "v_mfma_f32_16x16x32_bf16 v[156:159], a[12:15], v[44:47], v[156:159]\n"
    "s_add_u32 m0, s96, 0\n"
    "v_mul_f32 v198, v198, v244\n"
    "v_mul_f32 v199, v199, v244\n"
    "v_mfma_f32_16x16x32_bf16 v[160:163], a[0:3], v[12:15], 0\n"
    "buffer_load_dwordx4 v230, s[36:39], s52 offen lds\n"
    "v_mfma_f32_16x16x32_bf16 v[160:163], a[4:7], v[28:31], v[160:163]\n"
    "s_add_u32 s52, s52, 4096\n"
    "v_cvt_pk_bf16_f32 v196, v196, v197\n"
    "v_cvt_pk_bf16_f32 v197, v198, v199\n"
    "v_mfma_f32_16x16x32_bf16 v[160:163], a[8:11], a[152:155], v[160:163]\n"
    "ds_write_b64 v237, v[196:197] offset:1728\n"
    "v_mfma_f32_16x16x32_bf16 v[160:163], a[12:15], a[160:163], v[160:163]\n"
    "s_waitcnt lgkmcnt(3)\n"
    "v_mul_f32 v188, v188, v246\n"
    "v_mul_f32 v189, v189, v246\n"
    "v_mfma_f32_16x16x32_bf16 v[164:167], a[16:19], v[0:3], 0\n"
    "ds_read_b64_tr_b16 a[0:1], v234 offset:512\n"
    "v_mul_f32 v190, v190, v246\n"
    "v_mfma_f32_16x16x32_bf16 v[164:167], a[20:23], v[16:19], v[164:167]\n"
    "ds_read_b64_tr_b16 a[2:3], v234 offset:2560\n"
    "v_mul_f32 v191, v191, v246\n"
    "v_mfma_f32_16x16x32_bf16 v[164:167], a[24:27], a[148:151], v[164:167]\n"
    "ds_read_b64_tr_b16 a[4:5], v234 offset:4608\n"
    "v_cvt_pk_bf16_f32 v188, v188, v189\n"
    "v_mfma_f32_16x16x32_bf16 v[164:167], a[28:31], a[156:159], v[164:167]\n"
    "ds_read_b64_tr_b16 a[6:7], v234 offset:6656\n"
    "v_cvt_pk_bf16_f32 v189, v190, v191\n"
    "v_mfma_f32_16x16x32_bf16 v[168:171], a[16:19], v[4:7], 0\n"
    "ds_read_b64_tr_b16 a[8:9], v234 offset:8704\n"
    "v_mul_f32 v192, v192, v247\n"
    "v_mfma_f32_16x16x32_bf16 v[168:171], a[20:23], v[20:23], v[168:171]\n"
    "ds_read_b64_tr_b16 a[10:11], v234 offset:10752\n"
    "v_mul_f32 v193, v193, v247\n"
    "v_mfma_f32_16x16x32_bf16 v[168:171], a[24:27], v[32:35], v[168:171]\n"
    "ds_read_b64_tr_b16 a[12:13], v234 offset:12800\n"
    "v_mul_f32 v194, v194, v247\n"
    "v_mfma_f32_16x16x32_bf16 v[168:171], a[28:31], v[40:43], v[168:171]\n"
    "ds_read_b64_tr_b16 a[14:15], v234 offset:14848\n"
    "v_mul_f32 v195, v195, v247\n"
    "v_mfma_f32_16x16x32_bf16 v[172:175], a[16:19], v[8:11], 0\n"
    "s_add_u32 m0, s96, 4224\n"
    "ds_read_b64 v[252:253], v238 offset:0\n"
    "v_cvt_pk_bf16_f32 v192, v192, v193\n"
    "v_mfma_f32_16x16x32_bf16 v[172:175], a[20:23], v[24:27], v[172:175]\n"
    "buffer_load_dwordx4 v230, s[36:39], s52 offen lds\n"
    "v_mfma_f32_16x16x32_bf16 v[172:175], a[24:27], v[36:39], v[172:175]\n"
    "s_add_u32 s52, s52, 4096\n"
    "ds_write_b64 v237, v[188:189] offset:5760\n"
    "v_mfma_f32_16x16x32_bf16 v[172:175], a[28:31], v[44:47], v[172:175]\n"
    "ds_read_b64 v[254:255], v238 offset:128\n"
    "v_cvt_pk_bf16_f32 v193, v194, v195\n"
    "v_mfma_f32_16x16x32_bf16 v[176:179], a[16:19], v[12:15], 0\n"
    "s_add_u32 m0, s96, 8448\n"
    "ds_write_b64 v237, v[192:193] offset:8064\n"
    "v_mfma_f32_16x16x32_bf16 v[176:179], a[20:23], v[28:31], v[176:179]\n"
    "s_add_u32 s55, s54, s62\n"
    "buffer_load_dwordx4 v230, s[36:39], s52 offen lds\n"
    "v_mfma_f32_16x16x32_bf16 v[176:179], a[24:27], a[152:155], v[176:179]\n"
    "s_add_u32 s52, s52, 4096\n"
    "v_mul_f32 v200, v200, v245\n"
    "v_mul_f32 v201, v201, v245\n"
    "v_mfma_f32_16x16x32_bf16 v[176:179], a[28:31], a[160:163], v[176:179]\n"
    "s_waitcnt lgkmcnt(4)\n"
    "v_mul_f32 v202, v202, v245\n"
    "v_mul_f32 v203, v203, v245\n"
    "v_mfma_f32_16x16x32_bf16 v[180:183], a[0:3], v[0:3], 0\n"
    "s_add_u32 s54, s54, 16896\n"
    "ds_read_b64_tr_b16 a[16:17], v235 offset:512\n"
    "v_cvt_pk_bf16_f32 v200, v200, v201\n"
    "v_mfma_f32_16x16x32_bf16 v[180:183], a[4:7], v[16:19], v[180:183]\n"
    "s_sub_u32 s97, s54, 84480\n"
    "ds_read_b64_tr_b16 a[18:19], v235 offset:2560\n"
    "v_cvt_pk_bf16_f32 v201, v202, v203\n"
    "v_mfma_f32_16x16x32_bf16 v[180:183], a[8:11], a[148:151], v[180:183]\n"
    "s_min_u32 s54, s54, s97\n"
    "ds_read_b64_tr_b16 a[20:21], v235 offset:4608\n"
    "v_mul_f32 v204, v204, v246\n"
    "v_mfma_f32_16x16x32_bf16 v[180:183], a[12:15], a[156:159], v[180:183]\n"
    "s_sub_u32 s97, s54, s67\n"
    "ds_read_b64_tr_b16 a[22:23], v235 offset:6656\n"
    "v_mul_f32 v205, v205, v246\n"
    "v_mfma_f32_16x16x32_bf16 v[184:187], a[0:3], v[4:7], 0\n"
    "s_mov_b32 s67, s54\n"
    "ds_read_b64_tr_b16 a[24:25], v235 offset:8704\n"
    "v_mul_f32 v206, v206, v246\n"
    "v_mfma_f32_16x16x32_bf16 v[184:187], a[4:7], v[20:23], v[184:187]\n"
    "ds_read_b64_tr_b16 a[26:27], v235 offset:10752\n"
    "v_mul_f32 v207, v207, v246\n"
    "v_mfma_f32_16x16x32_bf16 v[184:187], a[8:11], v[32:35], v[184:187]\n"
    "ds_read_b64_tr_b16 a[28:29], v235 offset:12800\n"
    "v_cvt_pk_bf16_f32 v204, v204, v205\n"
    "v_mfma_f32_16x16x32_bf16 v[184:187], a[12:15], v[40:43], v[184:187]\n"
    "ds_read_b64_tr_b16 a[30:31], v235 offset:14848\n"
    "v_add_u32 v234, s97, v234\n"
    "v_mfma_f32_16x16x32_bf16 v[188:191], a[0:3], v[8:11], 0\n"
    "s_add_u32 m0, s96, 12672\n"
    "v_xor_b32 v235, 32, v234\n"
    "v_cvt_pk_bf16_f32 v205, v206, v207\n"
    "v_mfma_f32_16x16x32_bf16 v[188:191], a[4:7], v[24:27], v[188:191]\n"
    "buffer_load_dwordx4 v230, s[36:39], s52 offen lds\n"
    "v_mfma_f32_16x16x32_bf16 v[188:191], a[8:11], v[36:39], v[188:191]\n"
    "s_add_u32 s52, s52, 4096\n"
    "ds_write_b64 v237, v[200:201] offset:4032\n"
    "v_mfma_f32_16x16x32_bf16 v[188:191], a[12:15], v[44:47], v[188:191]\n"
    "s_waitcnt lgkmcnt(10)\n"
    "ds_write_b64 v237, v[204:205] offset:6336\n"
    "v_mfma_f32_16x16x32_bf16 v[192:195], a[0:3], v[12:15], 0\n"
    "buffer_store_dwordx4 v[252:255], v232, s[48:51], s24 offen offset:0 sc1\n"
    "v_mfma_f32_16x16x32_bf16 v[192:195], a[4:7], v[28:31], v[192:195]\n"
    "s_nop 0\n"
    "ds_read_b64 v[252:253], v238 offset:64\n"
    "v_mul_f32 v208, v208, v247\n"
    "v_mfma_f32_16x16x32_bf16 v[192:195], a[8:11], a[152:155], v[192:195]\n"
    "ds_read_b64 v[254:255], v238 offset:192\n"
    "v_mul_f32 v209, v209, v247\n"
    "v_mfma_f32_16x16x32_bf16 v[192:195], a[12:15], a[160:163], v[192:195]\n"
    "s_waitcnt vmcnt(14) lgkmcnt(4)\n"
    "ds_inc_u32 v228, v236\n"
    "v_mfma_f32_16x16x32_bf16 v[196:199], a[16:19], v[0:3], 0\n"
    "ds_read_b64_tr_b16 a[0:1], v234 offset:0\n"
    "v_mul_f32 v210, v210, v247\n"
    "v_mfma_f32_16x16x32_bf16 v[196:199], a[20:23], v[16:19], v[196:199]\n"
    "ds_read_b64_tr_b16 a[2:3], v234 offset:2048\n"
    "v_mul_f32 v211, v211, v247\n"
    "v_mfma_f32_16x16x32_bf16 v[196:199], a[24:27], a[148:151], v[196:199]\n"
    "s_add_u32 s61, s61, 1\n"
    "ds_read_b64_tr_b16 a[4:5], v234 offset:4096\n"
    "v_cvt_pk_bf16_f32 v208, v208, v209\n"
    "v_mfma_f32_16x16x32_bf16 v[196:199], a[28:31], a[156:159], v[196:199]\n"
    "ds_read_b64_tr_b16 a[6:7], v234 offset:6144\n"
    "v_cvt_pk_bf16_f32 v209, v210, v211\n"
    "v_mfma_f32_16x16x32_bf16 v[200:203], a[16:19], v[4:7], 0\n"
    "ds_read_b64_tr_b16 a[8:9], v234 offset:8192\n"
    "v_mfma_f32_16x16x32_bf16 v[200:203], a[20:23], v[20:23], v[200:203]\n"
    "ds_read_b64_tr_b16 a[10:11], v234 offset:10240\n"
    "v_mfma_f32_16x16x32_bf16 v[200:203], a[24:27], v[32:35], v[200:203]\n"
    "ds_read_b64_tr_b16 a[12:13], v234 offset:12288\n"
    "v_mfma_f32_16x16x32_bf16 v[200:203], a[28:31], v[40:43], v[200:203]\n"
    "ds_read_b32 v229, v230\n"
    "v_mfma_f32_16x16x32_bf16 v[204:207], a[16:19], v[8:11], 0\n"
    "s_waitcnt lgkmcnt(9)\n"
    "ds_read_b64_tr_b16 a[14:15], v234 offset:14336\n"
    "v_mfma_f32_16x16x32_bf16 v[204:207], a[20:23], v[24:27], v[204:207]\n"
    "buffer_store_dwordx4 v[252:255], v232, s[48:51], s25 offen offset:0 sc1\n"
    "v_mfma_f32_16x16x32_bf16 v[204:207], a[24:27], v[36:39], v[204:207]\n"
    "ds_write_b64 v237, v[208:209] offset:8640\n"
    "v_mfma_f32_16x16x32_bf16 v[204:207], a[28:31], v[44:47], v[204:207]\n"
    "ds_read_b64 v[252:253], v238 offset:2304\n"
    "v_mfma_f32_16x16x32_bf16 v[208:211], a[16:19], v[12:15], 0\n"
    "ds_read_b64 v[254:255], v238 offset:2432\n"
    "v_mfma_f32_16x16x32_bf16 v[208:211], a[20:23], v[28:31], v[208:211]\n"
    "s_waitcnt lgkmcnt(4)\n"
    "v_cmp_gt_u32 vcc, s61, v229\n"
    "v_mfma_f32_16x16x32_bf16 v[208:211], a[24:27], a[152:155], v[208:211]\n"
    "v_mfma_f32_16x16x32_bf16 v[208:211], a[28:31], a[160:163], v[208:211]\n"
    "s_waitcnt lgkmcnt(3)\n"
    "v_mfma_f32_16x16x32_bf16 v[148:151], a[0:3], a[164:167], v[148:151]\n"
    "s_cbranch_vccnz L_sp28%=\n"
    "L_rt28%=:\n"
    "ds_read_b64_tr_b16 a[16:17], v235 offset:0\n"
    "v_mfma_f32_16x16x32_bf16 v[148:151], a[4:7], a[172:175], v[148:151]\n"
    "ds_read_b64_tr_b16 a[18:19], v235 offset:2048\n"
    "v_mfma_f32_16x16x32_bf16 v[148:151], a[8:11], a[180:183], v[148:151]\n"
    "ds_read_b64_tr_b16 a[20:21], v235 offset:4096\n"
    "v_mfma_f32_16x16x32_bf16 v[148:151], a[12:15], a[188:191], v[148:151]\n"
    "ds_read_b64_tr_b16 a[22:23], v235 offset:6144\n"
    "v_mfma_f32_16x16x32_bf16 v[152:155], a[0:3], v[48:51], v[152:155]\n"
    "ds_read_b64_tr_b16 a[24:25], v235 offset:8192\n"
    "v_mfma_f32_16x16x32_bf16 v[152:155], a[4:7], v[56:59], v[152:155]\n"
    "ds_read_b64_tr_b16 a[26:27], v235 offset:10240\n"
    "v_mfma_f32_16x16x32_bf16 v[152:155], a[8:11], v[64:67], v[152:155]\n"
    "ds_read_b64_tr_b16 a[28:29], v235 offset:12288\n"
    "v_mfma_f32_16x16x32_bf16 v[152:155], a[12:15], v[72:75], v[152:155]\n"
    "ds_read_b64_tr_b16 a[30:31], v235 offset:14336\n"
    "v_mfma_f32_16x16x32_bf16 v[156:159], a[0:3], v[52:55], v[156:159]\n"
    "s_add_u32 m0, s55, 0\n"
    "v_mfma_f32_16x16x32_bf16 v[156:159], a[4:7], v[60:63], v[156:159]\n"
    "buffer_load_dwordx4 v230, s[36:39], s52 offen lds\n"
    "v_mfma_f32_16x16x32_bf16 v[156:159], a[8:11], v[68:71], v[156:159]\n"
    "s_add_u32 s52, s52, 4096\n"
    "v_mfma_f32_16x16x32_bf16 v[156:159], a[12:15], v[76:79], v[156:159]\n"
    "v_mfma_f32_16x16x32_bf16 v[160:163], a[0:3], a[168:171], v[160:163]\n"
    "s_waitcnt lgkmcnt(8)\n"
    "v_mfma_f32_16x16x32_bf16 v[160:163], a[4:7], a[176:179], v[160:163]\n"
    "buffer_store_dwordx4 v[252:255], v232, s[48:51], s26 offen offset:0 sc1\n"
    "v_mfma_f32_16x16x32_bf16 v[160:163], a[8:11], a[184:187], v[160:163]\n"
    "s_nop 0\n"
    "ds_read_b64 v[252:253], v238 offset:2368\n"
    "v_mfma_f32_16x16x32_bf16 v[160:163], a[12:15], a[192:195], v[160:163]\n"
    "s_waitcnt lgkmcnt(1)\n"
    "ds_read_b64 v[254:255], v238 offset:2496\n"
    "v_mfma_f32_16x16x32_bf16 v[164:167], a[16:19], a[164:167], v[164:167]\n"
    "ds_read_b64_tr_b16 a[0:1], v234 offset:512\n"
    "v_mfma_f32_16x16x32_bf16 v[164:167], a[20:23], a[172:175], v[164:167]\n"
    "ds_read_b64_tr_b16 a[2:3], v234 offset:2560\n"
    "v_mfma_f32_16x16x32_bf16 v[164:167], a[24:27], a[180:183], v[164:167]\n"
    "ds_read_b64_tr_b16 a[4:5], v234 offset:4608\n"
    "v_mfma_f32_16x16x32_bf16 v[164:167], a[28:31], a[188:191], v[164:167]\n"
    "ds_read_b64_tr_b16 a[6:7], v234 offset:6656\n"
    "v_mfma_f32_16x16x32_bf16 v[168:171], a[16:19], v[48:51], v[168:171]\n"
    "ds_read_b64_tr_b16 a[8:9], v234 offset:8704\n"
    "v_mfma_f32_16x16x32_bf16 v[168:171], a[20:23], v[56:59], v[168:171]\n"
    "ds_read_b64_tr_b16 a[10:11], v234 offset:10752\n"
    "v_mfma_f32_16x16x32_bf16 v[168:171], a[24:27], v[64:67], v[168:171]\n"
    "ds_read_b64_tr_b16 a[12:13], v234 offset:12800\n"
    "v_mfma_f32_16x16x32_bf16 v[168:171], a[28:31], v[72:75], v[168:171]\n"
    "ds_read_b64_tr_b16 a[14:15], v234 offset:14848\n"
    "v_mfma_f32_16x16x32_bf16 v[172:175], a[16:19], v[52:55], v[172:175]\n"
    "s_add_u32 m0, s55, 4224\n"
    "v_mfma_f32_16x16x32_bf16 v[172:175], a[20:23], v[60:63], v[172:175]\n"
    "buffer_load_dwordx4 v230, s[36:39], s52 offen lds\n"
    "v_mfma_f32_16x16x32_bf16 v[172:175], a[24:27], v[68:71], v[172:175]\n"
    "s_add_u32 s52, s52, 4096\n"
    "v_mfma_f32_16x16x32_bf16 v[172:175], a[28:31], v[76:79], v[172:175]\n"
    "v_mfma_f32_16x16x32_bf16 v[176:179], a[16:19], a[168:171], v[176:179]\n"
    "s_add_u32 m0, s55, 8448\n"
    "s_add_u32 s96, s54, s62\n"
    "v_mfma_f32_16x16x32_bf16 v[176:179], a[20:23], a[176:179], v[176:179]\n"
    "s_add_u32 s54, s54, 16896\n"
    "buffer_load_dwordx4 v230, s[36:39], s52 offen lds\n"
    "v_mfma_f32_16x16x32_bf16 v[176:179], a[24:27], a[184:187], v[176:179]\n"
    "s_add_u32 s52, s52, 4096\n"
    "s_sub_u32 s97, s54, 84480\n"
    "v_mfma_f32_16x16x32_bf16 v[176:179], a[28:31], a[192:195], v[176:179]\n"
    "s_waitcnt lgkmcnt(0)\n"
    "s_min_u32 s54, s54, s97\n"
    "v_mfma_f32_16x16x32_bf16 v[180:183], a[0:3], a[164:167], v[180:183]\n"
    "s_sub_u32 s97, s54, s67\n"
    "ds_read_b64_tr_b16 a[16:17], v235 offset:512\n"
    "s_mov_b32 s68, s72\n"
    "v_mfma_f32_16x16x32_bf16 v[180:183], a[4:7], a[172:175], v[180:183]\n"
    "s_mov_b32 s67, s54\n"
    "ds_read_b64_tr_b16 a[18:19], v235 offset:2560\n"
    "s_mov_b32 s69, s73\n"
    "v_mfma_f32_16x16x32_bf16 v[180:183], a[8:11], a[180:183], v[180:183]\n"
    "s_mov_b32 s70, s74\n"
    "ds_read_b64_tr_b16 a[20:21], v235 offset:4608\n"
    "v_mfma_f32_16x16x32_bf16 v[180:183], a[12:15], a[188:191], v[180:183]\n"
    "s_mov_b32 s71, s75\n"
    "ds_read_b64_tr_b16 a[22:23], v235 offset:6656\n"
    "v_mfma_f32_16x16x32_bf16 v[184:187], a[0:3], v[48:51], v[184:187]\n"
    "s_mov_b32 s33, s34\n"
    "ds_read_b64_tr_b16 a[24:25], v235 offset:8704\n"
    "v_mfma_f32_16x16x32_bf16 v[184:187], a[4:7], v[56:59], v[184:187]\n"
    "s_and_b32 s80, s23, 255\n"
    "ds_read_b64_tr_b16 a[26:27], v235 offset:10752\n"
    "v_mfma_f32_16x16x32_bf16 v[184:187], a[8:11], v[64:67], v[184:187]\n"
    "s_bfe_u32 s81, s23, 0x20008\n"
    "ds_read_b64_tr_b16 a[28:29], v235 offset:12800\n"
    "v_mfma_f32_16x16x32_bf16 v[184:187], a[12:15], v[72:75], v[184:187]\n"
    "s_lshr_b32 s23, s23, 10\n"
    "ds_read_b64_tr_b16 a[30:31], v235 offset:14848\n"
    "v_add_u32 v234, s97, v234\n"
    "v_mfma_f32_16x16x32_bf16 v[188:191], a[0:3], v[52:55], v[188:191]\n"
    "s_add_u32 m0, s55, 12672\n"
    "v_xor_b32 v235, 32, v234\n"
    "s_lshl_b32 s74, s80, 18\n"
    "v_mfma_f32_16x16x32_bf16 v[188:191], a[4:7], v[60:63], v[188:191]\n"
    "s_mul_i32 s80, s80, 376832\n"
    "buffer_load_dwordx4 v230, s[36:39], s52 offen lds\n"
    "v_mfma_f32_16x16x32_bf16 v[188:191], a[8:11], v[68:71], v[188:191]\n"
    "s_add_u32 s52, s52, 4096\n"
    "s_lshl_b32 s34, s81, 7\n"
    "v_mfma_f32_16x16x32_bf16 v[188:191], a[12:15], v[76:79], v[188:191]\n"
    "s_waitcnt lgkmcnt(15)\n"
    "s_and_b32 s34, s34, 384\n"
    "v_mfma_f32_16x16x32_bf16 v[192:195], a[0:3], a[168:171], v[192:195]\n"
    "s_lshl_b32 s81, s81, 17\n"
    "buffer_store_dwordx4 v[252:255], v232, s[48:51], s27 offen offset:0 sc1\n"
    "v_mfma_f32_16x16x32_bf16 v[192:195], a[4:7], a[176:179], v[192:195]\n"
    "s_add_u32 s81, s81, s79\n"
    "ds_read_b64 v[252:253], v238 offset:4608\n"
    "v_mfma_f32_16x16x32_bf16 v[192:195], a[8:11], a[184:187], v[192:195]\n"
    "s_add_u32 s72, s80, s81\n"
    "ds_read_b64 v[254:255], v238 offset:4736\n"
    "v_mfma_f32_16x16x32_bf16 v[192:195], a[12:15], a[192:195], v[192:195]\n"
    "s_waitcnt vmcnt(13) lgkmcnt(2)\n"
    "ds_inc_u32 v228, v236\n"
    "v_mfma_f32_16x16x32_bf16 v[196:199], a[16:19], a[164:167], v[196:199]\n"
    "s_add_u32 s73, s80, 376832\n"
    "ds_read_b64_tr_b16 a[0:1], v234 offset:0\n"
    "v_mfma_f32_16x16x32_bf16 v[196:199], a[20:23], a[172:175], v[196:199]\n"
    "s_cmp_gt_u32 s81, 344064\n"
    "s_cselect_b32 s75, 0xff800000, 0\n"
    "ds_read_b64_tr_b16 a[2:3], v234 offset:2048\n"
    "v_mfma_f32_16x16x32_bf16 v[196:199], a[24:27], a[180:183], v[196:199]\n"
    "s_add_u32 s61, s61, 1\n"
    "ds_read_b64_tr_b16 a[4:5], v234 offset:4096\n"
    "v_mfma_f32_16x16x32_bf16 v[196:199], a[28:31], a[188:191], v[196:199]\n"
    "ds_read_b64_tr_b16 a[6:7], v234 offset:6144\n"
    "v_mfma_f32_16x16x32_bf16 v[200:203], a[16:19], v[48:51], v[200:203]\n"
    "ds_read_b64_tr_b16 a[8:9], v234 offset:8192\n"
    "v_mfma_f32_16x16x32_bf16 v[200:203], a[20:23], v[56:59], v[200:203]\n"
    "ds_read_b64_tr_b16 a[10:11], v234 offset:10240\n"
    "v_mfma_f32_16x16x32_bf16 v[200:203], a[24:27], v[64:67], v[200:203]\n"
    "ds_read_b64_tr_b16 a[12:13], v234 offset:12288\n"
    "v_mfma_f32_16x16x32_bf16 v[200:203], a[28:31], v[72:75], v[200:203]\n"
    "ds_read_b32 v229, v230\n"
    "v_mfma_f32_16x16x32_bf16 v[204:207], a[16:19], v[52:55], v[204:207]\n"
    "s_waitcnt lgkmcnt(9)\n"
    "ds_read_b64_tr_b16 a[14:15], v234 offset:14336\n"
    "v_mfma_f32_16x16x32_bf16 v[204:207], a[20:23], v[60:63], v[204:207]\n"
    "buffer_store_dwordx4 v[252:255], v232, s[48:51], s28 offen offset:0 sc1\n"
    "v_mfma_f32_16x16x32_bf16 v[204:207], a[24:27], v[68:71], v[204:207]\n"
    "s_cmp_lt_u32 s77, 3\n"
    "s_cselect_b32 s73, s73, 0\n"
    "s_cselect_b32 s76, 0x4000000, 0\n"
    "v_mfma_f32_16x16x32_bf16 v[204:207], a[28:31], v[76:79], v[204:207]\n"
    "s_add_u32 s77, s77, 1\n"
    "ds_read_b64 v[252:253], v238 offset:4672\n"
    "v_mfma_f32_16x16x32_bf16 v[208:211], a[16:19], a[168:171], v[208:211]\n"
    "ds_read_b64 v[254:255], v238 offset:4800\n"
    "v_mfma_f32_16x16x32_bf16 v[208:211], a[20:23], a[176:179], v[208:211]\n"
    "s_waitcnt lgkmcnt(3)\n"
    "v_cmp_gt_u32 vcc, s61, v229\n"
    "v_mfma_f32_16x16x32_bf16 v[208:211], a[24:27], a[184:187], v[208:211]\n"
    "v_mfma_f32_16x16x32_bf16 v[208:211], a[28:31], a[192:195], v[208:211]\n"
    "s_waitcnt lgkmcnt(2)\n"
    "v_mfma_f32_16x16x32_bf16 v[148:151], a[0:3], v[80:83], v[148:151]\n"
    "s_cbranch_vccnz L_sp29%=\n"
    "L_rt29%=:\n"
    "ds_read_b64_tr_b16 a[16:17], v235 offset:0\n"
    "v_mfma_f32_16x16x32_bf16 v[148:151], a[4:7], v[92:95], v[148:151]\n"
    "ds_read_b64_tr_b16 a[18:19], v235 offset:2048\n"
    "v_mfma_f32_16x16x32_bf16 v[148:151], a[8:11], v[100:103], v[148:151]\n"
    "ds_read_b64_tr_b16 a[20:21], v235 offset:4096\n"
    "v_mfma_f32_16x16x32_bf16 v[148:151], a[12:15], v[108:111], v[148:151]\n"
    "ds_read_b64_tr_b16 a[22:23], v235 offset:6144\n"
    "v_mfma_f32_16x16x32_bf16 v[152:155], a[0:3], v[84:87], v[152:155]\n"
    "ds_read_b64_tr_b16 a[24:25], v235 offset:8192\n"
    "v_mfma_f32_16x16x32_bf16 v[152:155], a[4:7], a[200:203], v[152:155]\n"
    "ds_read_b64_tr_b16 a[26:27], v235 offset:10240\n"
    "v_mfma_f32_16x16x32_bf16 v[152:155], a[8:11], a[208:211], v[152:155]\n"
    "ds_read_b64_tr_b16 a[28:29], v235 offset:12288\n"
    "v_mfma_f32_16x16x32_bf16 v[152:155], a[12:15], a[216:219], v[152:155]\n"
    "ds_read_b64_tr_b16 a[30:31], v235 offset:14336\n"
    "v_mfma_f32_16x16x32_bf16 v[156:159], a[0:3], v[88:91], v[156:159]\n"
    "s_add_u32 m0, s96, 0\n"
    "v_mfma_f32_16x16x32_bf16 v[156:159], a[4:7], a[204:207], v[156:159]\n"
    "buffer_load_dwordx4 v230, s[36:39], s52 offen lds\n"
    "v_mfma_f32_16x16x32_bf16 v[156:159], a[8:11], a[212:215], v[156:159]\n"
    "s_add_u32 s52, s52, 4096\n"
    "v_mfma_f32_16x16x32_bf16 v[156:159], a[12:15], a[220:223], v[156:159]\n"
    "v_mfma_f32_16x16x32_bf16 v[160:163], a[0:3], a[196:199], v[160:163]\n"
    "s_add_u32 m0, s96, 4224\n"
    "v_mfma_f32_16x16x32_bf16 v[160:163], a[4:7], v[96:99], v[160:163]\n"
    "buffer_load_dwordx4 v230, s[36:39], s52 offen lds\n"
    "v_mfma_f32_16x16x32_bf16 v[160:163], a[8:11], v[104:107], v[160:163]\n"
    "s_add_u32 s52, s52, 4096\n"
    "v_mfma_f32_16x16x32_bf16 v[160:163], a[12:15], v[112:115], v[160:163]\n"
    "s_waitcnt lgkmcnt(0)\n"
    "v_mfma_f32_16x16x32_bf16 v[164:167], a[16:19], v[80:83], v[164:167]\n"
    "ds_read_b64_tr_b16 a[0:1], v234 offset:512\n"
    "v_mfma_f32_16x16x32_bf16 v[164:167], a[20:23], v[92:95], v[164:167]\n"
    "ds_read_b64_tr_b16 a[2:3], v234 offset:2560\n"
    "v_mfma_f32_16x16x32_bf16 v[164:167], a[24:27], v[100:103], v[164:167]\n"
    "ds_read_b64_tr_b16 a[4:5], v234 offset:4608\n"
    "v_mfma_f32_16x16x32_bf16 v[164:167], a[28:31], v[108:111], v[164:167]\n"
    "ds_read_b64_tr_b16 a[6:7], v234 offset:6656\n"
    "v_mfma_f32_16x16x32_bf16 v[168:171], a[16:19], v[84:87], v[168:171]\n"
    "ds_read_b64_tr_b16 a[8:9], v234 offset:8704\n"
    "v_mfma_f32_16x16x32_bf16 v[168:171], a[20:23], a[200:203], v[168:171]\n"
    "ds_read_b64_tr_b16 a[10:11], v234 offset:10752\n"
    "v_mfma_f32_16x16x32_bf16 v[168:171], a[24:27], a[208:211], v[168:171]\n"
    "ds_read_b64_tr_b16 a[12:13], v234 offset:12800\n"
    "v_mfma_f32_16x16x32_bf16 v[168:171], a[28:31], a[216:219], v[168:171]\n"
    "ds_read_b64_tr_b16 a[14:15], v234 offset:14848\n"
    "v_mfma_f32_16x16x32_bf16 v[172:175], a[16:19], v[88:91], v[172:175]\n"
    "s_waitcnt lgkmcnt(15)\n"
    "v_mfma_f32_16x16x32_bf16 v[172:175], a[20:23], a[204:207], v[172:175]\n"
    "buffer_store_dwordx4 v[252:255], v232, s[48:51], s29 offen offset:0 sc1\n"
    "v_mfma_f32_16x16x32_bf16 v[172:175], a[24:27], a[212:215], v[172:175]\n"
    "s_nop 0\n"
    "ds_read_b64 v[252:253], v238 offset:6912\n"
    "v_mfma_f32_16x16x32_bf16 v[172:175], a[28:31], a[220:223], v[172:175]\n"
    "ds_read_b64 v[254:255], v238 offset:7040\n"
    "v_mfma_f32_16x16x32_bf16 v[176:179], a[16:19], a[196:199], v[176:179]\n"
    "s_add_u32 m0, s96, 8448\n"
    "s_add_u32 s55, s54, s62\n"
    "v_mfma_f32_16x16x32_bf16 v[176:179], a[20:23], v[96:99], v[176:179]\n"
    "s_add_u32 s54, s54, 16896\n"
    "buffer_load_dwordx4 v230, s[36:39], s52 offen lds\n"
    "v_mfma_f32_16x16x32_bf16 v[176:179], a[24:27], v[104:107], v[176:179]\n"
    "s_add_u32 s52, s52, 4096\n"
    "s_sub_u32 s97, s54, 84480\n"
    "v_mfma_f32_16x16x32_bf16 v[176:179], a[28:31], v[112:115], v[176:179]\n"
    "s_waitcnt lgkmcnt(2)\n"
    "s_min_u32 s54, s54, s97\n"
    "v_mfma_f32_16x16x32_bf16 v[180:183], a[0:3], v[80:83], v[180:183]\n"
    "s_sub_u32 s97, s54, s67\n"
    "ds_read_b64_tr_b16 a[16:17], v235 offset:512\n"
    "v_mfma_f32_16x16x32_bf16 v[180:183], a[4:7], v[92:95], v[180:183]\n"
    "s_mov_b32 s67, s54\n"
    "ds_read_b64_tr_b16 a[18:19], v235 offset:2560\n"
    "v_mfma_f32_16x16x32_bf16 v[180:183], a[8:11], v[100:103], v[180:183]\n"
    "ds_read_b64_tr_b16 a[20:21], v235 offset:4608\n"
    "v_mfma_f32_16x16x32_bf16 v[180:183], a[12:15], v[108:111], v[180:183]\n"
    "ds_read_b64_tr_b16 a[22:23], v235 offset:6656\n"
    "v_mfma_f32_16x16x32_bf16 v[184:187], a[0:3], v[84:87], v[184:187]\n"
    "ds_read_b64_tr_b16 a[24:25], v235 offset:8704\n"
    "v_mfma_f32_16x16x32_bf16 v[184:187], a[4:7], a[200:203], v[184:187]\n"
    "ds_read_b64_tr_b16 a[26:27], v235 offset:10752\n"
    "v_mfma_f32_16x16x32_bf16 v[184:187], a[8:11], a[208:211], v[184:187]\n"
    "ds_read_b64_tr_b16 a[28:29], v235 offset:12800\n"
    "v_mfma_f32_16x16x32_bf16 v[184:187], a[12:15], a[216:219], v[184:187]\n"
    "ds_read_b64_tr_b16 a[30:31], v235 offset:14848\n"
    "v_add_u32 v234, s97, v234\n"
    "v_mfma_f32_16x16x32_bf16 v[188:191], a[0:3], v[88:91], v[188:191]\n"
    "s_add_u32 m0, s96, 12672\n"
    "v_xor_b32 v235, 32, v234\n"
    "v_mfma_f32_16x16x32_bf16 v[188:191], a[4:7], a[204:207], v[188:191]\n"
    "buffer_load_dwordx4 v230, s[36:39], s52 offen lds\n"
    "v_mfma_f32_16x16x32_bf16 v[188:191], a[8:11], a[212:215], v[188:191]\n"
    "s_add_u32 s52, s52, 4096\n"
    "v_mfma_f32_16x16x32_bf16 v[188:191], a[12:15], a[220:223], v[188:191]\n"
    "v_mfma_f32_16x16x32_bf16 v[192:195], a[0:3], a[196:199], v[192:195]\n"
    "s_waitcnt lgkmcnt(8)\n"
    "v_mfma_f32_16x16x32_bf16 v[192:195], a[4:7], v[96:99], v[192:195]\n"
    "buffer_store_dwordx4 v[252:255], v232, s[48:51], s30 offen offset:0 sc1\n"
    "v_mfma_f32_16x16x32_bf16 v[192:195], a[8:11], v[104:107], v[192:195]\n"
    "s_nop 0\n"
    "ds_read_b64 v[252:253], v238 offset:6976\n"
    "v_mfma_f32_16x16x32_bf16 v[192:195], a[12:15], v[112:115], v[192:195]\n"
    "s_waitcnt vmcnt(15) lgkmcnt(1)\n"
    "ds_inc_u32 v228, v236\n"
    "v_mfma_f32_16x16x32_bf16 v[196:199], a[16:19], v[80:83], v[196:199]\n"
    "ds_read_b64_tr_b16 a[0:1], v234 offset:0\n"
    "v_mfma_f32_16x16x32_bf16 v[196:199], a[20:23], v[92:95], v[196:199]\n"
    "ds_read_b64_tr_b16 a[2:3], v234 offset:2048\n"
    "v_mfma_f32_16x16x32_bf16 v[196:199], a[24:27], v[100:103], v[196:199]\n"
    "s_add_u32 s61, s61, 1\n"
    "ds_read_b64 v[254:255], v238 offset:7104\n"
    "v_mfma_f32_16x16x32_bf16 v[196:199], a[28:31], v[108:111], v[196:199]\n"
    "ds_read_b64_tr_b16 a[4:5], v234 offset:4096\n"
    "v_mfma_f32_16x16x32_bf16 v[200:203], a[16:19], v[84:87], v[200:203]\n"
    "ds_read_b64_tr_b16 a[6:7], v234 offset:6144\n"
    "v_mfma_f32_16x16x32_bf16 v[200:203], a[20:23], a[200:203], v[200:203]\n"
    "ds_read_b64_tr_b16 a[8:9], v234 offset:8192\n"
    "v_mfma_f32_16x16x32_bf16 v[200:203], a[24:27], a[208:211], v[200:203]\n"
    "ds_read_b64_tr_b16 a[10:11], v234 offset:10240\n"
    "v_mfma_f32_16x16x32_bf16 v[200:203], a[28:31], a[216:219], v[200:203]\n"
    "ds_read_b64_tr_b16 a[12:13], v234 offset:12288\n"
    "v_mfma_f32_16x16x32_bf16 v[204:207], a[16:19], v[88:91], v[204:207]\n"
    "s_waitcnt lgkmcnt(5)\n"
    "ds_read_b32 v229, v230\n"
    "ds_read_b64_tr_b16 a[14:15], v234 offset:14336\n"
    "v_mfma_f32_16x16x32_bf16 v[204:207], a[20:23], a[204:207], v[204:207]\n"
    "buffer_store_dwordx4 v[252:255], v232, s[48:51], s31 offen offset:0 sc1\n"
    "v_mfma_f32_16x16x32_bf16 v[204:207], a[24:27], a[212:215], v[204:207]\n"
    "s_nop 0\n"
    "v_xor_b32 v232, 128, v232\n"
    "v_mfma_f32_16x16x32_bf16 v[204:207], a[28:31], a[220:223], v[204:207]\n"
    "v_mfma_f32_16x16x32_bf16 v[208:211], a[16:19], a[196:199], v[208:211]\n"
    "v_mfma_f32_16x16x32_bf16 v[208:211], a[20:23], v[96:99], v[208:211]\n"
    "s_waitcnt lgkmcnt(1)\n"
    "v_cmp_gt_u32 vcc, s61, v229\n"
    "v_mfma_f32_16x16x32_bf16 v[208:211], a[24:27], v[104:107], v[208:211]\n"
    "v_mfma_f32_16x16x32_bf16 v[208:211], a[28:31], v[112:115], v[208:211]\n"
    "s_waitcnt lgkmcnt(0)\n"
    "v_mfma_f32_16x16x32_bf16 v[148:151], a[0:3], v[116:119], v[148:151]\n"
    "s_cbranch_vccnz L_sp30%=\n"
    "L_rt30%=:\n"
    "ds_read_b64_tr_b16 a[16:17], v235 offset:0\n"
    "v_mfma_f32_16x16x32_bf16 v[148:151], a[4:7], v[124:127], v[148:151]\n"
    "ds_read_b64_tr_b16 a[18:19], v235 offset:2048\n"
    "v_mfma_f32_16x16x32_bf16 v[148:151], a[8:11], v[132:135], v[148:151]\n"
    "ds_read_b64_tr_b16 a[20:21], v235 offset:4096\n"
    "v_mfma_f32_16x16x32_bf16 v[148:151], a[12:15], v[140:143], v[148:151]\n"
    "ds_read_b64_tr_b16 a[22:23], v235 offset:6144\n"
    "v_mfma_f32_16x16x32_bf16 v[152:155], a[0:3], a[224:227], v[152:155]\n"
    "ds_read_b64_tr_b16 a[24:25], v235 offset:8192\n"
    "v_mfma_f32_16x16x32_bf16 v[152:155], a[4:7], a[232:235], v[152:155]\n"
    "ds_read_b64_tr_b16 a[26:27], v235 offset:10240\n"
    "v_mfma_f32_16x16x32_bf16 v[152:155], a[8:11], a[240:243], v[152:155]\n"
    "ds_read_b64_tr_b16 a[28:29], v235 offset:12288\n"
    "v_mfma_f32_16x16x32_bf16 v[152:155], a[12:15], a[248:251], v[152:155]\n"
    "ds_read_b64_tr_b16 a[30:31], v235 offset:14336\n"
    "v_mul_f32 v148, v148, v244\n"
    "v_mfma_f32_16x16x32_bf16 v[156:159], a[0:3], a[228:231], v[156:159]\n"
    "s_add_u32 m0, s55, 0\n"
    "v_mul_f32 v149, v149, v244\n"
    "v_mul_f32 v150, v150, v244\n"
    "v_mfma_f32_16x16x32_bf16 v[156:159], a[4:7], a[236:239], v[156:159]\n"
    "buffer_load_dwordx4 v230, s[36:39], s52 offen lds\n"
    "v_mfma_f32_16x16x32_bf16 v[156:159], a[8:11], a[244:247], v[156:159]\n"
    "s_add_u32 s52, s52, 4096\n"
    "v_mul_f32 v151, v151, v244\n"
    "v_cvt_pk_bf16_f32 v148, v148, v149\n"
    "v_mfma_f32_16x16x32_bf16 v[156:159], a[12:15], a[252:255], v[156:159]\n"
    "v_cvt_pk_bf16_f32 v149, v150, v151\n"
    "v_mul_f32 v152, v152, v245\n"
    "v_mfma_f32_16x16x32_bf16 v[160:163], a[0:3], v[120:123], v[160:163]\n"
    "s_add_u32 m0, s55, 4224\n"
    "ds_write_b64 v237, v[148:149] offset:0\n"
    "v_mfma_f32_16x16x32_bf16 v[160:163], a[4:7], v[128:131], v[160:163]\n"
    "buffer_load_dwordx4 v230, s[36:39], s52 offen lds\n"
    "v_mfma_f32_16x16x32_bf16 v[160:163], a[8:11], v[136:139], v[160:163]\n"
    "s_add_u32 s52, s52, 4096\n"
    "v_mul_f32 v153, v153, v245\n"
    "v_mul_f32 v154, v154, v245\n"
    "v_mfma_f32_16x16x32_bf16 v[160:163], a[12:15], v[144:147], v[160:163]\n"
    "s_waitcnt lgkmcnt(1)\n"
    "v_mul_f32 v155, v155, v245\n"
    "v_cvt_pk_bf16_f32 v152, v152, v153\n"
    "v_mfma_f32_16x16x32_bf16 v[164:167], a[16:19], v[116:119], v[164:167]\n"
    "ds_read_b64_tr_b16 a[0:1], v234 offset:512\n"
    "v_cvt_pk_bf16_f32 v153, v154, v155\n"
    "v_mfma_f32_16x16x32_bf16 v[164:167], a[20:23], v[124:127], v[164:167]\n"
    "ds_read_b64_tr_b16 a[2:3], v234 offset:2560\n"
    "v_mul_f32 v156, v156, v246\n"
    "v_mfma_f32_16x16x32_bf16 v[164:167], a[24:27], v[132:135], v[164:167]\n"
    "ds_read_b64_tr_b16 a[4:5], v234 offset:4608\n"
    "v_mul_f32 v157, v157, v246\n"
    "v_mfma_f32_16x16x32_bf16 v[164:167], a[28:31], v[140:143], v[164:167]\n"
    "ds_read_b64_tr_b16 a[6:7], v234 offset:6656\n"
    "v_mul_f32 v158, v158, v246\n"
    "v_mfma_f32_16x16x32_bf16 v[168:171], a[16:19], a[224:227], v[168:171]\n"
    "ds_read_b64_tr_b16 a[8:9], v234 offset:8704\n"
    "v_mul_f32 v159, v159, v246\n"
    "v_mfma_f32_16x16x32_bf16 v[168:171], a[20:23], a[232:235], v[168:171]\n"
    "ds_read_b64_tr_b16 a[10:11], v234 offset:10752\n"
    "v_cvt_pk_bf16_f32 v156, v156, v157\n"
    "v_mfma_f32_16x16x32_bf16 v[168:171], a[24:27], a[240:243], v[168:171]\n"
    "ds_read_b64_tr_b16 a[12:13], v234 offset:12800\n"
    "v_cvt_pk_bf16_f32 v157, v158, v159\n"
    "v_mfma_f32_16x16x32_bf16 v[168:171], a[28:31], a[248:251], v[168:171]\n"
    "ds_read_b64_tr_b16 a[14:15], v234 offset:14848\n"
    "v_mul_f32 v164, v164, v244\n"
    "v_mfma_f32_16x16x32_bf16 v[172:175], a[16:19], a[228:231], v[172:175]\n"
    "v_mul_f32 v165, v165, v244\n"
    "v_mul_f32 v166, v166, v244\n"
    "v_mfma_f32_16x16x32_bf16 v[172:175], a[20:23], a[236:239], v[172:175]\n"
    "v_mul_f32 v167, v167, v244\n"
    "v_cvt_pk_bf16_f32 v164, v164, v165\n"
    "v_mfma_f32_16x16x32_bf16 v[172:175], a[24:27], a[244:247], v[172:175]\n"
    "v_cvt_pk_bf16_f32 v165, v166, v167\n"
    "v_mul_f32 v160, v160, v247\n"
    "v_mfma_f32_16x16x32_bf16 v[172:175], a[28:31], a[252:255], v[172:175]\n"
    "s_add_u32 m0, s55, 8448\n"
    "ds_write_b64 v237, v[152:153] offset:2304\n"
    "v_mfma_f32_16x16x32_bf16 v[176:179], a[16:19], v[120:123], v[176:179]\n"
    "s_add_u32 s96, s54, s62\n"
    "buffer_load_dwordx4 v230, s[36:39], s52 offen lds\n"
    "v_mfma_f32_16x16x32_bf16 v[176:179], a[20:23], v[128:131], v[176:179]\n"
    "s_add_u32 s52, s52, 4096\n"
    "ds_write_b64 v237, v[164:165] offset:576\n"
    "v_mfma_f32_16x16x32_bf16 v[176:179], a[24:27], v[136:139], v[176:179]\n"
    "s_add_u32 s54, s54, 16896\n"
    "v_mul_f32 v168, v168, v245\n"
    "v_mul_f32 v169, v169, v245\n"
    "v_mfma_f32_16x16x32_bf16 v[176:179], a[28:31], v[144:147], v[176:179]\n"
    "s_waitcnt lgkmcnt(2)\n"
    "v_mul_f32 v170, v170, v245\n"
    "v_mul_f32 v171, v171, v245\n"
    "v_mfma_f32_16x16x32_bf16 v[180:183], a[0:3], v[116:119], v[180:183]\n"
    "s_sub_u32 s97, s54, 84480\n"
    "ds_read_b64_tr_b16 a[16:17], v235 offset:512\n"
    "v_cvt_pk_bf16_f32 v168, v168, v169\n"
    "v_mfma_f32_16x16x32_bf16 v[180:183], a[4:7], v[124:127], v[180:183]\n"
    "s_min_u32 s54, s54, s97\n"
    "ds_read_b64_tr_b16 a[18:19], v235 offset:2560\n"
    "v_cvt_pk_bf16_f32 v169, v170, v171\n"
    "v_mfma_f32_16x16x32_bf16 v[180:183], a[8:11], v[132:135], v[180:183]\n"
    "s_sub_u32 s97, s54, s66\n"
    "ds_read_b64_tr_b16 a[20:21], v235 offset:4608\n"
    "v_mul_f32 v161, v161, v247\n"
    "v_mfma_f32_16x16x32_bf16 v[180:183], a[12:15], v[140:143], v[180:183]\n"
    "s_mov_b32 s66, s54\n"
    "ds_read_b64_tr_b16 a[22:23], v235 offset:6656\n"
    "v_add_u32 v233, s97, v233\n"
    "v_mfma_f32_16x16x32_bf16 v[184:187], a[0:3], a[224:227], v[184:187]\n"
    "ds_read_b64_tr_b16 a[24:25], v235 offset:8704\n"
    "v_mul_f32 v162, v162, v247\n"
    "v_mfma_f32_16x16x32_bf16 v[184:187], a[4:7], a[232:235], v[184:187]\n"
    "ds_read_b64_tr_b16 a[26:27], v235 offset:10752\n"
    "v_mul_f32 v163, v163, v247\n"
    "v_mfma_f32_16x16x32_bf16 v[184:187], a[8:11], a[240:243], v[184:187]\n"
    "ds_read_b64_tr_b16 a[28:29], v235 offset:12800\n"
    "v_cvt_pk_bf16_f32 v160, v160, v161\n"
    "v_mfma_f32_16x16x32_bf16 v[184:187], a[12:15], a[248:251], v[184:187]\n"
    "ds_read_b64_tr_b16 a[30:31], v235 offset:14848\n"
    "v_cvt_pk_bf16_f32 v161, v162, v163\n"
    "v_mfma_f32_16x16x32_bf16 v[188:191], a[0:3], a[228:231], v[188:191]\n"
    "ds_write_b64 v237, v[156:157] offset:4608\n"
    "v_mfma_f32_16x16x32_bf16 v[188:191], a[4:7], a[236:239], v[188:191]\n"
    "ds_write_b64 v237, v[168:169] offset:2880\n"
    "v_mfma_f32_16x16x32_bf16 v[188:191], a[8:11], a[244:247], v[188:191]\n"
    "v_mul_f32 v172, v172, v246\n"
    "v_mul_f32 v173, v173, v246\n"
    "v_mfma_f32_16x16x32_bf16 v[188:191], a[12:15], a[252:255], v[188:191]\n"
    "s_add_u32 m0, s55, 12672\n"
    "v_mul_f32 v174, v174, v246\n"
    "v_mul_f32 v175, v175, v246\n"
    "v_mfma_f32_16x16x32_bf16 v[192:195], a[0:3], v[120:123], v[192:195]\n"
    "buffer_load_dwordx4 v230, s[36:39], s52 offen lds\n"
    "v_mfma_f32_16x16x32_bf16 v[192:195], a[4:7], v[128:131], v[192:195]\n"
    "s_add_u32 s52, s52, 4096\n"
    "v_cvt_pk_bf16_f32 v172, v172, v173\n"
    "v_cvt_pk_bf16_f32 v173, v174, v175\n"
    "v_mfma_f32_16x16x32_bf16 v[192:195], a[8:11], v[136:139], v[192:195]\n"
    "ds_write_b64 v237, v[160:161] offset:6912\n"
    "v_mfma_f32_16x16x32_bf16 v[192:195], a[12:15], v[144:147], v[192:195]\n"
    "s_waitcnt vmcnt(13) lgkmcnt(3)\n"
    "ds_inc_u32 v228, v236\n"
    "v_mfma_f32_16x16x32_bf16 v[196:199], a[16:19], v[116:119], v[196:199]\n"
    "ds_read_b128 v[148:151], v233 offset:0\n"
    "v_mul_f32 v176, v176, v247\n"
    "v_mfma_f32_16x16x32_bf16 v[196:199], a[20:23], v[124:127], v[196:199]\n"
    "ds_read_b128 v[152:155], v233 offset:64\n"
    "v_mul_f32 v177, v177, v247\n"
    "v_mfma_f32_16x16x32_bf16 v[196:199], a[24:27], v[132:135], v[196:199]\n"
    "ds_write_b64 v237, v[172:173] offset:5184\n"
    "v_mfma_f32_16x16x32_bf16 v[196:199], a[28:31], v[140:143], v[196:199]\n"
    "v_mul_f32 v178, v178, v247\n"
    "v_mul_f32 v179, v179, v247\n"
    "v_mfma_f32_16x16x32_bf16 v[200:203], a[16:19], a[224:227], v[200:203]\n"
    "v_cvt_pk_bf16_f32 v176, v176, v177\n"
    "v_cvt_pk_bf16_f32 v177, v178, v179\n"
    "v_mfma_f32_16x16x32_bf16 v[200:203], a[20:23], a[232:235], v[200:203]\n"
    "ds_read_b128 v[156:159], v233 offset:128\n"
    "v_mul_f32 v180, v180, v244\n"
    "v_mfma_f32_16x16x32_bf16 v[200:203], a[24:27], a[240:243], v[200:203]\n"
    "ds_write_b64 v237, v[176:177] offset:7488\n"
    "ds_read_b128 v[160:163], v233 offset:192\n"
    "v_mfma_f32_16x16x32_bf16 v[200:203], a[28:31], a[248:251], v[200:203]\n"
    "ds_read_b128 v[164:167], v233 offset:256\n"
    "v_mul_f32 v181, v181, v244\n"
    "v_mfma_f32_16x16x32_bf16 v[204:207], a[16:19], a[228:231], v[204:207]\n"
    "ds_read_b128 v[168:171], v233 offset:320\n"
    "v_mul_f32 v182, v182, v244\n"
    "v_mfma_f32_16x16x32_bf16 v[204:207], a[20:23], a[236:239], v[204:207]\n"
    "ds_read_b128 v[172:175], v233 offset:384\n"
    "v_mul_f32 v183, v183, v244\n"
    "v_mfma_f32_16x16x32_bf16 v[204:207], a[24:27], a[244:247], v[204:207]\n"
    "ds_read_b128 v[176:179], v233 offset:448\n"
    "v_cvt_pk_bf16_f32 v180, v180, v181\n"
    "v_mfma_f32_16x16x32_bf16 v[204:207], a[28:31], a[252:255], v[204:207]\n"
    "v_cvt_pk_bf16_f32 v181, v182, v183\n"
    "v_mul_f32 v184, v184, v245\n"
    "v_mfma_f32_16x16x32_bf16 v[208:211], a[16:19], v[120:123], v[208:211]\n"
    "s_waitcnt vmcnt(24)\n"
    "v_mul_f32 v185, v185, v245\n"
    "v_mul_f32 v186, v186, v245\n"
    "v_mfma_f32_16x16x32_bf16 v[208:211], a[20:23], v[128:131], v[208:211]\n"
    "v_mul_f32 v187, v187, v245\n"
    "v_cvt_pk_bf16_f32 v184, v184, v185\n"
    "v_mfma_f32_16x16x32_bf16 v[208:211], a[24:27], v[136:139], v[208:211]\n"
    "v_cvt_pk_bf16_f32 v185, v186, v187\n"
    "v_mul_f32 v188, v188, v246\n"
    "v_mul_f32 v189, v189, v246\n"
    "v_mul_f32 v190, v190, v246\n"
    "v_mul_f32 v191, v191, v246\n"
    "v_cvt_pk_bf16_f32 v188, v188, v189\n"
    "v_cvt_pk_bf16_f32 v189, v190, v191\n"
    "v_mul_f32 v192, v192, v247\n"
    "v_mul_f32 v193, v193, v247\n"
    "v_mul_f32 v194, v194, v247\n"
    "v_mul_f32 v195, v195, v247\n"
    "v_cvt_pk_bf16_f32 v192, v192, v193\n"
    "v_cvt_pk_bf16_f32 v193, v194, v195\n"
    "v_mfma_f32_16x16x32_bf16 v[208:211], a[28:31], v[144:147], v[208:211]\n"
    "s_waitcnt lgkmcnt(0)\n"
    "ds_write_b64 v237, v[180:181] offset:1152\n"
    "ds_write_b64 v237, v[184:185] offset:3456\n"
    "ds_write_b64 v237, v[188:189] offset:5760\n"
    "ds_write_b64 v237, v[192:193] offset:8064\n"
    "s_sub_u32 s65, s65, 1\n"
    "s_cmp_lg_u32 s65, 0\n"
    "s_cbranch_scc1 L_blk%=\n"
    "v_mul_f32 v196, v196, v244\n"
    "v_mul_f32 v197, v197, v244\n"
    "v_mul_f32 v198, v198, v244\n"
    "v_mul_f32 v199, v199, v244\n"
    "v_cvt_pk_bf16_f32 v196, v196, v197\n"
    "v_cvt_pk_bf16_f32 v197, v198, v199\n"
    "ds_write_b64 v237, v[196:197] offset:1728\n"
    "ds_read_b64 v[0:1], v238 offset:0\n"
    "ds_read_b64 v[2:3], v238 offset:128\n"
    "ds_read_b64 v[4:5], v238 offset:64\n"
    "ds_read_b64 v[6:7], v238 offset:192\n"
    "v_mul_f32 v200, v200, v245\n"
    "v_mul_f32 v201, v201, v245\n"
    "v_mul_f32 v202, v202, v245\n"
    "v_mul_f32 v203, v203, v245\n"
    "v_cvt_pk_bf16_f32 v200, v200, v201\n"
    "v_cvt_pk_bf16_f32 v201, v202, v203\n"
    "ds_write_b64 v237, v[200:201] offset:4032\n"
    "ds_read_b64 v[8:9], v238 offset:2304\n"
    "ds_read_b64 v[10:11], v238 offset:2432\n"
    "ds_read_b64 v[12:13], v238 offset:2368\n"
    "ds_read_b64 v[14:15], v238 offset:2496\n"
    "v_mul_f32 v204, v204, v246\n"
    "v_mul_f32 v205, v205, v246\n"
    "v_mul_f32 v206, v206, v246\n"
    "v_mul_f32 v207, v207, v246\n"
    "v_cvt_pk_bf16_f32 v204, v204, v205\n"
    "v_cvt_pk_bf16_f32 v205, v206, v207\n"
    "ds_write_b64 v237, v[204:205] offset:6336\n"
    "ds_read_b64 v[16:17], v238 offset:4608\n"
    "ds_read_b64 v[18:19], v238 offset:4736\n"
    "ds_read_b64 v[20:21], v238 offset:4672\n"
    "ds_read_b64 v[22:23], v238 offset:4800\n"
    "v_mul_f32 v208, v208, v247\n"
    "v_mul_f32 v209, v209, v247\n"
    "v_mul_f32 v210, v210, v247\n"
    "v_mul_f32 v211, v211, v247\n"
    "v_cvt_pk_bf16_f32 v208, v208, v209\n"
    "v_cvt_pk_bf16_f32 v209, v210, v211\n"
    "ds_write_b64 v237, v[208:209] offset:8640\n"
    "ds_read_b64 v[24:25], v238 offset:6912\n"
    "ds_read_b64 v[26:27], v238 offset:7040\n"
    "ds_read_b64 v[28:29], v238 offset:6976\n"
    "ds_read_b64 v[30:31], v238 offset:7104\n"
    "s_waitcnt lgkmcnt(15)\n"
    "buffer_store_dwordx4 v[0:3], v232, s[48:51], s24 offen offset:0 sc1\n"
    "s_waitcnt lgkmcnt(15)\n"
    "buffer_store_dwordx4 v[4:7], v232, s[48:51], s25 offen offset:0 sc1\n"
    "s_waitcnt lgkmcnt(12)\n"
    "buffer_store_dwordx4 v[8:11], v232, s[48:51], s26 offen offset:0 sc1\n"
    "s_waitcnt lgkmcnt(10)\n"
    "buffer_store_dwordx4 v[12:15], v232, s[48:51], s27 offen offset:0 sc1\n"
    "s_waitcnt lgkmcnt(7)\n"
    "buffer_store_dwordx4 v[16:19], v232, s[48:51], s28 offen offset:0 sc1\n"
    "s_waitcnt lgkmcnt(5)\n"
    "buffer_store_dwordx4 v[20:23], v232, s[48:51], s29 offen offset:0 sc1\n"
    "s_waitcnt lgkmcnt(2)\n"
    "buffer_store_dwordx4 v[24:27], v232, s[48:51], s30 offen offset:0 sc1\n"
    "s_waitcnt lgkmcnt(0)\n"
    "buffer_store_dwordx4 v[28:31], v232, s[48:51], s31 offen offset:0 sc1\n"
    "s_waitcnt vmcnt(8) lgkmcnt(0)\n"
    "s_barrier\n"
    "ds_read_b32 v229, v230 offset:1024\n"
    "s_waitcnt lgkmcnt(0)\n"
    "v_cmp_ne_u32 vcc, 0, v229\n"
    "s_nop 4\n"
    "s_or_b64 s[84:85], s[84:85], vcc\n"
    "s_or_b32 %0, s84, s85\n"
    "s_branch L_end%=\n"
    "L_sp31%=:\n"
    "s_movk_i32 s92, 0x7fff\n"
    "L_lp31%=:\n"
    "ds_read_b32 v229, v230\n"
    "s_waitcnt lgkmcnt(0)\n"
    "v_cmp_gt_u32 vcc, s61, v229\n"
    "s_cbranch_vccz L_rt31%=\n"
    "s_sub_u32 s92, s92, 1\n"
    "s_cbranch_scc0 L_lp31%=\n"
    "ds_write_b32 v228, v236 offset:1024\n"
    "s_waitcnt lgkmcnt(0)\n"
    "s_branch L_rt31%=\n"
    "L_sp0%=:\n"
    "s_movk_i32 s92, 0x7fff\n"
    "L_lp0%=:\n"
    "ds_read_b32 v229, v230\n"
    "s_waitcnt lgkmcnt(0)\n"
    "v_cmp_gt_u32 vcc, s61, v229\n"
    "s_cbranch_vccz L_rt0%=\n"
    "s_sub_u32 s92, s92, 1\n"
    "s_cbranch_scc0 L_lp0%=\n"
    "ds_write_b32 v228, v236 offset:1024\n"
    "s_waitcnt lgkmcnt(0)\n"
    "s_branch L_rt0%=\n"
    "L_sp1%=:\n"
    "s_movk_i32 s92, 0x7fff\n"
    "L_lp1%=:\n"
    "ds_read_b32 v229, v230\n"
    "s_waitcnt lgkmcnt(0)\n"
    "v_cmp_gt_u32 vcc, s61, v229\n"
    "s_cbranch_vccz L_rt1%=\n"
    "s_sub_u32 s92, s92, 1\n"
    "s_cbranch_scc0 L_lp1%=\n"
    "ds_write_b32 v228, v236 offset:1024\n"
    "s_waitcnt lgkmcnt(0)\n"
    "s_branch L_rt1%=\n"
    "L_sp2%=:\n"
    "s_movk_i32 s92, 0x7fff\n"
    "L_lp2%=:\n"
    "ds_read_b32 v229, v230\n"
    "s_waitcnt lgkmcnt(0)\n"
    "v_cmp_gt_u32 vcc, s61, v229\n"
    "s_cbranch_vccz L_rt2%=\n"
    "s_sub_u32 s92, s92, 1\n"
    "s_cbranch_scc0 L_lp2%=\n"
    "ds_write_b32 v228, v236 offset:1024\n"
    "s_waitcnt lgkmcnt(0)\n"
    "s_branch L_rt2%=\n"
    "L_sp3%=:\n"
    "s_movk_i32 s92, 0x7fff\n"
    "L_lp3%=:\n"
    "ds_read_b32 v229, v230\n"
    "s_waitcnt lgkmcnt(0)\n"
    "v_cmp_gt_u32 vcc, s61, v229\n"
    "s_cbranch_vccz L_rt3%=\n"
    "s_sub_u32 s92, s92, 1\n"
    "s_cbranch_scc0 L_lp3%=\n"
    "ds_write_b32 v228, v236 offset:1024\n"
    "s_waitcnt lgkmcnt(0)\n"
    "s_branch L_rt3%=\n"
    "L_sp4%=:\n"
    "s_movk_i32 s92, 0x7fff\n"
    "L_lp4%=:\n"
    "ds_read_b32 v229, v230\n"
    "s_waitcnt lgkmcnt(0)\n"
    "v_cmp_gt_u32 vcc, s61, v229\n"
    "s_cbranch_vccz L_rt4%=\n"
    "s_sub_u32 s92, s92, 1\n"
    "s_cbranch_scc0 L_lp4%=\n"
    "ds_write_b32 v228, v236 offset:1024\n"
    "s_waitcnt lgkmcnt(0)\n"
    "s_branch L_rt4%=\n"
    "L_sp5%=:\n"
    "s_movk_i32 s92, 0x7fff\n"
    "L_lp5%=:\n"
    "ds_read_b32 v229, v230\n"
    "s_waitcnt lgkmcnt(0)\n"
    "v_cmp_gt_u32 vcc, s61, v229\n"
    "s_cbranch_vccz L_rt5%=\n"
    "s_sub_u32 s92, s92, 1\n"
    "s_cbranch_scc0 L_lp5%=\n"
    "ds_write_b32 v228, v236 offset:1024\n"
    "s_waitcnt lgkmcnt(0)\n"
    "s_branch L_rt5%=\n"
    "L_sp6%=:\n"
    "s_movk_i32 s92, 0x7fff\n"
    "L_lp6%=:\n"
    "ds_read_b32 v229, v230\n"
    "s_waitcnt lgkmcnt(0)\n"
    "v_cmp_gt_u32 vcc, s61, v229\n"
    "s_cbranch_vccz L_rt6%=\n"
    "s_sub_u32 s92, s92, 1\n"
    "s_cbranch_scc0 L_lp6%=\n"
    "ds_write_b32 v228, v236 offset:1024\n"
    "s_waitcnt lgkmcnt(0)\n"
    "s_branch L_rt6%=\n"
    "L_sp7%=:\n"
    "s_movk_i32 s92, 0x7fff\n"
    "L_lp7%=:\n"
    "ds_read_b32 v229, v230\n"
    "s_waitcnt lgkmcnt(0)\n"
    "v_cmp_gt_u32 vcc, s61, v229\n"
    "s_cbranch_vccz L_rt7%=\n"
    "s_sub_u32 s92, s92, 1\n"
    "s_cbranch_scc0 L_lp7%=\n"
    "ds_write_b32 v228, v236 offset:1024\n"
    "s_waitcnt lgkmcnt(0)\n"
    "s_branch L_rt7%=\n"
    "L_sp8%=:\n"
    "s_movk_i32 s92, 0x7fff\n"
    "L_lp8%=:\n"
    "ds_read_b32 v229, v230\n"
    "s_waitcnt lgkmcnt(0)\n"
    "v_cmp_gt_u32 vcc, s61, v229\n"
    "s_cbranch_vccz L_rt8%=\n"
    "s_sub_u32 s92, s92, 1\n"
    "s_cbranch_scc0 L_lp8%=\n"
    "ds_write_b32 v228, v236 offset:1024\n"
    "s_waitcnt lgkmcnt(0)\n"
    "s_branch L_rt8%=\n"
    "L_sp9%=:\n"
    "s_movk_i32 s92, 0x7fff\n"
    "L_lp9%=:\n"
    "ds_read_b32 v229, v230\n"
    "s_waitcnt lgkmcnt(0)\n"
    "v_cmp_gt_u32 vcc, s61, v229\n"
    "s_cbranch_vccz L_rt9%=\n"
    "s_sub_u32 s92, s92, 1\n"
    "s_cbranch_scc0 L_lp9%=\n"
    "ds_write_b32 v228, v236 offset:1024\n"
    "s_waitcnt lgkmcnt(0)\n"
    "s_branch L_rt9%=\n"
    "L_sp10%=:\n"
    "s_movk_i32 s92, 0x7fff\n"
    "L_lp10%=:\n"
    "ds_read_b32 v229, v230\n"
    "s_waitcnt lgkmcnt(0)\n"
    "v_cmp_gt_u32 vcc, s61, v229\n"
    "s_cbranch_vccz L_rt10%=\n"
    "s_sub_u32 s92, s92, 1\n"
    "s_cbranch_scc0 L_lp10%=\n"
    "ds_write_b32 v228, v236 offset:1024\n"
    "s_waitcnt lgkmcnt(0)\n"
    "s_branch L_rt10%=\n"
    "L_sp11%=:\n"
    "s_movk_i32 s92, 0x7fff\n"
    "L_lp11%=:\n"
    "ds_read_b32 v229, v230\n"
    "s_waitcnt lgkmcnt(0)\n"
    "v_cmp_gt_u32 vcc, s61, v229\n"
    "s_cbranch_vccz L_rt11%=\n"
    "s_sub_u32 s92, s92, 1\n"
    "s_cbranch_scc0 L_lp11%=\n"
    "ds_write_b32 v228, v236 offset:1024\n"
    "s_waitcnt lgkmcnt(0)\n"
    "s_branch L_rt11%=\n"
    "L_sp12%=:\n"
    "s_movk_i32 s92, 0x7fff\n"
    "L_lp12%=:\n"
    "ds_read_b32 v229, v230\n"
    "s_waitcnt lgkmcnt(0)\n"
    "v_cmp_gt_u32 vcc, s61, v229\n"
    "s_cbranch_vccz L_rt12%=\n"
    "s_sub_u32 s92, s92, 1\n"
    "s_cbranch_scc0 L_lp12%=\n"
    "ds_write_b32 v228, v236 offset:1024\n"
    "s_waitcnt lgkmcnt(0)\n"
    "s_branch L_rt12%=\n"
    "L_sp13%=:\n"
    "s_movk_i32 s92, 0x7fff\n"
    "L_lp13%=:\n"
    "ds_read_b32 v229, v230\n"
    "s_waitcnt lgkmcnt(0)\n"
    "v_cmp_gt_u32 vcc, s61, v229\n"
    "s_cbranch_vccz L_rt13%=\n"
    "s_sub_u32 s92, s92, 1\n"
    "s_cbranch_scc0 L_lp13%=\n"
    "ds_write_b32 v228, v236 offset:1024\n"
    "s_waitcnt lgkmcnt(0)\n"
    "s_branch L_rt13%=\n"
    "L_sp14%=:\n"
    "s_movk_i32 s92, 0x7fff\n"
    "L_lp14%=:\n"
    "ds_read_b32 v229, v230\n"
    "s_waitcnt lgkmcnt(0)\n"
    "v_cmp_gt_u32 vcc, s61, v229\n"
    "s_cbranch_vccz L_rt14%=\n"
    "s_sub_u32 s92, s92, 1\n"
    "s_cbranch_scc0 L_lp14%=\n"
    "ds_write_b32 v228, v236 offset:1024\n"
    "s_waitcnt lgkmcnt(0)\n"
    "s_branch L_rt14%=\n"
    "L_sp15%=:\n"
    "s_movk_i32 s92, 0x7fff\n"
    "L_lp15%=:\n"
    "ds_read_b32 v229, v230\n"
    "s_waitcnt lgkmcnt(0)\n"
    "v_cmp_gt_u32 vcc, s61, v229\n"
    "s_cbranch_vccz L_rt15%=\n"
    "s_sub_u32 s92, s92, 1\n"
    "s_cbranch_scc0 L_lp15%=\n"
    "ds_write_b32 v228, v236 offset:1024\n"
    "s_waitcnt lgkmcnt(0)\n"
    "s_branch L_rt15%=\n"
    "L_sp16%=:\n"
    "s_movk_i32 s92, 0x7fff\n"
    "L_lp16%=:\n"
    "ds_read_b32 v229, v230\n"
    "s_waitcnt lgkmcnt(0)\n"
    "v_cmp_gt_u32 vcc, s61, v229\n"
    "s_cbranch_vccz L_rt16%=\n"
    "s_sub_u32 s92, s92, 1\n"
    "s_cbranch_scc0 L_lp16%=\n"
    "ds_write_b32 v228, v236 offset:1024\n"
    "s_waitcnt lgkmcnt(0)\n"
    "s_branch L_rt16%=\n"
    "L_sp17%=:\n"
    "s_movk_i32 s92, 0x7fff\n"
    "L_lp17%=:\n"
    "ds_read_b32 v229, v230\n"
    "s_waitcnt lgkmcnt(0)\n"
    "v_cmp_gt_u32 vcc, s61, v229\n"
    "s_cbranch_vccz L_rt17%=\n"
    "s_sub_u32 s92, s92, 1\n"
    "s_cbranch_scc0 L_lp17%=\n"
    "ds_write_b32 v228, v236 offset:1024\n"
    "s_waitcnt lgkmcnt(0)\n"
    "s_branch L_rt17%=\n"
    "L_sp18%=:\n"
    "s_movk_i32 s92, 0x7fff\n"
    "L_lp18%=:\n"
    "ds_read_b32 v229, v230\n"
    "s_waitcnt lgkmcnt(0)\n"
    "v_cmp_gt_u32 vcc, s61, v229\n"
    "s_cbranch_vccz L_rt18%=\n"
    "s_sub_u32 s92, s92, 1\n"
    "s_cbranch_scc0 L_lp18%=\n"
    "ds_write_b32 v228, v236 offset:1024\n"
    "s_waitcnt lgkmcnt(0)\n"
    "s_branch L_rt18%=\n"
    "L_sp19%=:\n"
    "s_movk_i32 s92, 0x7fff\n"
    "L_lp19%=:\n"
    "ds_read_b32 v229, v230\n"
    "s_waitcnt lgkmcnt(0)\n"
    "v_cmp_gt_u32 vcc, s61, v229\n"
    "s_cbranch_vccz L_rt19%=\n"
    "s_sub_u32 s92, s92, 1\n"
    "s_cbranch_scc0 L_lp19%=\n"
    "ds_write_b32 v228, v236 offset:1024\n"
    "s_waitcnt lgkmcnt(0)\n"
    "s_branch L_rt19%=\n"
    "L_sp20%=:\n"
    "s_movk_i32 s92, 0x7fff\n"
    "L_lp20%=:\n"
    "ds_read_b32 v229, v230\n"
    "s_waitcnt lgkmcnt(0)\n"
    "v_cmp_gt_u32 vcc, s61, v229\n"
    "s_cbranch_vccz L_rt20%=\n"
    "s_sub_u32 s92, s92, 1\n"
    "s_cbranch_scc0 L_lp20%=\n"
    "ds_write_b32 v228, v236 offset:1024\n"
    "s_waitcnt lgkmcnt(0)\n"
    "s_branch L_rt20%=\n"
    "L_sp21%=:\n"
    "s_movk_i32 s92, 0x7fff\n"
    "L_lp21%=:\n"
    "ds_read_b32 v229, v230\n"
    "s_waitcnt lgkmcnt(0)\n"
    "v_cmp_gt_u32 vcc, s61, v229\n"
    "s_cbranch_vccz L_rt21%=\n"
    "s_sub_u32 s92, s92, 1\n"
    "s_cbranch_scc0 L_lp21%=\n"
    "ds_write_b32 v228, v236 offset:1024\n"
    "s_waitcnt lgkmcnt(0)\n"
    "s_branch L_rt21%=\n"
    "L_sp22%=:\n"
    "s_movk_i32 s92, 0x7fff\n"
    "L_lp22%=:\n"
    "ds_read_b32 v229, v230\n"
    "s_waitcnt lgkmcnt(0)\n"
    "v_cmp_gt_u32 vcc, s61, v229\n"
    "s_cbranch_vccz L_rt22%=\n"
    "s_sub_u32 s92, s92, 1\n"
    "s_cbranch_scc0 L_lp22%=\n"
    "ds_write_b32 v228, v236 offset:1024\n"
    "s_waitcnt lgkmcnt(0)\n"
    "s_branch L_rt22%=\n"
    "L_sp23%=:\n"
    "s_movk_i32 s92, 0x7fff\n"
    "L_lp23%=:\n"
    "ds_read_b32 v229, v230\n"
    "s_waitcnt lgkmcnt(0)\n"
    "v_cmp_gt_u32 vcc, s61, v229\n"
    "s_cbranch_vccz L_rt23%=\n"
    "s_sub_u32 s92, s92, 1\n"
    "s_cbranch_scc0 L_lp23%=\n"
    "ds_write_b32 v228, v236 offset:1024\n"
    "s_waitcnt lgkmcnt(0)\n"
    "s_branch L_rt23%=\n"
    "L_sp24%=:\n"
    "s_movk_i32 s92, 0x7fff\n"
    "L_lp24%=:\n"
    "ds_read_b32 v229, v230\n"
    "s_waitcnt lgkmcnt(0)\n"
    "v_cmp_gt_u32 vcc, s61, v229\n"
    "s_cbranch_vccz L_rt24%=\n"
    "s_sub_u32 s92, s92, 1\n"
    "s_cbranch_scc0 L_lp24%=\n"
    "ds_write_b32 v228, v236 offset:1024\n"
    "s_waitcnt lgkmcnt(0)\n"
    "s_branch L_rt24%=\n"
    "L_sp25%=:\n"
    "s_movk_i32 s92, 0x7fff\n"
    "L_lp25%=:\n"
    "ds_read_b32 v229, v230\n"
    "s_waitcnt lgkmcnt(0)\n"
    "v_cmp_gt_u32 vcc, s61, v229\n"
    "s_cbranch_vccz L_rt25%=\n"
    "s_sub_u32 s92, s92, 1\n"
    "s_cbranch_scc0 L_lp25%=\n"
    "ds_write_b32 v228, v236 offset:1024\n"
    "s_waitcnt lgkmcnt(0)\n"
    "s_branch L_rt25%=\n"
    "L_sp26%=:\n"
    "s_movk_i32 s92, 0x7fff\n"
    "L_lp26%=:\n"
    "ds_read_b32 v229, v230\n"
    "s_waitcnt lgkmcnt(0)\n"
    "v_cmp_gt_u32 vcc, s61, v229\n"
    "s_cbranch_vccz L_rt26%=\n"
    "s_sub_u32 s92, s92, 1\n"
    "s_cbranch_scc0 L_lp26%=\n"
    "ds_write_b32 v228, v236 offset:1024\n"
    "s_waitcnt lgkmcnt(0)\n"
    "s_branch L_rt26%=\n"
    "L_sp27%=:\n"
    "s_movk_i32 s92, 0x7fff\n"
    "L_lp27%=:\n"
    "ds_read_b32 v229, v230\n"
    "s_waitcnt lgkmcnt(0)\n"
    "v_cmp_gt_u32 vcc, s61, v229\n"
    "s_cbranch_vccz L_rt27%=\n"
    "s_sub_u32 s92, s92, 1\n"
    "s_cbranch_scc0 L_lp27%=\n"
    "ds_write_b32 v228, v236 offset:1024\n"
    "s_waitcnt lgkmcnt(0)\n"
    "s_branch L_rt27%=\n"
    "L_sp28%=:\n"
    "s_movk_i32 s92, 0x7fff\n"
    "L_lp28%=:\n"
    "ds_read_b32 v229, v230\n"
    "s_waitcnt lgkmcnt(0)\n"
    "v_cmp_gt_u32 vcc, s61, v229\n"
    "s_cbranch_vccz L_rt28%=\n"
    "s_sub_u32 s92, s92, 1\n"
    "s_cbranch_scc0 L_lp28%=\n"
    "ds_write_b32 v228, v236 offset:1024\n"
    "s_waitcnt lgkmcnt(0)\n"
    "s_branch L_rt28%=\n"
    "L_sp29%=:\n"
    "s_movk_i32 s92, 0x7fff\n"
    "L_lp29%=:\n"
    "ds_read_b32 v229, v230\n"
    "s_waitcnt lgkmcnt(0)\n"
    "v_cmp_gt_u32 vcc, s61, v229\n"
    "s_cbranch_vccz L_rt29%=\n"
    "s_sub_u32 s92, s92, 1\n"
    "s_cbranch_scc0 L_lp29%=\n"
    "ds_write_b32 v228, v236 offset:1024\n"
    "s_waitcnt lgkmcnt(0)\n"
    "s_branch L_rt29%=\n"
    "L_sp30%=:\n"
    "s_movk_i32 s92, 0x7fff\n"
    "L_lp30%=:\n"
    "ds_read_b32 v229, v230\n"
    "s_waitcnt lgkmcnt(0)\n"
    "v_cmp_gt_u32 vcc, s61, v229\n"
    "s_cbranch_vccz L_rt30%=\n"
    "s_sub_u32 s92, s92, 1\n"
    "s_cbranch_scc0 L_lp30%=\n"
    "ds_write_b32 v228, v236 offset:1024\n"
    "s_waitcnt lgkmcnt(0)\n"
    "s_branch L_rt30%=\n"
    "L_end%=:\n"
    : "=s"(bad)
    : "s"(__builtin_amdgcn_kernarg_segment_ptr()), "s"(wid), "s"(bid), "s"(tab)
    : "v0","v1","v2","v3","v4","v5","v6","v7","v8","v9","v10","v11","v12","v13","v14","v15","v16","v17","v18","v19","v20","v21","v22","v23","v24","v25","v26","v27","v28","v29","v30","v31","v32","v33","v34","v35","v36","v37","v38","v39","v40","v41","v42","v43","v44","v45","v46","v47","v48","v49","v50","v51","v52","v53","v54","v55","v56","v57","v58","v59","v60","v61","v62","v63","v64","v65","v66","v67","v68","v69","v70","v71","v72","v73","v74","v75","v76","v77","v78","v79","v80","v81","v82","v83","v84","v85","v86","v87","v88","v89","v90","v91","v92","v93","v94","v95","v96","v97","v98","v99","v100","v101","v102","v103","v104","v105","v106","v107","v108","v109","v110","v111","v112","v113","v114","v115","v116","v117","v118","v119","v120","v121","v122","v123","v124","v125","v126","v127","v128","v129","v130","v131","v132","v133","v134","v135","v136","v137","v138","v139","v140","v141","v142","v143","v144","v145","v146","v147","v148","v149","v150","v151","v152","v153","v154","v155","v156","v157","v158","v159","v160","v161","v162","v163","v164","v165","v166","v167","v168","v169","v170","v171","v172","v173","v174","v175","v176","v177","v178","v179","v180","v181","v182","v183","v184","v185","v186","v187","v188","v189","v190","v191","v192","v193","v194","v195","v196","v197","v198","v199","v200","v201","v202","v203","v204","v205","v206","v207","v208","v209","v210","v211","v212","v213","v214","v215","v216","v217","v218","v219","v220","v221","v222","v223","v224","v225","v226","v227","v228","v229","v230","v231","v232","v233","v234","v235","v236","v237","v238","v239","v240","v241","v242","v243","v244","v245","v246","v247","v248","v249","v250","v251","v252","v253","v254","v255","a0","a1","a2","a3","a4","a5","a6","a7","a8","a9","a10","a11","a12","a13","a14","a15","a16","a17","a18","a19","a20","a21","a22","a23","a24","a25","a26","a27","a28","a29","a30","a31","a32","a33","a34","a35","a36","a37","a38","a39","a40","a41","a42","a43","a44","a45","a46","a47","a48","a49","a50","a51","a52","a53","a54","a55","a56","a57","a58","a59","a60","a61","a62","a63","a64","a65","a66","a67","a68","a69","a70","a71","a72","a73","a74","a75","a76","a77","a78","a79","a80","a81","a82","a83","a84","a85","a86","a87","a88","a89","a90","a91","a92","a93","a94","a95","a96","a97","a98","a99","a100","a101","a102","a103","a104","a105","a106","a107","a108","a109","a110","a111","a112","a113","a114","a115","a116","a117","a118","a119","a120","a121","a122","a123","a124","a125","a126","a127","a128","a129","a130","a131","a132","a133","a134","a135","a136","a137","a138","a139","a140","a141","a142","a143","a144","a145","a146","a147","a148","a149","a150","a151","a152","a153","a154","a155","a156","a157","a158","a159","a160","a161","a162","a163","a164","a165","a166","a167","a168","a169","a170","a171","a172","a173","a174","a175","a176","a177","a178","a179","a180","a181","a182","a183","a184","a185","a186","a187","a188","a189","a190","a191","a192","a193","a194","a195","a196","a197","a198","a199","a200","a201","a202","a203","a204","a205","a206","a207","a208","a209","a210","a211","a212","a213","a214","a215","a216","a217","a218","a219","a220","a221","a222","a223","a224","a225","a226","a227","a228","a229","a230","a231","a232","a233","a234","a235","a236","a237","a238","a239","a240","a241","a242","a243","a244","a245","a246","a247","a248","a249","a250","a251","a252","a253","a254","a255","s20","s21","s22","s23","s24","s25","s26","s27","s28","s29","s30","s31","s33","s34","s35","s36","s37","s38","s39","s40","s41","s42","s43","s44","s45","s46","s47","s48","s49","s50","s51","s52","s53","s54","s55","s56","s57","s58","s59","s60","s61","s62","s63","s64","s65","s66","s67","s68","s69","s70","s71","s72","s73","s74","s75","s76","s77","s78","s79","s80","s81","s82","s83","s84","s85","s86","s87","s88","s89","s90","s91","s92","s93","s94","s95","s96","s97","s98","s99","vcc","scc","memory");
  __builtin_amdgcn_s_barrier();
  if (bad != 0u) {
    int lane;
    asm volatile("s_waitcnt vmcnt(0)" ::: "memory");
    asm volatile("v_mbcnt_lo_u32_b32 %0, -1, 0\n\tv_mbcnt_hi_u32_b32 %0, -1, %0" : "=v"(lane));
    for (int i = 0; i < 6; ++i) {
      const int e = tab >> (10 * (i >> 1)), head = e & 255, part = (e >> 8) & 3;
      const int row0 = 256 * part + 64 * wid + 32 * (i & 1);
      if (row0 < SQ)
        exact_rows(Q + (size_t)head * SQ * DH, K + (size_t)head * SK * DH, V + (size_t)head * SK * DH, O + (size_t)head * SQ * DH, row0,
                   lds + wid * (LDS_BYTES / 4 / 64 * 64), lane);
    }
  }
}

extern "C" void kernel_launch(void* const* d_in, const int* in_sizes, int n_in, void* d_out, int out_size, void* d_ws, size_t ws_size,
                              hipStream_t stream) {
  attn_fwd<<<256, 256, 0, stream>>>((const unsigned short*)d_in[0], (const unsigned short*)d_in[1], (const unsigned short*)d_in[2],
                                    (unsigned short*)d_out);
}
```
